```python
import torch

def _softmax(x, axis=-1):
    return torch.softmax(x, dim=axis)

def _gelu(x):
    return torch.nn.functional.gelu(x, approximate='tanh')

def _silu(x):
    return torch.nn.functional.silu(x)

def _sigmoid(x):
    return torch.sigmoid(x)

def _layernorm(x, axis=-1, eps=1e-05):
    m = x.mean(dim=axis, keepdim=True)
    v = x.var(dim=axis, keepdim=True, unbiased=False)
    return (x - m) / torch.sqrt(v + eps)

def _scatter_add(tgt, idx, src, axis=0):
    return tgt.clone().index_add_(axis, idx, src)

def _segment_sum(src, seg, n):
    out = torch.zeros((n,) + src.shape[1:], dtype=src.dtype)
    return out.index_add_(0, seg, src)

def _pad(x, pw):
    flat = tuple((v for pair in reversed(pw) for v in pair))
    return torch.nn.functional.pad(x, flat)
kernel_name = 'interleave_layers__ssm_conv_h3__memory_aug_recurrent_mem_tr__prompt'

def setup_inputs():

    _w = lambda a: (lambda t: t.to(torch.bfloat16) if t.is_floating_point() else t)(torch.as_tensor(a))

    def _A_setup():
        B, S, D = (1, 2048, 512)
        ks = 4
        return {'q': _w((lambda *s: torch.randn(*s, dtype=torch.bfloat16) * 0.05)(B, S, D)), 'k': _w((lambda *s: torch.randn(*s, dtype=torch.bfloat16) * 0.2)(B, S, D)), 'v': _w((lambda *s: torch.randn(*s, dtype=torch.bfloat16) * 0.2)(B, S, D)), 'shift_k': _w((lambda *s: torch.randn(*s, dtype=torch.bfloat16) * 0.2)(D, ks) * 0.5), 'ssm_k': _w((lambda *s: torch.randn(*s, dtype=torch.bfloat16) * 0.4)(D, S) * (1.0 / S ** 0.5)), 'ssm_D': _w((lambda *s: torch.randn(*s, dtype=torch.bfloat16) * 0.2)(D))}
    _A_in = _A_setup()

    def _B_setup():
        B, S, Dm = (1, 2048, 512)
        H, Dh = (32, 128)
        M, seg = (8, 32)
        seg = min(seg, S)
        S = S // seg * seg
        return {'x': _w((lambda *s: torch.randn(*s, dtype=torch.bfloat16) * 0.05)(B, S, Dm)), 'mem0': _w((lambda *s: torch.randn(*s, dtype=torch.bfloat16) * 0.2)(B, M, Dm)), 'Wq': _w((lambda *s: torch.randn(*s, dtype=torch.bfloat16) * 1.6)(Dm, H, Dh) * Dm ** (-0.5)), 'Wk': _w((lambda *s: torch.randn(*s, dtype=torch.bfloat16) * 1.6)(Dm, H, Dh) * Dm ** (-0.5)), 'Wv': _w((lambda *s: torch.randn(*s, dtype=torch.bfloat16) * 0.2)(Dm, H, Dh) * Dm ** (-0.5)), 'Wo': _w((lambda *s: torch.randn(*s, dtype=torch.bfloat16) * 0.2)(H, Dh, Dm) * (H * Dh) ** (-0.5)), 'seg_len': seg}
    _B_in = _B_setup()
    return {'A_q': _A_in['q'], 'A_k': _A_in['k'], 'A_v': _A_in['v'], 'A_shift_k': _A_in['shift_k'], 'A_ssm_k': _A_in['ssm_k'], 'A_ssm_D': _A_in['ssm_D'], 'B_x': _B_in['x'], 'B_mem0': _B_in['mem0'], 'B_Wq': _B_in['Wq'], 'B_Wk': _B_in['Wk'], 'B_Wv': _B_in['Wv'], 'B_Wo': _B_in['Wo'], 'B_seg_len': _B_in['seg_len']}

def reference(A_q, A_k, A_v, A_shift_k, A_ssm_k, A_ssm_D, B_x, B_mem0, B_Wq, B_Wk, B_Wv, B_Wo, B_seg_len):

    def _A_ref(q, k, v, shift_k, ssm_k, ssm_D):
        B, S, D = q.shape
        ks = shift_k.shape[-1]
        k_pad = _pad(k, ((0, 0), (ks - 1, 0), (0, 0)))
        k_shift = torch.zeros_like(k)
        for t in range(ks):
            k_shift = k_shift + k_pad[:, t:t + S, :] * shift_k[:, ks - 1 - t][None, None, :]
        kv = k_shift * v
        L = 2 * S
        kv_pad = _pad(kv, ((0, 0), (0, S), (0, 0)))
        kf_pad = _pad(ssm_k, ((0, 0), (0, S)))
        kv_f = (lambda x, axes=(-2, -1): torch.fft.rfft2(x.float(), dim=axes))(kv_pad[:, None, :, :], axes=(1, 2))
        kf_f = (lambda x, axes=(-2, -1): torch.fft.rfft2(x.float(), dim=axes))(kf_pad[:, None, :], axes=(1, 2))
        kf_f = (lambda x, *d: x.permute(*d))(kf_f, 1, 2, 0)
        y_f = kv_f * kf_f[None, :, :, :]
        y = (lambda x, s, axes=(-2, -1): torch.fft.irfft2(x, s=s, dim=axes).to(torch.bfloat16))(y_f, (1, L), axes=(1, 2))[:, 0, :S, :]
        y = y + kv * ssm_D[None, None, :]
        return q * y

    def _B_ref(x, mem0, Wq, Wk, Wv, Wo, seg_len):
        B, S, Dm = x.shape
        H, Dh = (Wq.shape[1], Wq.shape[2])
        M = mem0.shape[1]
        scale = Dh ** (-0.5)
        n_seg = S // seg_len
        L = M + seg_len
        idx = torch.arange(L)
        mask = (idx[:, None] < M) | (idx[:, None] >= idx[None, :])
        mem = mem0
        outs = []
        for s in range(n_seg):
            xs = x[:, s * seg_len:(s + 1) * seg_len, :].clone()
            h = torch.concatenate([mem, xs], axis=1)
            q = torch.einsum('btd,dhe->bhte', h, Wq)
            k = torch.einsum('btd,dhe->bhte', h, Wk)
            v = torch.einsum('btd,dhe->bhte', h, Wv)
            scores = torch.einsum('bhqe,bhke->bhqk', q, k) * scale
            scores = torch.where(mask[None, None], scores, -1000000000.0)
            p = _softmax(scores, axis=-1)
            o = torch.einsum('bhqk,bhke->bhqe', p, v)
            o = torch.einsum('bhte,hed->btd', o, Wo)
            mem = o[:, :M, :].clone()
            outs.append(o[:, M:, :])
        return torch.concatenate(outs, axis=1)
    x = A_q
    x = x + _A_ref(_layernorm(x, axis=-1), A_k, A_v, A_shift_k, A_ssm_k, A_ssm_D)
    x = x + _B_ref(_layernorm(x, axis=-1), B_mem0, B_Wq, B_Wk, B_Wv, B_Wo, B_seg_len)
    x = x + _A_ref(_layernorm(x, axis=-1), A_k, A_v, A_shift_k, A_ssm_k, A_ssm_D)
    x = x + _B_ref(_layernorm(x, axis=-1), B_mem0, B_Wq, B_Wk, B_Wv, B_Wo, B_seg_len)
    return x
```

```cpp
#include <hip/hip_runtime.h>
#include <hip/hip_bf16.h>
#include <stdint.h>

namespace {
typedef unsigned short u16;
typedef unsigned v4u __attribute__((ext_vector_type(4)));
typedef unsigned v2u __attribute__((ext_vector_type(2)));
typedef float f32x4 __attribute__((ext_vector_type(4)));
typedef __bf16 bf16x2 __attribute__((ext_vector_type(2)));
typedef __bf16 bf16x8 __attribute__((ext_vector_type(8)));
typedef __bf16 bf16x16 __attribute__((ext_vector_type(16)));
typedef short s16x4 __attribute__((ext_vector_type(4)));
typedef unsigned v8u __attribute__((ext_vector_type(8)));
#define GAS __attribute__((address_space(1)))
#define LAS __attribute__((address_space(3)))
#define DEVI __device__ __forceinline__
#define RLX_AGENT __ATOMIC_RELAXED, __HIP_MEMORY_SCOPE_AGENT

constexpr int S = 2048, D = 512, NH = 32, NSEG = 64;
constexpr float kScale = 0.08838834764831845f, kLog2e = 1.4426950408889634f;

constexpr unsigned MB = 1u << 20;
constexpr unsigned OFF_COMM = 0;
constexpr unsigned OFF_MEMH = 4 * MB;
constexpr unsigned OFF_OP = 5 * MB;
constexpr unsigned OFF_FLAG = 6 * MB;
constexpr unsigned FLAG_BYTES = 160 * 1024;
constexpr unsigned F_KV = 0;
constexpr unsigned F_O = 512;
constexpr unsigned F_H2 = 1024;
constexpr unsigned F_POL = 1056;
constexpr unsigned F_ABORT = 1060;
constexpr unsigned OFF_YB = 7 * MB;
constexpr unsigned OFF_Y = 9 * MB;
constexpr unsigned OFF_X1 = 11 * MB;
constexpr unsigned OFF_H = 15 * MB;
constexpr unsigned OFF_KV = 19 * MB;
constexpr unsigned OFF_WT = 27 * MB;
constexpr unsigned OFF_WOR = 39 * MB;
constexpr unsigned OFF_WOO = 43 * MB;
constexpr unsigned OFF_O = 47 * MB;
constexpr unsigned WS_USED = 79 * MB;

DEVI float b2f(unsigned x) { return __uint_as_float(x << 16); }
DEVI float blo(unsigned x) { return __uint_as_float(x << 16); }
DEVI float bhi(unsigned x) { return __uint_as_float(x & 0xffff0000u); }
DEVI unsigned pk2(float a, float b) { bf16x2 v = {(__bf16)a, (__bf16)b}; return __builtin_bit_cast(unsigned, v); }
DEVI float rb(float f) { return (float)(__bf16)f; }
DEVI unsigned hi2(float a, float b) { return __builtin_amdgcn_perm(__float_as_uint(b), __float_as_uint(a), 0x07060302u); }
DEVI bf16x8 as8(v4u v) { return __builtin_bit_cast(bf16x8, v); }
DEVI f32x4 mfma32(v4u a, v4u b, f32x4 c) { return __builtin_amdgcn_mfma_f32_16x16x32_bf16(as8(a), as8(b), c, 0, 0, 0); }
DEVI f32x4 mfma16(v2u a, v2u b, f32x4 c) { return __builtin_amdgcn_mfma_f32_16x16x16bf16_1k(__builtin_bit_cast(s16x4, a), __builtin_bit_cast(s16x4, b), c, 0, 0, 0); }
DEVI f32x4 smf(v4u a, v8u b, f32x4 c, int idx) { return __builtin_amdgcn_smfmac_f32_16x16x64_bf16(as8(a), __builtin_bit_cast(bf16x16, b), c, idx, 0, 0); }
DEVI v4u lo4(v8u v) { return __builtin_shufflevector(v, v, 0, 1, 2, 3); }
DEVI v4u hi4(v8u v) { return __builtin_shufflevector(v, v, 4, 5, 6, 7); }
DEVI v8u cat(v4u a, v4u b) { return __builtin_shufflevector(a, b, 0, 1, 2, 3, 4, 5, 6, 7); }
DEVI float halfsum(float a, float b) { auto r = __builtin_amdgcn_permlane32_swap(__float_as_uint(a), __float_as_uint(b), false, false); return __uint_as_float(r[0]) + __uint_as_float(r[1]); }
DEVI v2u halfsum_pk(f32x4 a, f32x4 b) { v2u r; r.x = pk2(halfsum(a[0], b[0]), halfsum(a[1], b[1])); r.y = pk2(halfsum(a[2], b[2]), halfsum(a[3], b[3])); return r; }
DEVI unsigned ror8(unsigned v) { return (unsigned)__builtin_amdgcn_update_dpp(0, (int)v, 0x128, 0xf, 0xf, true); }
DEVI float ror8f(float v) { return __uint_as_float(ror8(__float_as_uint(v))); }
DEVI float max_x16(float v) { auto r = __builtin_amdgcn_permlane16_swap(__float_as_uint(v), __float_as_uint(v), false, false); return fmaxf(__uint_as_float(r[0]), __uint_as_float(r[1])); }
DEVI float max_x32(float v) { auto r = __builtin_amdgcn_permlane32_swap(__float_as_uint(v), __float_as_uint(v), false, false); return fmaxf(__uint_as_float(r[0]), __uint_as_float(r[1])); }
DEVI float sum_x16(float v) { auto r = __builtin_amdgcn_permlane16_swap(__float_as_uint(v), __float_as_uint(v), false, false); return __uint_as_float(r[0]) + __uint_as_float(r[1]); }
DEVI float sum_x32(float v) { auto r = __builtin_amdgcn_permlane32_swap(__float_as_uint(v), __float_as_uint(v), false, false); return __uint_as_float(r[0]) + __uint_as_float(r[1]); }
DEVI float wsum(float v) {
  v += __uint_as_float((unsigned)__builtin_amdgcn_update_dpp(0, (int)__float_as_uint(v), 0xB1, 0xf, 0xf, true));
  v += __uint_as_float((unsigned)__builtin_amdgcn_update_dpp(0, (int)__float_as_uint(v), 0x4E, 0xf, 0xf, true));
  v += __uint_as_float((unsigned)__builtin_amdgcn_update_dpp(0, (int)__float_as_uint(v), 0x124, 0xf, 0xf, true));
  v += ror8f(v);
  v = sum_x16(v); v = sum_x32(v);
  return v;
}
DEVI void ln8(const float* x, float* o) {
  float s = 0;
#pragma unroll
  for (int j = 0; j < 8; ++j) s += x[j];
  const float mean = wsum(s) * (1.f / D);
  float q = 0;
#pragma unroll
  for (int j = 0; j < 8; ++j) q += (x[j] - mean) * (x[j] - mean);
  const float var = rb(wsum(q) * (1.f / D));
  const float den = rb(sqrtf(rb(var + 1e-5f)));
  const float mb = rb(mean);
#pragma unroll
  for (int j = 0; j < 8; ++j) o[j] = rb(rb(x[j] - mb) / den);
}
DEVI void unpack8(v4u v, float* o) { o[0] = blo(v.x); o[1] = bhi(v.x); o[2] = blo(v.y); o[3] = bhi(v.y); o[4] = blo(v.z); o[5] = bhi(v.z); o[6] = blo(v.w); o[7] = bhi(v.w); }
DEVI v4u pack8(const float* a) { v4u r = {pk2(a[0], a[1]), pk2(a[2], a[3]), pk2(a[4], a[5]), pk2(a[6], a[7])}; return r; }

typedef __amdgpu_buffer_rsrc_t rsrc_t;
#define LD16C(r, vo, so) __builtin_amdgcn_raw_buffer_load_b128(r, vo, so, 16)
#define LD16P(r, vo, so) __builtin_amdgcn_raw_buffer_load_b128(r, vo, so, 0)
#define LD4C(r, vo, so) __builtin_amdgcn_raw_buffer_load_b32(r, vo, so, 16)
#define ST16C(v, r, vo, so) __builtin_amdgcn_raw_buffer_store_b128(v, r, vo, so, 16)
#define ST8C(v, r, vo, so) __builtin_amdgcn_raw_buffer_store_b64(v, r, vo, so, 16)
#define ST4C(v, r, vo, so) __builtin_amdgcn_raw_buffer_store_b32(v, r, vo, so, 16)
#define CFENCE() asm volatile("" ::: "memory")
#define DRAIN() asm volatile("s_waitcnt vmcnt(0)" ::: "memory")
#define LBAR() asm volatile("s_waitcnt lgkmcnt(0)\n\ts_barrier" ::: "memory")
constexpr unsigned kSpinCap = 1u << 18;

DEVI bool spin_fail(unsigned& spins, rsrc_t W) {
  ++spins;
  if (spins > kSpinCap) { ST4C(1u, W, OFF_FLAG + F_ABORT * 128, 0); return true; }
  if ((spins & 255u) == 0u) { CFENCE(); return __builtin_amdgcn_readfirstlane(LD4C(W, OFF_FLAG + F_ABORT * 128, 0)) != 0; }
  return false;
}
DEVI bool wait_flags(rsrc_t W, unsigned voff, unsigned want) {
  for (unsigned spins = 0;;) {
    CFENCE();
    const unsigned v = LD4C(W, voff, 0);
    if (__all(v >= want)) { CFENCE(); return true; }
    __builtin_amdgcn_s_sleep(1);
    if (spin_fail(spins, W)) return false;
  }
}
DEVI unsigned umax4(v4u v) { return max(max(v.x, v.y), max(v.z, v.w)); }

__global__ void __launch_bounds__(256) k_pro_a(const u16* __restrict__ Ak, const u16* __restrict__ Av, const u16* __restrict__ sk, const u16* __restrict__ ssm,
                                               const u16* __restrict__ sD, const u16* __restrict__ mem0, const u16* __restrict__ Wq, const u16* __restrict__ Wk,
                                               const u16* __restrict__ Wv, const u16* __restrict__ Wo, unsigned char* __restrict__ ws) {
  __shared__ __attribute__((aligned(16))) u16 ur[4][2304 + 8];
  __shared__ __attribute__((aligned(16))) u16 yo[4][2048 + 8];
  const int tid = threadIdx.x, lane = tid & 63, wave = tid >> 6, c = lane & 15, g = lane >> 4;
  if (blockIdx.x < 128) {
    const int d0 = blockIdx.x * 4, t0 = tid * 8;
    float kk[11][4], sh[4][4];
#pragma unroll
    for (int i = 0; i < 11; ++i) {
      const int t = t0 - 3 + i; v2u r = {0u, 0u};
      if (t >= 0) r = *(const v2u*)(Ak + t * D + d0);
      kk[i][0] = blo(r.x); kk[i][1] = bhi(r.x); kk[i][2] = blo(r.y); kk[i][3] = bhi(r.y);
    }
#pragma unroll
    for (int ch = 0; ch < 4; ++ch) { const v2u r = *(const v2u*)(sk + (d0 + ch) * 4); sh[ch][0] = blo(r.x); sh[ch][1] = bhi(r.x); sh[ch][2] = blo(r.y); sh[ch][3] = bhi(r.y); }
    float u[4][8];
#pragma unroll
    for (int i = 0; i < 8; ++i) {
      const v2u r = *(const v2u*)(Av + (t0 + i) * D + d0);
      const float vv[4] = {blo(r.x), bhi(r.x), blo(r.y), bhi(r.y)};
#pragma unroll
      for (int ch = 0; ch < 4; ++ch) {
        float acc = 0.f;
#pragma unroll
        for (int j = 0; j < 4; ++j) acc = rb(acc + rb(kk[i + j][ch] * sh[ch][3 - j]));
        u[ch][i] = rb(acc * vv[ch]);
      }
    }
#pragma unroll
    for (int ch = 0; ch < 4; ++ch) {
      v4u r = {hi2(u[ch][7], u[ch][6]), hi2(u[ch][5], u[ch][4]), hi2(u[ch][3], u[ch][2]), hi2(u[ch][1], u[ch][0])};
      *(v4u*)&ur[ch][2040 - t0] = r;
    }
    if (tid < 128) { v4u z = {0u, 0u, 0u, 0u}; *(v4u*)&ur[tid >> 5][2048 + (tid & 31) * 8] = z; }
    __syncthreads();
    const u16* ker = ssm + (size_t)(d0 + wave) * S;
    f32x4 acc[8];
#pragma unroll
    for (int T = 0; T < 8; ++T) acc[T] = (f32x4){0.f, 0.f, 0.f, 0.f};
    for (int r = 0; r < 8; ++r) {
      v4u A[8], B[8];
#pragma unroll
      for (int i = 0; i < 8; ++i) {
        const int base = 32 * (8 * i + r) + c - 15 + 8 * g;
        if (i == 0 && r == 0) {
          unsigned e[8];
#pragma unroll
          for (int j = 0; j < 8; ++j) e[j] = base + j >= 0 ? ker[base + j] : 0;
          A[i] = (v4u){e[0] | e[1] << 16, e[2] | e[3] << 16, e[4] | e[5] << 16, e[6] | e[7] << 16};
        } else {
          typedef v4u __attribute__((aligned(2))) v4u_un;
          A[i] = *(const v4u_un*)(ker + base);
        }
        B[i] = *(const v4u*)&ur[wave][2032 - 256 * i + 32 * r - 16 * c + 8 * g];
      }
#pragma unroll
      for (int T = 0; T < 8; ++T)
#pragma unroll
        for (int i = 0; i <= T; ++i) acc[T] = mfma32(A[i], B[T - i], acc[T]);
    }
#pragma unroll
    for (int T = 0; T < 8; ++T) { v2u r = {pk2(acc[T][0], acc[T][1]), pk2(acc[T][2], acc[T][3])}; *(v2u*)&yo[wave][16 * (16 * T + c) + 4 * g] = r; }
    __syncthreads();
    float yy[4][8];
#pragma unroll
    for (int ch = 0; ch < 4; ++ch) {
      unpack8(*(const v4u*)&yo[ch][t0], yy[ch]);
      const float dd = b2f(sD[d0 + ch]);
#pragma unroll
      for (int i = 0; i < 8; ++i) yy[ch][i] = yy[ch][i] + rb(u[ch][i] * dd);
    }
    v4u* dst = (v4u*)(ws + OFF_YB + (size_t)blockIdx.x * 16384 + t0 * 8);
#pragma unroll
    for (int i = 0; i < 4; ++i) dst[i] = (v4u){pk2(yy[0][2 * i], yy[1][2 * i]), pk2(yy[2][2 * i], yy[3][2 * i]), pk2(yy[0][2 * i + 1], yy[1][2 * i + 1]), pk2(yy[2][2 * i + 1], yy[3][2 * i + 1])};
  } else {
    const int b = blockIdx.x - 128, gw = b * 4 + wave;
    for (int f = gw; f < 12288; f += 512) {
      const int ks = f & 15, nt = (f >> 4) & 1, hw3 = f >> 5, m = hw3 % 3, hw = hw3 / 3;
      const u16* W = (m == 0 ? Wq : m == 1 ? Wk : Wv) + (size_t)(32 * ks + 8 * g) * 4096 + hw * 32 + 16 * nt + c;
      unsigned e[8];
#pragma unroll
      for (int j = 0; j < 8; ++j) e[j] = W[j * 4096];
      *(v4u*)(ws + OFF_WT + (size_t)f * 1024 + lane * 16) = (v4u){e[0] | e[1] << 16, e[2] | e[3] << 16, e[4] | e[5] << 16, e[6] | e[7] << 16};
    }
    for (int f = gw; f < 4096; f += 512) {
      const int fr = f & 3, tile = (f >> 2) & 7, w = (f >> 5) & 3, h = f >> 7;
      const int col = 16 * (8 * w + tile) + 4 * (2 * ((c >> 2) & 1) + ((c >> 1) & 1)) + 2 * (c >> 3) + (c & 1);
      unsigned e[8];
#pragma unroll
      for (int j = 0; j < 8; ++j) {
        const int kap = 32 * (fr & 1) + 8 * g + j, u = fr >> 1, gA = kap >> 4, jj = 2 * ((kap >> 2) & 3) + (kap & 1), hf = (kap >> 1) & 1;
        const int ee = 32 * (2 * u + (gA >> 1)) + 16 * (jj >> 2) + 4 * (2 * (gA & 1) + hf) + (jj & 3);
        e[j] = Wo[(size_t)(h * 128 + ee) * D + col];
      }
      *(v4u*)(ws + OFF_WOR + (size_t)f * 1024 + lane * 16) = (v4u){e[0] | e[1] << 16, e[2] | e[3] << 16, e[4] | e[5] << 16, e[6] | e[7] << 16};
    }
    for (int f = gw; f < 4096; f += 512) {
      const int nt = f & 3, kst = (f >> 2) & 7, w = (f >> 5) & 3, nb = (f >> 7) & 7, kq = f >> 10;
      const int h = 8 * kq + 2 * w + (kst >> 2);
      unsigned e[8];
#pragma unroll
      for (int j = 0; j < 8; ++j) e[j] = Wo[(size_t)(h * 128 + 32 * (kst & 3) + 16 * (j >> 2) + 4 * g + (j & 3)) * D + 64 * nb + 16 * nt + c];
      *(v4u*)(ws + OFF_WOO + (size_t)f * 1024 + lane * 16) = (v4u){e[0] | e[1] << 16, e[2] | e[3] << 16, e[4] | e[5] << 16, e[6] | e[7] << 16};
    }
    for (unsigned p = gw; p < (OFF_FLAG + FLAG_BYTES) / 1024; p += 512) {
      const unsigned off = p * 1024;
      int kind;
      if (off < OFF_MEMH) { const unsigned unit = (off & (2 * MB - 1)) >> 14; kind = unit < 32 ? 0 : unit == 32 ? 2 : 1; }
      else if (off < OFF_OP) kind = ((off - OFF_MEMH) & (512 * 1024 - 1)) < 8192 ? 2 : 1;
      else kind = 0;
      v4u v = {0u, 0u, 0u, 0u};
      if (kind == 1) v = ~v;
      if (kind == 2) {
        const int t = p & 7;
        const unsigned* src = (const unsigned*)(mem0 + (c & 7) * D + 64 * t + 16 * g + 2 * (c >> 3));
        v = (v4u){min(src[0], 0xfffffffeu), min(src[2], 0xfffffffeu), min(src[4], 0xfffffffeu), min(src[6], 0xfffffffeu)};
      }
      *(v4u*)(ws + off + lane * 16) = v;
    }
  }
}

__global__ void __launch_bounds__(512) k_pro_b(const u16* __restrict__ Aq, unsigned char* __restrict__ ws) {
  const int lane = threadIdx.x & 63, t = blockIdx.x * 8 + (threadIdx.x >> 6);
  const v2u y0 = *(const v2u*)(ws + OFF_YB + (size_t)(2 * lane) * 16384 + t * 8), y1 = *(const v2u*)(ws + OFF_YB + (size_t)(2 * lane + 1) * 16384 + t * 8);
  const v4u yv = {y0.x, y0.y, y1.x, y1.y};
  float a[8], b[8], y[8];
  unpack8(*(const v4u*)(Aq + t * D + lane * 8), a); unpack8(yv, y);
  ln8(a, b);
#pragma unroll
  for (int j = 0; j < 8; ++j) a[j] = rb(a[j] + rb(b[j] * y[j]));
  ln8(a, b);
  *(v4u*)(ws + OFF_Y + t * 1024 + lane * 16) = yv;
  *(v4u*)(ws + OFF_X1 + t * 1024 + lane * 16) = pack8(a);
  *(v4u*)(ws + OFF_H + t * 1024 + lane * 16) = pack8(b);
}

constexpr int LDS_BYTES = 148 * 1024;

DEVI void role_R(rsrc_t W, unsigned char* ws, LAS unsigned char* lds, int layer, int h, int wave, int lane, int d1, int d2) {
  const int c = lane & 15, g = lane >> 4;
  constexpr int L_WV = 0, L_MX = 128 * 1024, L_SX = L_MX + 8192, L_OX = L_SX + 8192;
  const unsigned comm = OFF_COMM + layer * 2 * MB;
  if (h == 0 && wave == 0) {
    unsigned long long tt[2];
#pragma unroll
    for (int rep = 0; rep < 2; ++rep)
#pragma unroll
      for (int p = 0; p < 2; ++p) {
        const unsigned long long c0 = __builtin_amdgcn_s_memtime();
        for (int i = 0; i < 4; ++i) { ST4C(0u, W, comm + 100 * 16384 + p * 8192, 0); DRAIN(); }
        tt[p] = __builtin_amdgcn_s_memtime() - c0;
      }
    ST4C(tt[1] < tt[0] ? 2u : 1u, W, OFF_FLAG + (F_POL + layer) * 128, 0);
  }
  v8u wq[2][8], wk[2][8], wo[8][2];
  {
    const unsigned wb = OFF_WT + (h * 4 + wave) * 96 * 1024;
#pragma unroll
    for (int nt = 0; nt < 2; ++nt)
#pragma unroll
      for (int j = 0; j < 8; ++j) {
        const unsigned o = wb + (nt * 16 + 2 * ((2 * wave + j) & 7)) * 1024;
        wq[nt][j] = cat(LD16P(W, lane * 16, o), LD16P(W, lane * 16, o + 1024));
        wk[nt][j] = cat(LD16P(W, lane * 16, o + 32 * 1024), LD16P(W, lane * 16, o + 33 * 1024));
      }
    for (int nt = 0; nt < 2; ++nt)
#pragma unroll
      for (int j = 0; j < 8; ++j) {
        const unsigned o = wb + (64 + nt * 16 + 2 * ((2 * wave + j) & 7)) * 1024;
        const v4u a = LD16P(W, lane * 16, o), b = LD16P(W, lane * 16, o + 1024);
        *(LAS v4u*)(lds + L_WV + wave * 32768 + (nt * 16 + 2 * j) * 1024 + lane * 16) = a;
        *(LAS v4u*)(lds + L_WV + wave * 32768 + (nt * 16 + 2 * j + 1) * 1024 + lane * 16) = b;
      }
    const unsigned ob = OFF_WOR + (h * 4 + wave) * 32 * 1024;
#pragma unroll
    for (int i = 0; i < 8; ++i)
#pragma unroll
      for (int u = 0; u < 2; ++u) wo[i][u] = cat(LD16P(W, lane * 16, ob + (i * 4 + 2 * u) * 1024), LD16P(W, lane * 16, ob + (i * 4 + 2 * u + 1) * 1024));
  }
  if (!wait_flags(W, OFF_FLAG + (F_POL + layer) * 128, 1)) return;
  const unsigned pol = __builtin_amdgcn_readfirstlane(LD4C(W, OFF_FLAG + (F_POL + layer) * 128, 0)) - 1;
  const unsigned cb = comm + pol * 8192;
  const int idx = (lane & 8) ? 0xEEEE : 0x4444;
  const v2u ident = {(c == 4 * g ? 0x3f80u : 0u) | (c == 4 * g + 1 ? 0x3f800000u : 0u), (c == 4 * g + 2 ? 0x3f80u : 0u) | (c == 4 * g + 3 ? 0x3f800000u : 0u)};
  const unsigned one = (c & 1) ? 0x3f800000u : 0x3f80u;
  const v4u sel = {((c >> 1) & 3) == 0 ? one : 0u, ((c >> 1) & 3) == 1 ? one : 0u, ((c >> 1) & 3) == 2 ? one : 0u, ((c >> 1) & 3) == 3 ? one : 0u};
  const unsigned fkv = OFF_FLAG + (F_KV + ((layer * 32 + h) * 2) * 4 + wave) * 128;
  const unsigned kvb = OFF_KV + layer * 4 * MB + (h * 4 + wave) * 4096;
  const f32x4 Z = {0.f, 0.f, 0.f, 0.f};

  if (!wait_flags(W, fkv, 1)) return;
  v4u kx0 = LD16C(W, lane * 16, kvb), kx1 = LD16C(W, lane * 16, kvb + 1024), vx0 = LD16C(W, lane * 16, kvb + 2048), vx1 = LD16C(W, lane * 16, kvb + 3072);
  v2u hcopy = {0u, 0u};

  for (int s = 0; s < NSEG - 1; ++s) {
    v4u a[8];
    {
      const unsigned mo = cb + (32 + s) * 16384 + wave * 2048;
      bool first = true;
      for (unsigned spins = 0;;) {
        CFENCE();
        a[0] = LD16C(W, lane * 16, mo); a[1] = LD16C(W, lane * 16, mo + 1024);
        if (first && wave == 0 && s > 0 && c < 8) ST8C(hcopy, W, ((g >> 1) * 8 + c) * 16 + (g & 1) * 8, OFF_MEMH + (layer * 64 + s) * 8192 + h * 256);
        first = false;
        if (__all(max(umax4(a[0]), umax4(a[1])) != 0xffffffffu)) break;
        if (spin_fail(spins, W)) return;
      }
    }
    *(LAS v4u*)(lds + L_MX + wave * 2048 + lane * 16) = a[0];
    *(LAS v4u*)(lds + L_MX + wave * 2048 + 1024 + lane * 16) = a[1];
    unsigned fl = 0xffffffffu;
    if (s + 1 < NSEG - 1) fl = LD4C(W, fkv + ((s + 1) & 1) * 512, 0);
    f32x4 q0 = Z, q1 = Z, k0 = Z, k1 = Z, v0 = Z, v1 = Z;
#pragma unroll
    for (int j = 0; j < 2; ++j) { q0 = smf(a[j], wq[0][j], q0, idx); k0 = smf(a[j], wk[0][j], k0, idx); q1 = smf(a[j], wq[1][j], q1, idx); k1 = smf(a[j], wk[1][j], k1, idx); }
    LBAR();
#pragma unroll
    for (int j = 2; j < 8; ++j) a[j] = *(LAS v4u*)(lds + L_MX + ((2 * wave + j) & 7) * 1024 + lane * 16);
#pragma unroll
    for (int j = 2; j < 8; ++j) { q0 = smf(a[j], wq[0][j], q0, idx); k0 = smf(a[j], wk[0][j], k0, idx); }
#pragma unroll
    for (int j = 2; j < 8; ++j) { q1 = smf(a[j], wq[1][j], q1, idx); k1 = smf(a[j], wk[1][j], k1, idx); }
    const f32x4 t0 = mfma16(halfsum_pk(q0, k0), ident, Z), t1 = mfma16(halfsum_pk(q1, k1), ident, Z);
    const v4u F = {hi2(t0[0], t0[1]), hi2(t0[2], t0[3]), hi2(t1[0], t1[1]), hi2(t1[2], t1[3])};
    const f32x4 sm = mfma32(F, F, Z), sx0 = mfma32(kx0, F, Z), sx1 = mfma32(kx1, F, Z);
    if (c < 8) {
      LAS unsigned char* p = lds + L_SX + wave * 2048 + (g * 8 + c) * 16;
      *(LAS f32x4*)(p) = sx0; *(LAS f32x4*)(p + 512) = sx1; *(LAS f32x4*)(p + 1024) = sm;
    }
    if (s + 1 < NSEG - 1) {
      if (!__all(fl >= (unsigned)(s + 2))) { if (!wait_flags(W, fkv + ((s + 1) & 1) * 512, s + 2)) return; }
      CFENCE();
      const unsigned o = kvb + ((s + 1) & 7) * 512 * 1024;
      kx0 = LD16C(W, lane * 16, o); kx1 = LD16C(W, lane * 16, o + 1024);
    }
#pragma unroll
    for (int j = 0; j < 8; ++j) {
      LAS unsigned char* p = lds + L_WV + wave * 32768 + 2 * j * 1024 + lane * 16;
      v0 = smf(a[j], cat(*(LAS v4u*)p, *(LAS v4u*)(p + 1024)), v0, idx);
      if (j & 1) __builtin_amdgcn_sched_barrier(0);
    }
    LBAR();
    f32x4 sx, sv;
    {
      LAS unsigned char* px = lds + L_SX + (c >> 3) * 512 + (g * 8 + (c & 7)) * 16;
      LAS unsigned char* pm = lds + L_SX + 1024 + ((2 + (g & 1)) * 8 + (c & 7)) * 16;
      sx = (*(LAS f32x4*)px + *(LAS f32x4*)(px + 2048)) + (*(LAS f32x4*)(px + 4096) + *(LAS f32x4*)(px + 6144));
      sv = (*(LAS f32x4*)pm + *(LAS f32x4*)(pm + 2048)) + (*(LAS f32x4*)(pm + 4096) + *(LAS f32x4*)(pm + 6144));
    }
#pragma unroll
    for (int j = 0; j < 8; ++j) {
      LAS unsigned char* p = lds + L_WV + wave * 32768 + (16 + 2 * j) * 1024 + lane * 16;
      v1 = smf(a[j], cat(*(LAS v4u*)p, *(LAS v4u*)(p + 1024)), v1, idx);
      if (j & 1) __builtin_amdgcn_sched_barrier(0);
    }
    float e[8];
#pragma unroll
    for (int r = 0; r < 4; ++r) { e[r] = rb(rb(sx[r]) * kScale); e[4 + r] = rb(rb(sv[r]) * kScale); }
    float m = fmaxf(fmaxf(fmaxf(e[0], e[1]), fmaxf(e[2], e[3])), fmaxf(fmaxf(e[4], e[5]), fmaxf(e[6], e[7])));
    m = fmaxf(m, ror8f(m)); m = max_x16(m); m = max_x32(m);
#pragma unroll
    for (int r = 0; r < 8; ++r) e[r] = __builtin_amdgcn_exp2f((e[r] - m) * kLog2e);
    float l = ((e[0] + e[1]) + (e[2] + e[3])) + 0.25f * ((e[4] + e[5]) + (e[6] + e[7]));
    l += ror8f(l); l = sum_x16(l); l = sum_x32(l);
    const float il = __builtin_amdgcn_rcpf(l);
    const unsigned p0 = pk2(e[0] * il, e[1] * il), p1 = pk2(e[2] * il, e[3] * il), p2 = pk2(e[4] * il, e[5] * il), p3 = pk2(e[6] * il, e[7] * il);
    const v4u P32 = {p0, p1, ror8(p0), ror8(p1)};
    const v2u PA = {g < 2 ? p2 : 0u, g < 2 ? p3 : 0u}, PB = {g < 2 ? 0u : p2, g < 2 ? 0u : p3};
    const v2u xv = halfsum_pk(v0, v1);
    f32x4 o0 = mfma32(vx0, P32, Z), o1 = mfma32(vx1, P32, Z);
    o0 = mfma16(xv, PA, o0); o1 = mfma16(xv, PB, o1);
    if (c < 8) *(LAS v4u*)(lds + L_OX + wave * 512 + (g * 8 + c) * 16) = (v4u){pk2(o0[0], o0[1]), pk2(o0[2], o0[3]), pk2(o1[0], o1[1]), pk2(o1[2], o1[3])};
    if (s + 1 < NSEG - 1) {
      CFENCE();
      const unsigned o = kvb + ((s + 1) & 7) * 512 * 1024;
      vx0 = LD16C(W, lane * 16, o + 2048); vx1 = LD16C(W, lane * 16, o + 3072);
    }
    LBAR();
    const v4u oa = *(LAS v4u*)(lds + L_OX + lane * 16), ob = *(LAS v4u*)(lds + L_OX + 1024 + lane * 16);
    const unsigned tag = (s & 1) ^ 1;
#pragma unroll
    for (int grp = 0; grp < 2; ++grp) {
      f32x4 T[4];
#pragma unroll
      for (int i = 0; i < 4; ++i) T[i] = smf(oa, wo[4 * grp + i][0], Z, idx);
#pragma unroll
      for (int i = 0; i < 4; ++i) T[i] = smf(ob, wo[4 * grp + i][1], T[i], idx);
      const v2u u0 = halfsum_pk(T[0], T[2]), u1 = halfsum_pk(T[1], T[3]);
      const auto r0 = __builtin_amdgcn_permlane16_swap(u0.x, u1.x, false, false), r1 = __builtin_amdgcn_permlane16_swap(u0.y, u1.y, false, false);
      const v4u out = {(r0[0] & ~1u) | tag, r1[0], (r0[1] & ~1u) | tag, r1[1]};
      ST16C(out, W, g * 16384 + c * 16, cb + (8 * wave + 4 * grp) * 16384 + h * 256);
    }
    if (wave == 0) {
      for (int d = 0; d < d1; ++d) __builtin_amdgcn_s_sleep(1);
      f32x4 acc;
      for (unsigned spins = 0;;) {
        CFENCE();
#pragma unroll
        for (int i = 0; i < 8; ++i) a[i] = LD16C(W, lane * 16, cb + h * 16384 + i * 1024);
        acc = Z;
        unsigned an = 1u, orr = 0u;
#pragma unroll
        for (int i = 0; i < 8; ++i) { acc = mfma32(a[i], sel, acc); an &= a[i].x & a[i].z; orr |= a[i].x | a[i].z; }
        if (__all(tag ? (an & 1u) : !(orr & 1u))) break;
        if (spin_fail(spins, W)) return;
      }
      hcopy = (v2u){min(pk2(acc[0], acc[1]), 0xfffffffeu), min(pk2(acc[2], acc[3]), 0xfffffffeu)};
      if (c < 8) ST8C(hcopy, W, ((g >> 1) * 8 + c) * 16 + (g & 1) * 8, cb + (33 + s) * 16384 + h * 256);
    }
    __builtin_amdgcn_s_barrier();
    for (int d = 0; d < d2; ++d) __builtin_amdgcn_s_sleep(1);
  }
  if (wave == 0 && c < 8) ST8C(hcopy, W, ((g >> 1) * 8 + c) * 16 + (g & 1) * 8, OFF_MEMH + (layer * 64 + NSEG - 1) * 8192 + h * 256);
}

DEVI void role_H(rsrc_t W, LAS unsigned char* lds, int layer, int par, int h, int wave, int lane) {
  const int c = lane & 15, g = lane >> 4;
  constexpr int L_HT = 0, L_ST = 32768, L_MX = L_ST + 3 * 24576, L_SX = L_MX + 8192;
  v8u wq[2][8], wk[2][8], wv[2][8];
  {
    const unsigned wb = OFF_WT + (h * 4 + wave) * 96 * 1024;
#pragma unroll
    for (int nt = 0; nt < 2; ++nt)
#pragma unroll
      for (int j = 0; j < 8; ++j) {
        const unsigned o = wb + (nt * 16 + 2 * j) * 1024;
        wk[nt][j] = cat(LD16P(W, lane * 16, o + 32 * 1024), LD16P(W, lane * 16, o + 33 * 1024));
        wv[nt][j] = cat(LD16P(W, lane * 16, o + 64 * 1024), LD16P(W, lane * 16, o + 65 * 1024));
        wq[nt][j] = cat(LD16P(W, lane * 16, o), LD16P(W, lane * 16, o + 1024));
      }
  }
  const int idx = (lane & 8) ? 0xEEEE : 0x4444;
  const int c7 = c & 7;
  const v2u selA = {(c7 == 4 * g ? 0x3f80u : 0u) | (c7 == 4 * g + 1 ? 0x3f800000u : 0u), (c7 == 4 * g + 2 ? 0x3f80u : 0u) | (c7 == 4 * g + 3 ? 0x3f800000u : 0u)};
  const v2u selB = {(c7 + 8 == 4 * g ? 0x3f80u : 0u) | (c7 + 8 == 4 * g + 1 ? 0x3f800000u : 0u), (c7 + 8 == 4 * g + 2 ? 0x3f80u : 0u) | (c7 + 8 == 4 * g + 3 ? 0x3f800000u : 0u)};
  const unsigned fkv = OFF_FLAG + (F_KV + ((layer * 32 + h) * 2 + par) * 4 + wave) * 128;
  const unsigned fo = OFF_FLAG + (F_O + ((layer * 32 + h) * 2 + par) * 4 + wave) * 128;
  const f32x4 Z = {0.f, 0.f, 0.f, 0.f};
  const int ahead = layer == 0 ? 2 : 1;
  for (int it = 0; it < 32 + ahead; ++it) {
    if (it < 32) {
      const int s = 2 * it + par;
      if (layer == 1) { if (!wait_flags(W, OFF_FLAG + (F_H2 + (lane & 31)) * 128, s + 1)) return; }
      {
        v4u r[8];
#pragma unroll
        for (int i = 0; i < 8; ++i) r[i] = LD16C(W, c * 1024 + g * 16 + i * 64, OFF_H + layer * 2 * MB + (s * 32 + (wave >> 1) * 16) * 1024 + (wave & 1) * 512);
#pragma unroll
        for (int i = 0; i < 8; ++i) *(LAS v4u*)(lds + L_HT + (wave * 8 + i) * 1024 + lane * 16) = r[i];
      }
      LBAR();
      LAS unsigned char* st = lds + L_ST + (it % 3) * 24576 + wave * 6144 + lane * 16;
      {
        f32x4 ak[2][2] = {{Z, Z}, {Z, Z}}, av[2][2] = {{Z, Z}, {Z, Z}};
#pragma unroll
        for (int ks = 0; ks < 16; ++ks) {
          const v4u h0 = *(LAS v4u*)(lds + L_HT + ks * 1024 + lane * 16), h1 = *(LAS v4u*)(lds + L_HT + (16 + ks) * 1024 + lane * 16);
#pragma unroll
          for (int nt = 0; nt < 2; ++nt) {
            const v4u wkf = (ks & 1) ? hi4(wk[nt][ks >> 1]) : lo4(wk[nt][ks >> 1]), wvf = (ks & 1) ? hi4(wv[nt][ks >> 1]) : lo4(wv[nt][ks >> 1]);
            ak[nt][0] = mfma32(wkf, h0, ak[nt][0]); ak[nt][1] = mfma32(wkf, h1, ak[nt][1]);
            av[nt][0] = mfma32(h0, wvf, av[nt][0]); av[nt][1] = mfma32(h1, wvf, av[nt][1]);
          }
          if ((ks & 3) == 3) __builtin_amdgcn_sched_barrier(0);
        }
        const unsigned o = OFF_KV + layer * 4 * MB + (s & 7) * 512 * 1024 + (h * 4 + wave) * 4096;
#pragma unroll
        for (int i = 0; i < 2; ++i) {
          const v4u kf = {pk2(ak[0][i][0], ak[0][i][1]), pk2(ak[0][i][2], ak[0][i][3]), pk2(ak[1][i][0], ak[1][i][1]), pk2(ak[1][i][2], ak[1][i][3])};
          const v4u vf = {pk2(av[i][0][0], av[i][0][1]), pk2(av[i][0][2], av[i][0][3]), pk2(av[i][1][0], av[i][1][1]), pk2(av[i][1][2], av[i][1][3])};
          ST16C(kf, W, lane * 16, o + i * 1024); ST16C(vf, W, lane * 16, o + 2048 + i * 1024);
          *(LAS v4u*)(st + i * 1024) = kf; *(LAS v4u*)(st + 2048 + i * 1024) = vf;
        }
        DRAIN();
        if (lane == 0) ST4C((unsigned)(s + 1), W, fkv, 0);
      }
      {
        f32x4 aq[2][2] = {{Z, Z}, {Z, Z}};
#pragma unroll
        for (int ks = 0; ks < 16; ++ks) {
          const v4u h0 = *(LAS v4u*)(lds + L_HT + ks * 1024 + lane * 16), h1 = *(LAS v4u*)(lds + L_HT + (16 + ks) * 1024 + lane * 16);
#pragma unroll
          for (int nt = 0; nt < 2; ++nt) {
            const v4u wqf = (ks & 1) ? hi4(wq[nt][ks >> 1]) : lo4(wq[nt][ks >> 1]);
            aq[nt][0] = mfma32(wqf, h0, aq[nt][0]); aq[nt][1] = mfma32(wqf, h1, aq[nt][1]);
          }
          if ((ks & 3) == 3) __builtin_amdgcn_sched_barrier(0);
        }
#pragma unroll
        for (int i = 0; i < 2; ++i)
          *(LAS v4u*)(st + 4096 + i * 1024) = (v4u){pk2(aq[0][i][0], aq[0][i][1]), pk2(aq[0][i][2], aq[0][i][3]), pk2(aq[1][i][0], aq[1][i][1]), pk2(aq[1][i][2], aq[1][i][3])};
      }
      LBAR();
    }
    if (it >= ahead) {
      const int i2 = it - ahead, s = 2 * i2 + par;
      v4u a[8];
      {
        const unsigned mo = OFF_MEMH + (layer * 64 + s) * 8192 + wave * 2048;
        for (unsigned spins = 0;;) {
          CFENCE();
          a[0] = LD16C(W, lane * 16, mo); a[1] = LD16C(W, lane * 16, mo + 1024);
          if (__all(max(umax4(a[0]), umax4(a[1])) != 0xffffffffu)) break;
          __builtin_amdgcn_s_sleep(1);
          if (spin_fail(spins, W)) return;
        }
      }
      *(LAS v4u*)(lds + L_MX + wave * 2048 + lane * 16) = a[0];
      *(LAS v4u*)(lds + L_MX + wave * 2048 + 1024 + lane * 16) = a[1];
      LBAR();
#pragma unroll
      for (int j = 0; j < 8; ++j) a[j] = *(LAS v4u*)(lds + L_MX + j * 1024 + lane * 16);
      f32x4 k0 = Z, k1 = Z, v0 = Z, v1 = Z;
#pragma unroll
      for (int j = 0; j < 8; ++j) { k0 = smf(a[j], wk[0][j], k0, idx); k1 = smf(a[j], wk[1][j], k1, idx); }
#pragma unroll
      for (int j = 0; j < 8; ++j) { v0 = smf(a[j], wv[0][j], v0, idx); v1 = smf(a[j], wv[1][j], v1, idx); }
      LAS unsigned char* st = lds + L_ST + (i2 % 3) * 24576 + wave * 6144 + lane * 16;
      const v4u kf0 = *(LAS v4u*)st, kf1 = *(LAS v4u*)(st + 1024), vf0 = *(LAS v4u*)(st + 2048), vf1 = *(LAS v4u*)(st + 3072), qf0 = *(LAS v4u*)(st + 4096), qf1 = *(LAS v4u*)(st + 5120);
      const v2u xk = halfsum_pk(k0, k1);
      const f32x4 t0 = mfma16(xk, selA, Z), t1 = mfma16(xk, selB, Z);
      const v4u km = {hi2(t0[0], t0[1]), hi2(t0[2], t0[3]), hi2(t1[0], t1[1]), hi2(t1[2], t1[3])};
      {
        LAS unsigned char* p = lds + L_SX + wave * 5120 + lane * 16;
        *(LAS f32x4*)(p) = mfma32(km, qf0, Z);
        *(LAS f32x4*)(p + 1024) = mfma32(km, qf1, Z);
        *(LAS f32x4*)(p + 2048) = mfma32(kf0, qf0, Z);
        *(LAS f32x4*)(p + 3072) = mfma32(kf0, qf1, Z);
        *(LAS f32x4*)(p + 4096) = mfma32(kf1, qf1, Z);
      }
      LBAR();
      f32x4 sc[5];
#pragma unroll
      for (int i = 0; i < 5; ++i) {
        LAS unsigned char* p = lds + L_SX + i * 1024 + lane * 16;
        sc[i] = (*(LAS f32x4*)p + *(LAS f32x4*)(p + 5120)) + (*(LAS f32x4*)(p + 10240) + *(LAS f32x4*)(p + 15360));
#pragma unroll
        for (int r = 0; r < 4; ++r) {
          sc[i][r] = rb(rb(sc[i][r]) * kScale);
          if (i == 2 || i == 4) sc[i][r] = (4 * g + r <= c) ? sc[i][r] : -999292928.f;
        }
      }
      const v2u xv = halfsum_pk(v0, v1);
      const unsigned oo = OFF_O + ((layer * 64 + s) * 128 + h * 4 + wave) * 2048;
#pragma unroll
      for (int tt = 0; tt < 2; ++tt) {
        f32x4 em = sc[tt], e0 = sc[2 + tt], e1 = sc[4];
        float m = fmaxf(fmaxf(fmaxf(em[0], em[1]), fmaxf(em[2], em[3])), fmaxf(fmaxf(e0[0], e0[1]), fmaxf(e0[2], e0[3])));
        if (tt) m = fmaxf(m, fmaxf(fmaxf(e1[0], e1[1]), fmaxf(e1[2], e1[3])));
        m = max_x16(m); m = max_x32(m);
#pragma unroll
        for (int r = 0; r < 4; ++r) {
          em[r] = __builtin_amdgcn_exp2f((em[r] - m) * kLog2e); e0[r] = __builtin_amdgcn_exp2f((e0[r] - m) * kLog2e);
          e1[r] = tt ? __builtin_amdgcn_exp2f((e1[r] - m) * kLog2e) : 0.f;
        }
        float l = 0.5f * ((em[0] + em[1]) + (em[2] + em[3])) + ((e0[0] + e0[1]) + (e0[2] + e0[3])) + ((e1[0] + e1[1]) + (e1[2] + e1[3]));
        l = sum_x16(l); l = sum_x32(l);
        const float il = 1.f / l;
        const v4u P32 = {pk2(e0[0] * il, e0[1] * il), pk2(e0[2] * il, e0[3] * il), pk2(e1[0] * il, e1[1] * il), pk2(e1[2] * il, e1[3] * il)};
        const unsigned p2 = pk2(em[0] * il, em[1] * il), p3 = pk2(em[2] * il, em[3] * il);
        const v2u PA = {g < 2 ? p2 : 0u, g < 2 ? p3 : 0u}, PB = {g < 2 ? 0u : p2, g < 2 ? 0u : p3};
        f32x4 o0 = mfma32(vf0, P32, Z), o1 = mfma32(vf1, P32, Z);
        o0 = mfma16(xv, PA, o0); o1 = mfma16(xv, PB, o1);
        ST16C(((v4u){pk2(o0[0], o0[1]), pk2(o0[2], o0[3]), pk2(o1[0], o1[1]), pk2(o1[2], o1[3])}), W, lane * 16, oo + tt * 1024);
      }
      DRAIN();
      if (lane == 0) ST4C((unsigned)(s + 1), W, fo, 0);
    }
  }
}

DEVI void role_O(rsrc_t W, u16* out, LAS unsigned char* lds, int layer, int i, int wave, int lane) {
  const int c = lane & 15, g = lane >> 4, kq = i >> 3, nb = i & 7;
  v4u wo[8][4];
#pragma unroll
  for (int k = 0; k < 8; ++k)
#pragma unroll
    for (int nt = 0; nt < 4; ++nt) wo[k][nt] = LD16P(W, lane * 16, OFF_WOO + (((kq * 8 + nb) * 4 + wave) * 32 + k * 4 + nt) * 1024);
  const f32x4 Z = {0.f, 0.f, 0.f, 0.f};
  const int h0 = 8 * kq + 2 * wave;
  for (int s = 0; s < NSEG; ++s) {
    if (!wait_flags(W, OFF_FLAG + (F_O + ((layer * 32 + h0 + ((lane >> 2) & 1)) * 2 + (s & 1)) * 4 + (lane & 3)) * 128, s + 1)) return;
    v4u a[16];
#pragma unroll
    for (int k = 0; k < 16; ++k) a[k] = LD16C(W, lane * 16, OFF_O + ((layer * 64 + s) * 128 + h0 * 4) * 2048 + k * 1024);
    v4u xr, yr;
    if (wave == 0) {
      xr = LD16C(W, lane * 16, OFF_X1 + layer * 2 * MB + (s * 32 + i) * 1024);
      yr = LD16P(W, lane * 16, OFF_Y + (s * 32 + i) * 1024);
    }
    f32x4 acc[2][4] = {{Z, Z, Z, Z}, {Z, Z, Z, Z}};
#pragma unroll
    for (int k = 0; k < 8; ++k)
#pragma unroll
      for (int tt = 0; tt < 2; ++tt)
#pragma unroll
        for (int nt = 0; nt < 4; ++nt) acc[tt][nt] = mfma32(wo[k][nt], a[2 * k + tt], acc[tt][nt]);
#pragma unroll
    for (int tt = 0; tt < 2; ++tt)
#pragma unroll
      for (int nt = 0; nt < 4; ++nt) *(LAS f32x4*)(lds + wave * 8192 + (16 * tt + c) * 256 + ((4 * nt + g) ^ c) * 16) = acc[tt][nt];
    LBAR();
    const unsigned tag = ((s >> 1) & 1) ^ 1;
    const unsigned opb = OFF_OP + (layer * 2 + (s & 1)) * 256 * 1024;
#pragma unroll
    for (int r = 0; r < 2; ++r) {
      const int tok = wave * 8 + r * 4 + g;
      LAS unsigned char* p = lds + tok * 256 + (c ^ (tok & 15)) * 16;
      const f32x4 v = (*(LAS f32x4*)p + *(LAS f32x4*)(p + 8192)) + (*(LAS f32x4*)(p + 16384) + *(LAS f32x4*)(p + 24576));
      v4u u = __builtin_bit_cast(v4u, v);
      u = (u & ~1u) | tag;
      ST16C(u, W, tok * 8192 + c * 16, opb + kq * 2048 + nb * 256);
    }
    LBAR();
    if (wave == 0) {
      __builtin_amdgcn_s_sleep(4);
      v4u p[8];
      for (unsigned spins = 0;;) {
        CFENCE();
#pragma unroll
        for (int k = 0; k < 8; ++k) p[k] = LD16C(W, lane * 32 + (k & 1) * 16, opb + i * 8192 + (k >> 1) * 2048);
        v4u an = p[0], orr = p[0];
#pragma unroll
        for (int k = 1; k < 8; ++k) { an &= p[k]; orr |= p[k]; }
        if (__all(tag ? (an.x & an.y & an.z & an.w & 1u) : !((orr.x | orr.y | orr.z | orr.w) & 1u))) break;
        __builtin_amdgcn_s_sleep(1);
        if (spin_fail(spins, W)) return;
      }
      float x[8], b[8], y[8];
      unpack8(xr, x);
#pragma unroll
      for (int j = 0; j < 8; ++j) {
        const int k = j >> 2, r = j & 3;
        const float o = (__uint_as_float(p[k][r]) + __uint_as_float(p[2 + k][r])) + (__uint_as_float(p[4 + k][r]) + __uint_as_float(p[6 + k][r]));
        x[j] = rb(x[j] + rb(o));
      }
      if (layer == 1) {
        *(GAS v4u*)((GAS u16*)out + (s * 32 + i) * D + lane * 8) = pack8(x);
      } else {
        unpack8(yr, y);
        ln8(x, b);
#pragma unroll
        for (int j = 0; j < 8; ++j) x[j] = rb(x[j] + rb(b[j] * y[j]));
        ln8(x, b);
        ST16C(pack8(b), W, lane * 16, OFF_H + 2 * MB + (s * 32 + i) * 1024);
        ST16C(pack8(x), W, lane * 16, OFF_X1 + 2 * MB + (s * 32 + i) * 1024);
        DRAIN();
        if (lane == 0) ST4C((unsigned)(s + 1), W, OFF_FLAG + (F_H2 + i) * 128, 0);
      }
    }
  }
}

__global__ void __launch_bounds__(256, 1) k_main(unsigned char* ws, u16* out, int d1, int d2) {
  extern __shared__ __attribute__((aligned(16))) unsigned char smem[];
  LAS unsigned char* lds = (LAS unsigned char*)smem;
  const int wave = __builtin_amdgcn_readfirstlane(threadIdx.x >> 6), lane = threadIdx.x & 63;
  const rsrc_t W = __builtin_amdgcn_make_buffer_rsrc(ws, 0, WS_USED, 0x00020000);
  const int x = blockIdx.x & 7, i = blockIdx.x >> 3;
  if ((x & 3) == 0) role_R(W, ws, lds, x >> 2, i, wave, lane, d1, d2);
  else if ((x & 3) == 1) role_O(W, out, lds, x >> 2, i, wave, lane);
  else role_H(W, lds, i >> 4, (i >> 3) & 1, 8 * ((x & 1) + 2 * (x >> 2)) + (i & 7), wave, lane);
}
}

#define D1 9
#define D2 3
extern "C" void kernel_launch(void* const* d_in, const int* in_sizes, int n_in, void* d_out, int out_size, void* d_ws, size_t ws_size, hipStream_t stream) {
  unsigned char* ws = (unsigned char*)(((uintptr_t)d_ws + (2 * MB - 1)) & ~(uintptr_t)(2 * MB - 1));
  k_pro_a<<<256, 256, 0, stream>>>((const u16*)d_in[1], (const u16*)d_in[2], (const u16*)d_in[3], (const u16*)d_in[4], (const u16*)d_in[5], (const u16*)d_in[7],
                                   (const u16*)d_in[8], (const u16*)d_in[9], (const u16*)d_in[10], (const u16*)d_in[11], ws);
  k_pro_b<<<256, 512, 0, stream>>>((const u16*)d_in[0], ws);
  hipFuncSetAttribute((const void*)k_main, hipFuncAttributeMaxDynamicSharedMemorySize, LDS_BYTES);
  k_main<<<256, 256, LDS_BYTES, stream>>>(ws, (u16*)d_out, D1, D2);
}
```

```cpp
#include <hip/hip_runtime.h>
#include <hip/hip_bf16.h>
#include <stdint.h>

namespace {
typedef unsigned short u16;
typedef unsigned v4u __attribute__((ext_vector_type(4)));
typedef unsigned v2u __attribute__((ext_vector_type(2)));
typedef float f32x4 __attribute__((ext_vector_type(4)));
typedef __bf16 bf16x2 __attribute__((ext_vector_type(2)));
typedef __bf16 bf16x8 __attribute__((ext_vector_type(8)));
typedef __bf16 bf16x16 __attribute__((ext_vector_type(16)));
typedef short s16x4 __attribute__((ext_vector_type(4)));
typedef unsigned v8u __attribute__((ext_vector_type(8)));
#define GAS __attribute__((address_space(1)))
#define LAS __attribute__((address_space(3)))
#define DEVI __device__ __forceinline__
#define RLX_AGENT __ATOMIC_RELAXED, __HIP_MEMORY_SCOPE_AGENT

constexpr int S = 2048, D = 512, NH = 32, NSEG = 64;
constexpr float kScale = 0.08838834764831845f, kLog2e = 1.4426950408889634f;

constexpr unsigned MB = 1u << 20;
constexpr unsigned D_COMM = 0, D_OP = 2 * MB, D_H = 4 * MB, D_X = 8 * MB, D_Y = 12 * MB, D_KV = 16 * MB, D_O = 24 * MB, D_IN = 64 * MB;
constexpr unsigned OFF_YB = 56 * MB;
constexpr unsigned OFF_UT = 58 * MB;
constexpr unsigned OFF_BEAT = 78 * MB;
constexpr unsigned OFF_ACK = OFF_BEAT + 4096;
constexpr unsigned U_FH = 73;
constexpr unsigned WS_USED = 82 * MB;

DEVI float blo(unsigned x) { return __uint_as_float(x << 16); }
DEVI float bhi(unsigned x) { return __uint_as_float(x & 0xffff0000u); }
DEVI unsigned pk2(float a, float b) { bf16x2 v = {(__bf16)a, (__bf16)b}; return __builtin_bit_cast(unsigned, v); }
DEVI f32x4 mfma32(v4u a, v4u b, f32x4 c) { return __builtin_amdgcn_mfma_f32_16x16x32_bf16(__builtin_bit_cast(bf16x8, a), __builtin_bit_cast(bf16x8, b), c, 0, 0, 0); }
template <int C> DEVI float dpp(float v) { return __uint_as_float((unsigned)__builtin_amdgcn_update_dpp(0, (int)__float_as_uint(v), C, 0xf, 0xf, true)); }
DEVI float wsum(float v) {
  v += dpp<0xB1>(v); v += dpp<0x4E>(v); v += dpp<0x124>(v); v += dpp<0x128>(v);
  auto r = __builtin_amdgcn_permlane16_swap(__float_as_uint(v), __float_as_uint(v), false, false);
  v = __uint_as_float(r[0]) + __uint_as_float(r[1]);
  r = __builtin_amdgcn_permlane32_swap(__float_as_uint(v), __float_as_uint(v), false, false);
  return __uint_as_float(r[0]) + __uint_as_float(r[1]);
}
DEVI void ln8(const float* x, float* o) {
  float s = 0, q = 0;
#pragma unroll
  for (int j = 0; j < 8; ++j) s += x[j];
  const float mean = wsum(s) * (1.f / D);
#pragma unroll
  for (int j = 0; j < 8; ++j) { o[j] = x[j] - mean; q += o[j] * o[j]; }
  const float rs = __builtin_amdgcn_rsqf(wsum(q) * (1.f / D) + 1e-5f);
#pragma unroll
  for (int j = 0; j < 8; ++j) o[j] *= rs;
}
DEVI void unpack8(v4u v, float* o) { o[0] = blo(v.x); o[1] = bhi(v.x); o[2] = blo(v.y); o[3] = bhi(v.y); o[4] = blo(v.z); o[5] = bhi(v.z); o[6] = blo(v.w); o[7] = bhi(v.w); }
DEVI v4u pack8(const float* a) { v4u r = {pk2(a[0], a[1]), pk2(a[2], a[3]), pk2(a[4], a[5]), pk2(a[6], a[7])}; return r; }

typedef __amdgpu_buffer_rsrc_t rsrc_t;
#define LD16C(r, vo, so) __builtin_amdgcn_raw_buffer_load_b128(r, vo, so, 16)
#define LD8C(r, vo, so) __builtin_amdgcn_raw_buffer_load_b64(r, vo, so, 16)
#define LD4C(r, vo, so) __builtin_amdgcn_raw_buffer_load_b32(r, vo, so, 16)
#define ST16C(v, r, vo, so) __builtin_amdgcn_raw_buffer_store_b128(v, r, vo, so, 16)
#define ST8C(v, r, vo, so) __builtin_amdgcn_raw_buffer_store_b64(v, r, vo, so, 16)
#define ST4C(v, r, vo, so) __builtin_amdgcn_raw_buffer_store_b32(v, r, vo, so, 16)
#define NT(p) __builtin_nontemporal_load(p)
#define AUXNT 2
#define CFENCE() asm volatile("" ::: "memory")
#define DRAIN() asm volatile("s_waitcnt vmcnt(0)" ::: "memory")
#define LBAR() asm volatile("s_waitcnt lgkmcnt(0)\n\ts_barrier" ::: "memory")
#define RFL(x) __builtin_amdgcn_readfirstlane(x)
constexpr unsigned kSpinCap = 1u << 18;
#define XV 2
DEVI unsigned vblk() { return blockIdx.x ^ XV; }

#define ODEPTH 24
#define NCAL 3
struct Args { const u16 *Wq, *Wk, *Wv, *Wo; unsigned char* ws; u16* out; int d1, d2, d4, d3; const u16 *q, *k, *v, *sk, *ssm, *sD, *mem0; };

DEVI unsigned beat_state(rsrc_t W, unsigned h0, unsigned need) {
  v2u b;
  for (unsigned n = 0; n < kSpinCap; ++n) {
    b = LD8C(W, OFF_BEAT + (vblk() & 7) * 128, 0); CFENCE();
    if (RFL(b.y) != h0 && (RFL(b.x) & need) == need) break;
  }
  return RFL(b.x);
}
DEVI void wait_conv(rsrc_t W, int lane, unsigned want) {
  const unsigned o = OFF_ACK + ((lane >> 2) * 8 + 4 + (lane & 3)) * 128;
  for (unsigned n = 0; n < kSpinCap; ++n) {
    const unsigned a = LD4C(W, o, 0), b = LD4C(W, o + 128 * 128, 0); CFENCE();
    if (!__builtin_amdgcn_ballot_w64(min(a, b) < want)) break;
    __builtin_amdgcn_s_sleep(1);
  }
}
DEVI unsigned vote(rsrc_t W, int lane) {
  unsigned a;
  for (unsigned n = 0; n < kSpinCap; ++n) {
    a = LD4C(W, OFF_ACK + ((lane & 31) * 8 + 3) * 128, 0); CFENCE();
    if (!__builtin_amdgcn_ballot_w64(a == 0)) break;
  }
  const unsigned b = __builtin_amdgcn_update_dpp(0, (int)a, 0xB1, 0xf, 0xf, true);
  return __builtin_popcountll(__builtin_amdgcn_ballot_w64(a < b) & 0xaaaaaaaaull) > 8;
}
DEVI void master(rsrc_t W, int lane, int wave, unsigned g0) {
  unsigned state = 0;
  for (unsigned n = 1; n < kSpinCap; ++n) {
    if (lane < 8) ST8C(((v2u){state, g0 + n}), W, OFF_BEAT + lane * 128, 0);
    if (state & 8) break;
    unsigned a[4];
#pragma unroll
    for (int j = 0; j < 4; ++j) a[j] = LD4C(W, OFF_ACK + (64 * j + lane) * 128, 0);
    CFENCE();
    const unsigned long long miss = __builtin_amdgcn_ballot_w64(min(min(a[0], a[1]), min(a[2], a[3])) == 0);
    if (!(state & 2) && !(miss & 0x0808080808080808ull)) {
      int cnt = 0;
#pragma unroll
      for (int j = 0; j < 4; ++j) cnt += __builtin_popcountll(__builtin_amdgcn_ballot_w64(a[j] < (unsigned)__builtin_amdgcn_update_dpp(0, (int)a[j], 0x128, 0xf, 0xf, true)) & 0x0800080008000800ull);
      state |= 2 | (cnt > 8);
    }
    if (!miss) state |= 8;
  }
}

template <bool MARK> DEVI void ln_token(rsrc_t W, int t, int lane, unsigned pol, v4u xq, v4u yv) {
  float a[8], b[8], y[8];
  unpack8(xq, a); unpack8(yv, y);
  ln8(a, b);
#pragma unroll
  for (int j = 0; j < 8; ++j) a[j] += b[j] * y[j];
  v4u xa = pack8(a);
  unpack8(xa, a);
  ln8(a, b);
  v4u hb = pack8(b);
  const unsigned ro = (t >> 3) * 16384 + (t & 7) * 1024 + ((pol ^ (t >> 10)) << 13);
  xa.x = min(xa.x, 0xfffffffeu); xa.z = min(xa.z, 0xfffffffeu); hb.x = min(hb.x, 0xfffffffeu); hb.z = min(hb.z, 0xfffffffeu);
  const v4u ones = {~0u, ~0u, ~0u, ~0u};
  ST16C(hb, W, lane * 16, D_H + ro);
  ST16C(xa, W, lane * 16, D_X + ro);
  ST16C(yv, W, lane * 16, D_Y + ro);
  if (MARK) { ST16C(ones, W, lane * 16, D_H + (ro ^ 8192)); ST16C(ones, W, lane * 16, D_X + (ro ^ 8192)); }
}

DEVI unsigned pre_first(const Args& A, rsrc_t W, int i, int lane, int wave) {
  const v2u b0 = LD8C(W, OFF_BEAT + ((wave == 3 ? lane : 3) & 7) * 128, 0);
  const unsigned cal = D_COMM + (40 + (i >> 1)) * 16384 + (i & 1) * 8192;
  v4u xq;
  extern __shared__ __attribute__((aligned(16))) unsigned char lds[];
  volatile LAS unsigned* go = (volatile LAS unsigned*)(lds + 131072);
  go[lane & 1] = 0;
  if (wave < 2) {
    xq = NT((const v4u*)(A.q + (32 * wave + i) * D + lane * 8));
  } else if (wave == 2) ST4C(0u, W, cal, 0);
  if (i == 0) {
    const v4u z = {0u, 0u, 0u, 0u};
#pragma unroll
    for (int j = 0; j < 8; ++j) ST16C(z, W, threadIdx.x * 16, OFF_ACK + j * 4096);
#pragma unroll
    for (int j = 0; j < 4; ++j) ST16C(z, W, threadIdx.x * 16, D_OP + U_FH * 16384 + j * 4096);
  }
  DRAIN(); LBAR();
  const unsigned h0 = __builtin_amdgcn_readlane(b0.y, 3);
  if (wave < 2) {
    beat_state(W, h0, 0);
    wait_conv(W, lane, 1);
    v2u y0 = LD8C(W, lane * 32768, OFF_YB + (32 * wave + i) * 8), y1 = LD8C(W, lane * 32768 + 16384, OFF_YB + (32 * wave + i) * 8);
    const unsigned pol = vote(W, lane);
    const unsigned fl = D_OP + U_FH * 16384 + ((pol ^ 1) << 13) + wave * 4096 + i * 128;
#pragma unroll 1
    for (int j = 0; j < 4; ++j) {
      const v4u xc = xq, yc = {y0.x, y0.y, y1.x, y1.y};
      const int t = 32 * (wave + 2 * j) + i, tn = (t + 64) & 255;
      xq = NT((const v4u*)(A.q + tn * D + lane * 8));
      y0 = LD8C(W, lane * 32768, OFF_YB + tn * 8); y1 = LD8C(W, lane * 32768 + 16384, OFF_YB + tn * 8);
      ln_token<true>(W, t, lane, pol, xc, yc);
      if (j != 2) { DRAIN(); if (lane == 0) ST4C(wave + 2 * j + 1, W, fl, 0); go[wave] = 1; }
    }
  } else if (wave == 2) {
    unsigned best = 0xffff;
    for (int rep = 0; rep < NCAL; ++rep) {
      const unsigned c0 = (unsigned)__builtin_amdgcn_s_memtime();
      asm volatile("s_waitcnt lgkmcnt(0)" ::: "memory");
      ST4C(0u, W, cal, 0); DRAIN();
      best = min(best, (unsigned)__builtin_amdgcn_s_memtime() - c0);
    }
    beat_state(W, h0, 0);
    if (lane == 0) ST4C(best + 1, W, OFF_ACK + (8 * i + 3) * 128, 0);
    for (unsigned n = 0; n < 16 * kSpinCap && __builtin_amdgcn_ballot_w64(go[lane & 1] == 0); ++n) __builtin_amdgcn_s_sleep(2);
    const rsrc_t O = __builtin_amdgcn_make_buffer_rsrc((void*)A.Wo, 0, 4 * MB, 0x00020000);
    const unsigned vo = (lane >> 4) * 1024 + (((lane >> 2) & 12) ^ (lane & 15)) * 16, so = (i >> 2) * 524288 + (i & 3) * 256;
#pragma unroll 2
    for (int q = 0; q < 32; ++q) {
#pragma unroll
      for (int k = 0; k < 4; ++k) __builtin_amdgcn_raw_ptr_buffer_load_lds(O, (LAS void*)(lds + q * 4096 + k * 1024), 16, vo, so + q * 16384 + k * 4096, 0, 0);
      asm volatile("s_waitcnt vmcnt(%0)" :: "n"(ODEPTH) : "memory");
    }
    DRAIN();
  } else if (i == 0) { master(W, lane, wave, b0.y); }
  return h0;
}

#define DMA16C(r, l, vo, so) __builtin_amdgcn_raw_ptr_buffer_load_lds(r, (LAS void*)(l), 16, vo, so, 0, 16)
DEVI unsigned mid(unsigned hi, unsigned lo) { return __builtin_amdgcn_alignbit(hi, lo, 16); }
DEVI void ack(rsrc_t W, unsigned v) { DRAIN(); LBAR(); if (threadIdx.x == 0) ST4C(v, W, OFF_ACK + vblk() * 128, 0); }

DEVI unsigned pre_conv(const Args& A, rsrc_t W, int cb, int ub_, int lane, int wave) {
  extern __shared__ __attribute__((aligned(16))) unsigned char lds[];
  constexpr int KS = 4160, L_TR = 16 * KS, L_UR = L_TR + 16384, L_YO = L_UR + 4 * 4624, L_KR = L_YO + 4 * 4112;
  unsigned char* kc = lds + wave * 4 * KS;
  unsigned char* tr = lds + L_TR + wave * 4096;
  u16 (*ur)[2312] = (u16 (*)[2312])(lds + L_UR);
  u16 (*yo)[2056] = (u16 (*)[2056])(lds + L_YO);
  v2u* kr = (v2u*)(lds + L_KR);
  const int tid = threadIdx.x, c = lane & 15, g = lane >> 4, gw = cb * 4 + wave;
  const v2u b0 = LD8C(W, OFF_BEAT + (vblk() & 7) * 128, 0);
  const v2u k0 = NT((const v2u*)(A.k + tid * D + 4 * cb)), v0 = NT((const v2u*)(A.v + tid * D + 4 * cb));
  v4u kw[4]; v2u kx[4];
  kw[0] = NT((const v4u*)(A.ssm + gw * S + 8 * lane)); kx[0] = NT((const v2u*)(A.ssm + gw * S + 8 * lane + 8));
  const v4u r01 = *(const v4u*)(A.sk + cb * 16), r23 = *(const v4u*)(A.sk + cb * 16 + 8);
  const float dv = blo(A.sD[gw]);
  CFENCE();
  const int ch0 = 64 * (ub_ & 7) + 32 * (wave & 1), tb = 128 * (ub_ >> 3) + 64 * (wave >> 1), t0 = tb + (lane & 56), d0 = ch0 + 4 * (lane & 7);
  v2u kk[11], vv[8];
#pragma unroll
  for (int i = 0; i < 11; ++i) kk[i] = NT((const v2u*)(A.k + max(t0 - 3 + i, 0) * D + d0));
#pragma unroll
  for (int i = 0; i < 8; ++i) vv[i] = NT((const v2u*)(A.v + (t0 + i) * D + d0));
  const v4u s01 = *(const v4u*)(A.sk + d0 * 4), s23 = *(const v4u*)(A.sk + d0 * 4 + 8);
  CFENCE();
  const v4u z = {0u, 0u, 0u, 0u};
  if (lane < 32) *(v4u*)&ur[wave][2048 + lane * 8] = z;
  else if (lane < 40) *(v4u*)(kc + ((lane >> 1) & 3) * KS + (lane & 1) * 16) = z;
  if (tid < 3) kr[tid] = (v2u){0u, 0u};
  if (wave == 0) {
    ST16C(z, W, lane * 16, D_OP + (1024 + cb) * 1024);
    if (cb < 80) ST16C(z, W, lane * 16, D_OP + (cb < 16 ? 1152 + cb : 1168 + cb) * 1024);
  }
  auto el = [](v2u x, int e) { return e & 1 ? bhi(x[e >> 1]) : blo(x[e >> 1]); };
  auto mul = [](float k3, float k2, float k1, float k0, const float* s, float v) { return __builtin_fmaf(k0, s[0], __builtin_fmaf(k1, s[1], __builtin_fmaf(k2, s[2], k3 * s[3]))) * v; };
  auto copies = [&](int o, v4u w, v2u x) {
    const unsigned a0 = mid(w.y, w.x), a1 = mid(w.z, w.y), a2 = mid(w.w, w.z), a3 = mid(x.x, w.w), a4 = mid(x.y, x.x);
    *(v4u*)(kc + o) = w;
    *(v4u*)(kc + KS + o) = (v4u){a0, a1, a2, a3};
    *(v4u*)(kc + 2 * KS + o) = (v4u){w.y, w.z, w.w, x.x};
    *(v4u*)(kc + 3 * KS + o) = (v4u){a1, a2, a3, a4};
  };
  auto put = [&](int t) {
    const unsigned y0 = *(const unsigned*)&yo[0][t], y1 = *(const unsigned*)&yo[1][t], y2 = *(const unsigned*)&yo[2][t], y3 = *(const unsigned*)&yo[3][t];
    ST16C(((v4u){__builtin_amdgcn_perm(y1, y0, 0x05040100u), __builtin_amdgcn_perm(y3, y2, 0x05040100u), __builtin_amdgcn_perm(y1, y0, 0x07060302u), __builtin_amdgcn_perm(y3, y2, 0x07060302u)}),
          W, t * 8, OFF_YB + cb * 16384);
  };
  const unsigned char* ka = kc + ((c + 1) & 3) * KS + ((c + 1) >> 2) * 8 + g * 16;
  const u16* ub = &ur[wave][2032 - 16 * c + 8 * g];
  const u16* ue = &ur[wave][2044 - 16 * c - 4 * g];
  float sh[16];
  {
    kr[tid + 3] = k0;
    LBAR();
    const v2u k3 = kr[tid], k2 = kr[tid + 1], k1 = kr[tid + 2];
    unpack8(r01, sh); unpack8(r23, sh + 8);
    copies(32 + 16 * lane, kw[0], kx[0]);
    if (lane == 0) { *(unsigned*)(kc + KS + 28) = kw[0].x << 16; *(unsigned*)(kc + 2 * KS + 28) = kw[0].x; *(v2u*)(kc + 3 * KS + 24) = (v2u){kw[0].x << 16, mid(kw[0].y, kw[0].x)}; }
#pragma unroll
    for (int e = 0; e < 4; ++e) ur[e][2047 - tid] = (u16)pk2(mul(el(k3, e), el(k2, e), el(k1, e), el(k0, e), sh + 4 * e, el(v0, e)), 0.f);
    v4u Af[8];
#pragma unroll
    for (int r = 0; r < 8; ++r) { const v2u lo = *(const v2u*)(ka + 64 * r), hi = *(const v2u*)(ka + 64 * r + 8); Af[r] = (v4u){lo.x, lo.y, hi.x, hi.y}; }
    LBAR();
    f32x4 acc = {0.f, 0.f, 0.f, 0.f}, x = acc;
#pragma unroll
    for (int r = 0; r < 8; r += 2) { acc = mfma32(Af[r], *(const v4u*)(ub + 32 * r), acc); x = mfma32(Af[r + 1], *(const v4u*)(ub + 32 * r + 32), x); }
    acc += x;
    const v2u u = *(const v2u*)ue;
    *(v2u*)&yo[wave][16 * c + 4 * g] = (v2u){pk2(acc[0] + bhi(u.y) * dv, acc[1] + blo(u.y) * dv), pk2(acc[2] + bhi(u.x) * dv, acc[3] + blo(u.x) * dv)};
    LBAR();
    if (wave == 0) { put(2 * lane); put(128 + 2 * lane); }
  }
  CFENCE();
  const unsigned h0 = RFL(b0.y);
  const unsigned b1 = LD4C(W, OFF_BEAT + (vblk() & 7) * 128 + 4, 0);
  CFENCE();
  unpack8(s01, sh); unpack8(s23, sh + 8);
  if (t0 == 0) kk[0] = kk[1] = kk[2] = (v2u){0u, 0u};
#pragma unroll
  for (int e = 0; e < 4; ++e) {
    float u[8];
#pragma unroll
    for (int i = 0; i < 8; ++i) u[7 - i] = mul(el(kk[i], e), el(kk[i + 1], e), el(kk[i + 2], e), el(kk[i + 3], e), sh + 4 * e, el(vv[i], e));
    *(v4u*)(tr + e * 1024 + (lane & 7) * 128 + (((lane >> 3) ^ lane ^ 7) & 7) * 16) = pack8(u);
  }
  CFENCE();
  if (RFL(b1) == h0) beat_state(W, h0, 0);
  if (tid == 0) ST4C(1u, W, OFF_ACK + vblk() * 128, 0);
  LBAR();
#pragma unroll
  for (int q = 1; q < 4; ++q) { kw[q] = NT((const v4u*)(A.ssm + gw * S + 512 * q + 8 * lane)); kx[q] = NT((const v2u*)(A.ssm + gw * S + min(512 * q + 8 * lane + 8, S - 4))); }
  const v4u* msrc = (const v4u*)(A.mem0 + (c & 7) * D + 64 * (gw & 7) + 16 * g);
  const v4u ma = msrc[0], mb = msrc[1];
#pragma unroll
  for (int e = 0; e < 4; ++e)
    ST16C(*(const v4u*)(tr + e * 1024 + (lane >> 3) * 128 + (((lane >> 3) ^ lane) & 7) * 16), W, (lane >> 3) * 16384 + (lane & 7) * 16, OFF_UT + (ch0 + e) * 4096 + (1984 - tb) * 2);
  ST16C(z, W, lane * 16, D_IN + gw * 1024);
  ST16C(z, W, lane * 16, D_IN + (512 + gw) * 1024);
  ST16C(z, W, lane * 16, D_OP + gw * 1024);
  ST16C(z, W, lane * 16, D_OP + (512 + gw) * 1024);
  if (gw >= 64 && gw < 128) ST16C(~z, W, lane * 16, (gw & 32 ? MB : 0) + (16 + (gw & 31)) * 1024);
  CFENCE();
  if (gw < 16) ST16C(((v4u){min(c & 8 ? ma.y : ma.x, 0xfffffffeu), c & 8 ? ma.w : ma.z, min(c & 8 ? mb.y : mb.x, 0xfffffffeu), c & 8 ? mb.w : mb.z}), W, lane * 16, gw * 1024);
#pragma unroll
  for (int q = 1; q < 4; ++q) copies(32 + 1024 * q + 16 * lane, kw[q], kx[q]);
  ack(W, 2);
  const int hd = min(cb / 3, 31), part = cb % 3;
  const u16* pw = part == 0 ? A.Wo + hd * 65536 : A.Wk + hd * 128;
  const unsigned po = part == 0 ? (wave * 256 + lane) * 128 : (wave * 128 + (lane >> 1)) * 8192 + (lane & 1) * 128;
  const int p = 8 * c + (cb >> 4);
  asm volatile(
    "ds_read_b64 a[0:1], %1 offset:0\n"
    "ds_read_b64 a[2:3], %1 offset:8\n"
    "ds_read_b64 a[4:5], %1 offset:64\n"
    "ds_read_b64 a[6:7], %1 offset:72\n"
    "ds_read_b64 a[8:9], %1 offset:128\n"
    "ds_read_b64 a[10:11], %1 offset:136\n"
    "ds_read_b64 a[12:13], %1 offset:192\n"
    "ds_read_b64 a[14:15], %1 offset:200\n"
    "ds_read_b64 a[16:17], %1 offset:256\n"
    "ds_read_b64 a[18:19], %1 offset:264\n"
    "ds_read_b64 a[20:21], %1 offset:320\n"
    "ds_read_b64 a[22:23], %1 offset:328\n"
    "ds_read_b64 a[24:25], %1 offset:384\n"
    "ds_read_b64 a[26:27], %1 offset:392\n"
    "ds_read_b64 a[28:29], %1 offset:448\n"
    "ds_read_b64 a[30:31], %1 offset:456\n"
    "ds_read_b64 a[32:33], %1 offset:512\n"
    "ds_read_b64 a[34:35], %1 offset:520\n"
    "ds_read_b64 a[36:37], %1 offset:576\n"
    "ds_read_b64 a[38:39], %1 offset:584\n"
    "ds_read_b64 a[40:41], %1 offset:640\n"
    "ds_read_b64 a[42:43], %1 offset:648\n"
    "ds_read_b64 a[44:45], %1 offset:704\n"
    "ds_read_b64 a[46:47], %1 offset:712\n"
    "ds_read_b64 a[48:49], %1 offset:768\n"
    "ds_read_b64 a[50:51], %1 offset:776\n"
    "ds_read_b64 a[52:53], %1 offset:832\n"
    "ds_read_b64 a[54:55], %1 offset:840\n"
    "ds_read_b64 a[56:57], %1 offset:896\n"
    "ds_read_b64 a[58:59], %1 offset:904\n"
    "ds_read_b64 a[60:61], %1 offset:960\n"
    "ds_read_b64 a[62:63], %1 offset:968\n"
    "ds_read_b64 a[64:65], %1 offset:1024\n"
    "ds_read_b64 a[66:67], %1 offset:1032\n"
    "ds_read_b64 a[68:69], %1 offset:1088\n"
    "ds_read_b64 a[70:71], %1 offset:1096\n"
    "ds_read_b64 a[72:73], %1 offset:1152\n"
    "ds_read_b64 a[74:75], %1 offset:1160\n"
    "ds_read_b64 a[76:77], %1 offset:1216\n"
    "ds_read_b64 a[78:79], %1 offset:1224\n"
    "ds_read_b64 a[80:81], %1 offset:1280\n"
    "ds_read_b64 a[82:83], %1 offset:1288\n"
    "ds_read_b64 a[84:85], %1 offset:1344\n"
    "ds_read_b64 a[86:87], %1 offset:1352\n"
    "ds_read_b64 a[88:89], %1 offset:1408\n"
    "ds_read_b64 a[90:91], %1 offset:1416\n"
    "ds_read_b64 a[92:93], %1 offset:1472\n"
    "ds_read_b64 a[94:95], %1 offset:1480\n"
    "ds_read_b64 a[96:97], %1 offset:1536\n"
    "ds_read_b64 a[98:99], %1 offset:1544\n"
    "ds_read_b64 a[100:101], %1 offset:1600\n"
    "ds_read_b64 a[102:103], %1 offset:1608\n"
    "ds_read_b64 a[104:105], %1 offset:1664\n"
    "ds_read_b64 a[106:107], %1 offset:1672\n"
    "ds_read_b64 a[108:109], %1 offset:1728\n"
    "ds_read_b64 a[110:111], %1 offset:1736\n"
    "ds_read_b64 a[112:113], %1 offset:1792\n"
    "ds_read_b64 a[114:115], %1 offset:1800\n"
    "ds_read_b64 a[116:117], %1 offset:1856\n"
    "ds_read_b64 a[118:119], %1 offset:1864\n"
    "ds_read_b64 a[120:121], %1 offset:1920\n"
    "ds_read_b64 a[122:123], %1 offset:1928\n"
    "ds_read_b64 a[124:125], %1 offset:1984\n"
    "ds_read_b64 a[126:127], %1 offset:1992\n"
    "ds_read_b64 a[128:129], %1 offset:2048\n"
    "ds_read_b64 a[130:131], %1 offset:2056\n"
    "ds_read_b64 a[132:133], %1 offset:2112\n"
    "ds_read_b64 a[134:135], %1 offset:2120\n"
    "ds_read_b64 a[136:137], %1 offset:2176\n"
    "ds_read_b64 a[138:139], %1 offset:2184\n"
    "ds_read_b64 a[140:141], %1 offset:2240\n"
    "ds_read_b64 a[142:143], %1 offset:2248\n"
    "ds_read_b64 a[144:145], %1 offset:2304\n"
    "ds_read_b64 a[146:147], %1 offset:2312\n"
    "ds_read_b64 a[148:149], %1 offset:2368\n"
    "ds_read_b64 a[150:151], %1 offset:2376\n"
    "ds_read_b64 a[152:153], %1 offset:2432\n"
    "ds_read_b64 a[154:155], %1 offset:2440\n"
    "ds_read_b64 a[156:157], %1 offset:2496\n"
    "ds_read_b64 a[158:159], %1 offset:2504\n"
    "ds_read_b64 a[160:161], %1 offset:2560\n"
    "ds_read_b64 a[162:163], %1 offset:2568\n"
    "ds_read_b64 a[164:165], %1 offset:2624\n"
    "ds_read_b64 a[166:167], %1 offset:2632\n"
    "ds_read_b64 a[168:169], %1 offset:2688\n"
    "ds_read_b64 a[170:171], %1 offset:2696\n"
    "ds_read_b64 a[172:173], %1 offset:2752\n"
    "ds_read_b64 a[174:175], %1 offset:2760\n"
    "ds_read_b64 a[176:177], %1 offset:2816\n"
    "ds_read_b64 a[178:179], %1 offset:2824\n"
    "ds_read_b64 a[180:181], %1 offset:2880\n"
    "ds_read_b64 a[182:183], %1 offset:2888\n"
    "ds_read_b64 a[184:185], %1 offset:2944\n"
    "ds_read_b64 a[186:187], %1 offset:2952\n"
    "ds_read_b64 a[188:189], %1 offset:3008\n"
    "ds_read_b64 a[190:191], %1 offset:3016\n"
    "ds_read_b64 a[192:193], %1 offset:3072\n"
    "ds_read_b64 a[194:195], %1 offset:3080\n"
    "ds_read_b64 a[196:197], %1 offset:3136\n"
    "ds_read_b64 a[198:199], %1 offset:3144\n"
    "ds_read_b64 a[200:201], %1 offset:3200\n"
    "ds_read_b64 a[202:203], %1 offset:3208\n"
    "ds_read_b64 a[204:205], %1 offset:3264\n"
    "ds_read_b64 a[206:207], %1 offset:3272\n"
    "ds_read_b64 a[208:209], %1 offset:3328\n"
    "ds_read_b64 a[210:211], %1 offset:3336\n"
    "ds_read_b64 a[212:213], %1 offset:3392\n"
    "ds_read_b64 a[214:215], %1 offset:3400\n"
    "ds_read_b64 a[216:217], %1 offset:3456\n"
    "ds_read_b64 a[218:219], %1 offset:3464\n"
    "ds_read_b64 a[220:221], %1 offset:3520\n"
    "ds_read_b64 a[222:223], %1 offset:3528\n"
    "ds_read_b64 a[224:225], %1 offset:3584\n"
    "ds_read_b64 a[226:227], %1 offset:3592\n"
    "ds_read_b64 a[228:229], %1 offset:3648\n"
    "ds_read_b64 a[230:231], %1 offset:3656\n"
    "ds_read_b64 a[232:233], %1 offset:3712\n"
    "ds_read_b64 a[234:235], %1 offset:3720\n"
    "ds_read_b64 a[236:237], %1 offset:3776\n"
    "ds_read_b64 a[238:239], %1 offset:3784\n"
    "ds_read_b64 a[240:241], %1 offset:3840\n"
    "ds_read_b64 a[242:243], %1 offset:3848\n"
    "ds_read_b64 a[244:245], %1 offset:3904\n"
    "ds_read_b64 a[246:247], %1 offset:3912\n"
    "ds_read_b64 a[248:249], %1 offset:3968\n"
    "ds_read_b64 a[250:251], %1 offset:3976\n"
    "ds_read_b64 a[252:253], %1 offset:4032\n"
    "ds_read_b64 a[254:255], %1 offset:4040\n"
    "ds_read_b128 v[128:131], %2 offset:3584\n"
    "ds_read_b128 v[132:135], %2 offset:3648\n"
    "ds_read_b128 v[136:139], %2 offset:3712\n"
    "ds_read_b128 v[140:143], %2 offset:3776\n"
    "ds_read_b128 v[144:147], %2 offset:3840\n"
    "ds_read_b128 v[148:151], %2 offset:3904\n"
    "ds_read_b128 v[152:155], %2 offset:3968\n"
    "ds_read_b128 v[156:159], %2 offset:4032\n"
    "s_mov_b32 s90, 0\n"
    "CP_%=:\n"
    "buffer_load_dword v240, %3, %0, 0 offen sc1\n"
    "s_add_u32 s90, s90, 1\n"
    "s_cmp_gt_u32 s90, 262144\n"
    "s_cbranch_scc1 CQ_%=\n"
    "s_waitcnt vmcnt(0)\n"
    "v_cmp_gt_u32 vcc, 2, v240\n"
    "s_cbranch_vccnz CP_%=\n"
    "CQ_%=:\n"
    "s_mov_b32 m0, %6\n"
    "s_waitcnt lgkmcnt(0)\n"
    "buffer_load_dwordx4 %5, %0, %7 offen offset:3072 sc1 lds\n"
    "buffer_load_dwordx4 %5, %0, %7 offen offset:2048 sc1 lds\n"
    "buffer_load_dwordx4 %5, %0, %7 offen offset:1024 sc1 lds\n"
    "buffer_load_dwordx4 %5, %0, %7 offen offset:0 sc1 lds\n"
    "v_mfma_f32_16x16x32_bf16 v[196:199], a[32:35], v[128:131], 0\n"
    "v_mfma_f32_16x16x32_bf16 v[200:203], a[64:67], v[128:131], 0\n"
    "v_mfma_f32_16x16x32_bf16 v[204:207], a[96:99], v[128:131], 0\n"
    "v_mfma_f32_16x16x32_bf16 v[208:211], a[128:131], v[128:131], 0\n"
    "v_mfma_f32_16x16x32_bf16 v[212:215], a[160:163], v[128:131], 0\n"
    "v_mfma_f32_16x16x32_bf16 v[216:219], a[192:195], v[128:131], 0\n"
    "v_mfma_f32_16x16x32_bf16 v[220:223], a[224:227], v[128:131], 0\n"
    "v_mfma_f32_16x16x32_bf16 v[196:199], a[36:39], v[132:135], v[196:199]\n"
    "v_mfma_f32_16x16x32_bf16 v[200:203], a[68:71], v[132:135], v[200:203]\n"
    "v_mfma_f32_16x16x32_bf16 v[204:207], a[100:103], v[132:135], v[204:207]\n"
    "v_mfma_f32_16x16x32_bf16 v[208:211], a[132:135], v[132:135], v[208:211]\n"
    "v_mfma_f32_16x16x32_bf16 v[212:215], a[164:167], v[132:135], v[212:215]\n"
    "v_mfma_f32_16x16x32_bf16 v[216:219], a[196:199], v[132:135], v[216:219]\n"
    "v_mfma_f32_16x16x32_bf16 v[220:223], a[228:231], v[132:135], v[220:223]\n"
    "v_mfma_f32_16x16x32_bf16 v[196:199], a[40:43], v[136:139], v[196:199]\n"
    "v_mfma_f32_16x16x32_bf16 v[200:203], a[72:75], v[136:139], v[200:203]\n"
    "v_mfma_f32_16x16x32_bf16 v[204:207], a[104:107], v[136:139], v[204:207]\n"
    "v_mfma_f32_16x16x32_bf16 v[208:211], a[136:139], v[136:139], v[208:211]\n"
    "v_mfma_f32_16x16x32_bf16 v[212:215], a[168:171], v[136:139], v[212:215]\n"
    "v_mfma_f32_16x16x32_bf16 v[216:219], a[200:203], v[136:139], v[216:219]\n"
    "v_mfma_f32_16x16x32_bf16 v[220:223], a[232:235], v[136:139], v[220:223]\n"
    "v_mfma_f32_16x16x32_bf16 v[196:199], a[44:47], v[140:143], v[196:199]\n"
    "v_mfma_f32_16x16x32_bf16 v[200:203], a[76:79], v[140:143], v[200:203]\n"
    "v_mfma_f32_16x16x32_bf16 v[204:207], a[108:111], v[140:143], v[204:207]\n"
    "v_mfma_f32_16x16x32_bf16 v[208:211], a[140:143], v[140:143], v[208:211]\n"
    "v_mfma_f32_16x16x32_bf16 v[212:215], a[172:175], v[140:143], v[212:215]\n"
    "v_mfma_f32_16x16x32_bf16 v[216:219], a[204:207], v[140:143], v[216:219]\n"
    "v_mfma_f32_16x16x32_bf16 v[220:223], a[236:239], v[140:143], v[220:223]\n"
    "v_mfma_f32_16x16x32_bf16 v[196:199], a[48:51], v[144:147], v[196:199]\n"
    "v_mfma_f32_16x16x32_bf16 v[200:203], a[80:83], v[144:147], v[200:203]\n"
    "v_mfma_f32_16x16x32_bf16 v[204:207], a[112:115], v[144:147], v[204:207]\n"
    "v_mfma_f32_16x16x32_bf16 v[208:211], a[144:147], v[144:147], v[208:211]\n"
    "v_mfma_f32_16x16x32_bf16 v[212:215], a[176:179], v[144:147], v[212:215]\n"
    "v_mfma_f32_16x16x32_bf16 v[216:219], a[208:211], v[144:147], v[216:219]\n"
    "v_mfma_f32_16x16x32_bf16 v[220:223], a[240:243], v[144:147], v[220:223]\n"
    "v_mfma_f32_16x16x32_bf16 v[196:199], a[52:55], v[148:151], v[196:199]\n"
    "v_mfma_f32_16x16x32_bf16 v[200:203], a[84:87], v[148:151], v[200:203]\n"
    "v_mfma_f32_16x16x32_bf16 v[204:207], a[116:119], v[148:151], v[204:207]\n"
    "v_mfma_f32_16x16x32_bf16 v[208:211], a[148:151], v[148:151], v[208:211]\n"
    "v_mfma_f32_16x16x32_bf16 v[212:215], a[180:183], v[148:151], v[212:215]\n"
    "v_mfma_f32_16x16x32_bf16 v[216:219], a[212:215], v[148:151], v[216:219]\n"
    "s_waitcnt vmcnt(0)\n"
    "v_mfma_f32_16x16x32_bf16 v[220:223], a[244:247], v[148:151], v[220:223]\n"
    "ds_read_b128 v[160:163], %2 offset:3072\n"
    "v_mfma_f32_16x16x32_bf16 v[196:199], a[56:59], v[152:155], v[196:199]\n"
    "ds_read_b128 v[164:167], %2 offset:3136\n"
    "v_mfma_f32_16x16x32_bf16 v[200:203], a[88:91], v[152:155], v[200:203]\n"
    "ds_read_b128 v[168:171], %2 offset:3200\n"
    "v_mfma_f32_16x16x32_bf16 v[204:207], a[120:123], v[152:155], v[204:207]\n"
    "ds_read_b128 v[172:175], %2 offset:3264\n"
    "v_mfma_f32_16x16x32_bf16 v[208:211], a[152:155], v[152:155], v[208:211]\n"
    "ds_read_b128 v[176:179], %2 offset:3328\n"
    "v_mfma_f32_16x16x32_bf16 v[212:215], a[184:187], v[152:155], v[212:215]\n"
    "ds_read_b128 v[180:183], %2 offset:3392\n"
    "v_mfma_f32_16x16x32_bf16 v[216:219], a[216:219], v[152:155], v[216:219]\n"
    "ds_read_b128 v[184:187], %2 offset:3456\n"
    "v_mfma_f32_16x16x32_bf16 v[220:223], a[248:251], v[152:155], v[220:223]\n"
    "ds_read_b128 v[188:191], %2 offset:3520\n"
    "v_mfma_f32_16x16x32_bf16 v[196:199], a[60:63], v[156:159], v[196:199]\n"
    "ds_read_b64 v[226:227], %8 offset:3072\n"
    "v_mfma_f32_16x16x32_bf16 v[200:203], a[92:95], v[156:159], v[200:203]\n"
    "v_mfma_f32_16x16x32_bf16 v[204:207], a[124:127], v[156:159], v[204:207]\n"
    "v_mfma_f32_16x16x32_bf16 v[208:211], a[156:159], v[156:159], v[208:211]\n"
    "v_mfma_f32_16x16x32_bf16 v[212:215], a[188:191], v[156:159], v[212:215]\n"
    "v_mfma_f32_16x16x32_bf16 v[216:219], a[220:223], v[156:159], v[216:219]\n"
    "v_mfma_f32_16x16x32_bf16 v[220:223], a[252:255], v[156:159], v[220:223]\n"
    "s_waitcnt lgkmcnt(8)\n"
    "v_mfma_f32_16x16x32_bf16 v[196:199], a[0:3], v[160:163], v[196:199]\n"
    "ds_read_b128 v[128:131], %2 offset:2560\n"
    "v_mfma_f32_16x16x32_bf16 v[200:203], a[32:35], v[160:163], v[200:203]\n"
    "ds_read_b128 v[132:135], %2 offset:2624\n"
    "v_mfma_f32_16x16x32_bf16 v[204:207], a[64:67], v[160:163], v[204:207]\n"
    "ds_read_b128 v[136:139], %2 offset:2688\n"
    "v_mfma_f32_16x16x32_bf16 v[208:211], a[96:99], v[160:163], v[208:211]\n"
    "ds_read_b128 v[140:143], %2 offset:2752\n"
    "v_mfma_f32_16x16x32_bf16 v[212:215], a[128:131], v[160:163], v[212:215]\n"
    "ds_read_b128 v[144:147], %2 offset:2816\n"
    "v_mfma_f32_16x16x32_bf16 v[216:219], a[160:163], v[160:163], v[216:219]\n"
    "ds_read_b128 v[148:151], %2 offset:2880\n"
    "v_mfma_f32_16x16x32_bf16 v[220:223], a[192:195], v[160:163], v[220:223]\n"
    "ds_read_b128 v[152:155], %2 offset:2944\n"
    "s_waitcnt lgkmcnt(14)\n"
    "v_mfma_f32_16x16x32_bf16 v[196:199], a[4:7], v[164:167], v[196:199]\n"
    "ds_read_b128 v[156:159], %2 offset:3008\n"
    "v_mfma_f32_16x16x32_bf16 v[200:203], a[36:39], v[164:167], v[200:203]\n"
    "ds_read_b64 v[228:229], %8 offset:2560\n"
    "v_mfma_f32_16x16x32_bf16 v[204:207], a[68:71], v[164:167], v[204:207]\n"
    "v_mov_b32 v245, %11\n"
    "v_mfma_f32_16x16x32_bf16 v[208:211], a[100:103], v[164:167], v[208:211]\n"
    "global_load_dword v244, v245, %10\n"
    "v_mfma_f32_16x16x32_bf16 v[212:215], a[132:135], v[164:167], v[212:215]\n"
    "v_mfma_f32_16x16x32_bf16 v[216:219], a[164:167], v[164:167], v[216:219]\n"
    "v_add_u32 v245, %12, v245\n"
    "v_mfma_f32_16x16x32_bf16 v[220:223], a[196:199], v[164:167], v[220:223]\n"
    "s_waitcnt lgkmcnt(15)\n"
    "v_mfma_f32_16x16x32_bf16 v[196:199], a[8:11], v[168:171], v[196:199]\n"
    "global_load_dword v244, v245, %10\n"
    "v_mfma_f32_16x16x32_bf16 v[200:203], a[40:43], v[168:171], v[200:203]\n"
    "v_mfma_f32_16x16x32_bf16 v[204:207], a[72:75], v[168:171], v[204:207]\n"
    "v_add_u32 v245, %12, v245\n"
    "v_mfma_f32_16x16x32_bf16 v[208:211], a[104:107], v[168:171], v[208:211]\n"
    "v_mfma_f32_16x16x32_bf16 v[212:215], a[136:139], v[168:171], v[212:215]\n"
    "global_load_dword v244, v245, %10\n"
    "v_mfma_f32_16x16x32_bf16 v[216:219], a[168:171], v[168:171], v[216:219]\n"
    "v_mfma_f32_16x16x32_bf16 v[220:223], a[200:203], v[168:171], v[220:223]\n"
    "v_add_u32 v245, %12, v245\n"
    "s_waitcnt lgkmcnt(14)\n"
    "v_mfma_f32_16x16x32_bf16 v[196:199], a[12:15], v[172:175], v[196:199]\n"
    "v_mfma_f32_16x16x32_bf16 v[200:203], a[44:47], v[172:175], v[200:203]\n"
    "global_load_dword v244, v245, %10\n"
    "v_mfma_f32_16x16x32_bf16 v[204:207], a[76:79], v[172:175], v[204:207]\n"
    "v_mfma_f32_16x16x32_bf16 v[208:211], a[108:111], v[172:175], v[208:211]\n"
    "v_add_u32 v245, %12, v245\n"
    "v_mfma_f32_16x16x32_bf16 v[212:215], a[140:143], v[172:175], v[212:215]\n"
    "v_mfma_f32_16x16x32_bf16 v[216:219], a[172:175], v[172:175], v[216:219]\n"
    "v_mfma_f32_16x16x32_bf16 v[220:223], a[204:207], v[172:175], v[220:223]\n"
    "s_waitcnt lgkmcnt(13)\n"
    "v_mfma_f32_16x16x32_bf16 v[196:199], a[16:19], v[176:179], v[196:199]\n"
    "v_mfma_f32_16x16x32_bf16 v[200:203], a[48:51], v[176:179], v[200:203]\n"
    "v_mfma_f32_16x16x32_bf16 v[204:207], a[80:83], v[176:179], v[204:207]\n"
    "v_mfma_f32_16x16x32_bf16 v[208:211], a[112:115], v[176:179], v[208:211]\n"
    "v_mfma_f32_16x16x32_bf16 v[212:215], a[144:147], v[176:179], v[212:215]\n"
    "v_mfma_f32_16x16x32_bf16 v[216:219], a[176:179], v[176:179], v[216:219]\n"
    "v_mfma_f32_16x16x32_bf16 v[220:223], a[208:211], v[176:179], v[220:223]\n"
    "s_waitcnt lgkmcnt(12)\n"
    "v_mfma_f32_16x16x32_bf16 v[196:199], a[20:23], v[180:183], v[196:199]\n"
    "v_mfma_f32_16x16x32_bf16 v[200:203], a[52:55], v[180:183], v[200:203]\n"
    "v_mfma_f32_16x16x32_bf16 v[204:207], a[84:87], v[180:183], v[204:207]\n"
    "v_mfma_f32_16x16x32_bf16 v[208:211], a[116:119], v[180:183], v[208:211]\n"
    "v_mfma_f32_16x16x32_bf16 v[212:215], a[148:151], v[180:183], v[212:215]\n"
    "v_mfma_f32_16x16x32_bf16 v[216:219], a[180:183], v[180:183], v[216:219]\n"
    "v_mfma_f32_16x16x32_bf16 v[220:223], a[212:215], v[180:183], v[220:223]\n"
    "s_waitcnt lgkmcnt(11)\n"
    "v_mfma_f32_16x16x32_bf16 v[196:199], a[24:27], v[184:187], v[196:199]\n"
    "v_mfma_f32_16x16x32_bf16 v[200:203], a[56:59], v[184:187], v[200:203]\n"
    "v_mfma_f32_16x16x32_bf16 v[204:207], a[88:91], v[184:187], v[204:207]\n"
    "v_mfma_f32_16x16x32_bf16 v[208:211], a[120:123], v[184:187], v[208:211]\n"
    "v_mfma_f32_16x16x32_bf16 v[212:215], a[152:155], v[184:187], v[212:215]\n"
    "v_mfma_f32_16x16x32_bf16 v[216:219], a[184:187], v[184:187], v[216:219]\n"
    "v_mfma_f32_16x16x32_bf16 v[220:223], a[216:219], v[184:187], v[220:223]\n"
    "s_waitcnt lgkmcnt(10)\n"
    "v_mfma_f32_16x16x32_bf16 v[196:199], a[28:31], v[188:191], v[196:199]\n"
    "v_mfma_f32_16x16x32_bf16 v[200:203], a[60:63], v[188:191], v[200:203]\n"
    "v_mfma_f32_16x16x32_bf16 v[204:207], a[92:95], v[188:191], v[204:207]\n"
    "v_mfma_f32_16x16x32_bf16 v[208:211], a[124:127], v[188:191], v[208:211]\n"
    "v_mfma_f32_16x16x32_bf16 v[212:215], a[156:159], v[188:191], v[212:215]\n"
    "v_mfma_f32_16x16x32_bf16 v[216:219], a[188:191], v[188:191], v[216:219]\n"
    "v_mfma_f32_16x16x32_bf16 v[220:223], a[220:223], v[188:191], v[220:223]\n"
    "s_waitcnt lgkmcnt(8)\n"
    "v_mfma_f32_16x16x32_bf16 v[200:203], a[0:3], v[128:131], v[200:203]\n"
    "ds_read_b128 v[160:163], %2 offset:2048\n"
    "v_mfma_f32_16x16x32_bf16 v[204:207], a[32:35], v[128:131], v[204:207]\n"
    "ds_read_b128 v[164:167], %2 offset:2112\n"
    "v_mfma_f32_16x16x32_bf16 v[208:211], a[64:67], v[128:131], v[208:211]\n"
    "ds_read_b128 v[168:171], %2 offset:2176\n"
    "v_mfma_f32_16x16x32_bf16 v[212:215], a[96:99], v[128:131], v[212:215]\n"
    "ds_read_b128 v[172:175], %2 offset:2240\n"
    "v_mfma_f32_16x16x32_bf16 v[216:219], a[128:131], v[128:131], v[216:219]\n"
    "ds_read_b128 v[176:179], %2 offset:2304\n"
    "v_mfma_f32_16x16x32_bf16 v[220:223], a[160:163], v[128:131], v[220:223]\n"
    "ds_read_b128 v[180:183], %2 offset:2368\n"
    "s_waitcnt lgkmcnt(13)\n"
    "v_mfma_f32_16x16x32_bf16 v[200:203], a[4:7], v[132:135], v[200:203]\n"
    "ds_read_b128 v[184:187], %2 offset:2432\n"
    "v_mfma_f32_16x16x32_bf16 v[204:207], a[36:39], v[132:135], v[204:207]\n"
    "ds_read_b128 v[188:191], %2 offset:2496\n"
    "v_mfma_f32_16x16x32_bf16 v[208:211], a[68:71], v[132:135], v[208:211]\n"
    "ds_read_b64 v[230:231], %8 offset:2048\n"
    "v_mfma_f32_16x16x32_bf16 v[212:215], a[100:103], v[132:135], v[212:215]\n"
    "v_and_b32 v240, 0xffff0000, v227\n"
    "v_mfma_f32_16x16x32_bf16 v[216:219], a[132:135], v[132:135], v[216:219]\n"
    "v_lshlrev_b32 v241, 16, v227\n"
    "v_mfma_f32_16x16x32_bf16 v[220:223], a[164:167], v[132:135], v[220:223]\n"
    "v_and_b32 v242, 0xffff0000, v226\n"
    "s_waitcnt lgkmcnt(15)\n"
    "v_mfma_f32_16x16x32_bf16 v[200:203], a[8:11], v[136:139], v[200:203]\n"
    "v_lshlrev_b32 v243, 16, v226\n"
    "v_mfma_f32_16x16x32_bf16 v[204:207], a[40:43], v[136:139], v[204:207]\n"
    "v_fma_f32 v196, v240, %4, v196\n"
    "v_mfma_f32_16x16x32_bf16 v[208:211], a[72:75], v[136:139], v[208:211]\n"
    "v_fma_f32 v197, v241, %4, v197\n"
    "v_mfma_f32_16x16x32_bf16 v[212:215], a[104:107], v[136:139], v[212:215]\n"
    "v_fma_f32 v198, v242, %4, v198\n"
    "v_mfma_f32_16x16x32_bf16 v[216:219], a[136:139], v[136:139], v[216:219]\n"
    "v_fma_f32 v199, v243, %4, v199\n"
    "v_mfma_f32_16x16x32_bf16 v[220:223], a[168:171], v[136:139], v[220:223]\n"
    "v_cvt_pk_bf16_f32 v196, v196, v197\n"
    "s_waitcnt lgkmcnt(14)\n"
    "v_mfma_f32_16x16x32_bf16 v[200:203], a[12:15], v[140:143], v[200:203]\n"
    "v_cvt_pk_bf16_f32 v197, v198, v199\n"
    "v_mfma_f32_16x16x32_bf16 v[204:207], a[44:47], v[140:143], v[204:207]\n"
    "ds_write_b64 %9, v[196:197] offset:512\n"
    "v_mfma_f32_16x16x32_bf16 v[208:211], a[76:79], v[140:143], v[208:211]\n"
    "v_mfma_f32_16x16x32_bf16 v[212:215], a[108:111], v[140:143], v[212:215]\n"
    "v_mfma_f32_16x16x32_bf16 v[216:219], a[140:143], v[140:143], v[216:219]\n"
    "v_mfma_f32_16x16x32_bf16 v[220:223], a[172:175], v[140:143], v[220:223]\n"
    "s_waitcnt lgkmcnt(14)\n"
    "v_mfma_f32_16x16x32_bf16 v[200:203], a[16:19], v[144:147], v[200:203]\n"
    "v_mfma_f32_16x16x32_bf16 v[204:207], a[48:51], v[144:147], v[204:207]\n"
    "v_mfma_f32_16x16x32_bf16 v[208:211], a[80:83], v[144:147], v[208:211]\n"
    "v_mfma_f32_16x16x32_bf16 v[212:215], a[112:115], v[144:147], v[212:215]\n"
    "v_mfma_f32_16x16x32_bf16 v[216:219], a[144:147], v[144:147], v[216:219]\n"
    "v_mfma_f32_16x16x32_bf16 v[220:223], a[176:179], v[144:147], v[220:223]\n"
    "s_waitcnt lgkmcnt(13)\n"
    "v_mfma_f32_16x16x32_bf16 v[200:203], a[20:23], v[148:151], v[200:203]\n"
    "v_mfma_f32_16x16x32_bf16 v[204:207], a[52:55], v[148:151], v[204:207]\n"
    "v_mfma_f32_16x16x32_bf16 v[208:211], a[84:87], v[148:151], v[208:211]\n"
    "v_mfma_f32_16x16x32_bf16 v[212:215], a[116:119], v[148:151], v[212:215]\n"
    "v_mfma_f32_16x16x32_bf16 v[216:219], a[148:151], v[148:151], v[216:219]\n"
    "v_mfma_f32_16x16x32_bf16 v[220:223], a[180:183], v[148:151], v[220:223]\n"
    "s_waitcnt lgkmcnt(12)\n"
    "v_mfma_f32_16x16x32_bf16 v[200:203], a[24:27], v[152:155], v[200:203]\n"
    "v_mfma_f32_16x16x32_bf16 v[204:207], a[56:59], v[152:155], v[204:207]\n"
    "v_mfma_f32_16x16x32_bf16 v[208:211], a[88:91], v[152:155], v[208:211]\n"
    "v_mfma_f32_16x16x32_bf16 v[212:215], a[120:123], v[152:155], v[212:215]\n"
    "v_mfma_f32_16x16x32_bf16 v[216:219], a[152:155], v[152:155], v[216:219]\n"
    "v_mfma_f32_16x16x32_bf16 v[220:223], a[184:187], v[152:155], v[220:223]\n"
    "s_waitcnt lgkmcnt(11)\n"
    "v_mfma_f32_16x16x32_bf16 v[200:203], a[28:31], v[156:159], v[200:203]\n"
    "v_mfma_f32_16x16x32_bf16 v[204:207], a[60:63], v[156:159], v[204:207]\n"
    "v_mfma_f32_16x16x32_bf16 v[208:211], a[92:95], v[156:159], v[208:211]\n"
    "v_mfma_f32_16x16x32_bf16 v[212:215], a[124:127], v[156:159], v[212:215]\n"
    "v_mfma_f32_16x16x32_bf16 v[216:219], a[156:159], v[156:159], v[216:219]\n"
    "v_mfma_f32_16x16x32_bf16 v[220:223], a[188:191], v[156:159], v[220:223]\n"
    "s_waitcnt lgkmcnt(9)\n"
    "v_mfma_f32_16x16x32_bf16 v[204:207], a[0:3], v[160:163], v[204:207]\n"
    "ds_read_b128 v[128:131], %2 offset:1536\n"
    "v_mfma_f32_16x16x32_bf16 v[208:211], a[32:35], v[160:163], v[208:211]\n"
    "ds_read_b128 v[132:135], %2 offset:1600\n"
    "v_mfma_f32_16x16x32_bf16 v[212:215], a[64:67], v[160:163], v[212:215]\n"
    "ds_read_b128 v[136:139], %2 offset:1664\n"
    "v_mfma_f32_16x16x32_bf16 v[216:219], a[96:99], v[160:163], v[216:219]\n"
    "ds_read_b128 v[140:143], %2 offset:1728\n"
    "v_mfma_f32_16x16x32_bf16 v[220:223], a[128:131], v[160:163], v[220:223]\n"
    "ds_read_b128 v[144:147], %2 offset:1792\n"
    "s_waitcnt lgkmcnt(13)\n"
    "v_mfma_f32_16x16x32_bf16 v[204:207], a[4:7], v[164:167], v[204:207]\n"
    "ds_read_b128 v[148:151], %2 offset:1856\n"
    "v_mfma_f32_16x16x32_bf16 v[208:211], a[36:39], v[164:167], v[208:211]\n"
    "ds_read_b128 v[152:155], %2 offset:1920\n"
    "v_mfma_f32_16x16x32_bf16 v[212:215], a[68:71], v[164:167], v[212:215]\n"
    "ds_read_b128 v[156:159], %2 offset:1984\n"
    "v_mfma_f32_16x16x32_bf16 v[216:219], a[100:103], v[164:167], v[216:219]\n"
    "ds_read_b64 v[232:233], %8 offset:1536\n"
    "v_mfma_f32_16x16x32_bf16 v[220:223], a[132:135], v[164:167], v[220:223]\n"
    "v_and_b32 v240, 0xffff0000, v229\n"
    "s_waitcnt lgkmcnt(15)\n"
    "v_mfma_f32_16x16x32_bf16 v[204:207], a[8:11], v[168:171], v[204:207]\n"
    "v_lshlrev_b32 v241, 16, v229\n"
    "v_mfma_f32_16x16x32_bf16 v[208:211], a[40:43], v[168:171], v[208:211]\n"
    "v_and_b32 v242, 0xffff0000, v228\n"
    "v_mfma_f32_16x16x32_bf16 v[212:215], a[72:75], v[168:171], v[212:215]\n"
    "v_lshlrev_b32 v243, 16, v228\n"
    "v_mfma_f32_16x16x32_bf16 v[216:219], a[104:107], v[168:171], v[216:219]\n"
    "v_fma_f32 v200, v240, %4, v200\n"
    "v_mfma_f32_16x16x32_bf16 v[220:223], a[136:139], v[168:171], v[220:223]\n"
    "v_fma_f32 v201, v241, %4, v201\n"
    "v_mfma_f32_16x16x32_bf16 v[204:207], a[12:15], v[172:175], v[204:207]\n"
    "v_fma_f32 v202, v242, %4, v202\n"
    "v_mfma_f32_16x16x32_bf16 v[208:211], a[44:47], v[172:175], v[208:211]\n"
    "v_fma_f32 v203, v243, %4, v203\n"
    "v_mfma_f32_16x16x32_bf16 v[212:215], a[76:79], v[172:175], v[212:215]\n"
    "v_cvt_pk_bf16_f32 v200, v200, v201\n"
    "v_mfma_f32_16x16x32_bf16 v[216:219], a[108:111], v[172:175], v[216:219]\n"
    "v_cvt_pk_bf16_f32 v201, v202, v203\n"
    "v_mfma_f32_16x16x32_bf16 v[220:223], a[140:143], v[172:175], v[220:223]\n"
    "ds_write_b64 %9, v[200:201] offset:1024\n"
    "s_waitcnt lgkmcnt(15)\n"
    "v_mfma_f32_16x16x32_bf16 v[204:207], a[16:19], v[176:179], v[204:207]\n"
    "v_mfma_f32_16x16x32_bf16 v[208:211], a[48:51], v[176:179], v[208:211]\n"
    "v_mfma_f32_16x16x32_bf16 v[212:215], a[80:83], v[176:179], v[212:215]\n"
    "v_mfma_f32_16x16x32_bf16 v[216:219], a[112:115], v[176:179], v[216:219]\n"
    "v_mfma_f32_16x16x32_bf16 v[220:223], a[144:147], v[176:179], v[220:223]\n"
    "s_waitcnt lgkmcnt(14)\n"
    "v_mfma_f32_16x16x32_bf16 v[204:207], a[20:23], v[180:183], v[204:207]\n"
    "v_mfma_f32_16x16x32_bf16 v[208:211], a[52:55], v[180:183], v[208:211]\n"
    "v_mfma_f32_16x16x32_bf16 v[212:215], a[84:87], v[180:183], v[212:215]\n"
    "v_mfma_f32_16x16x32_bf16 v[216:219], a[116:119], v[180:183], v[216:219]\n"
    "v_mfma_f32_16x16x32_bf16 v[220:223], a[148:151], v[180:183], v[220:223]\n"
    "s_waitcnt lgkmcnt(13)\n"
    "v_mfma_f32_16x16x32_bf16 v[204:207], a[24:27], v[184:187], v[204:207]\n"
    "v_mfma_f32_16x16x32_bf16 v[208:211], a[56:59], v[184:187], v[208:211]\n"
    "v_mfma_f32_16x16x32_bf16 v[212:215], a[88:91], v[184:187], v[212:215]\n"
    "v_mfma_f32_16x16x32_bf16 v[216:219], a[120:123], v[184:187], v[216:219]\n"
    "v_mfma_f32_16x16x32_bf16 v[220:223], a[152:155], v[184:187], v[220:223]\n"
    "s_waitcnt lgkmcnt(12)\n"
    "v_mfma_f32_16x16x32_bf16 v[204:207], a[28:31], v[188:191], v[204:207]\n"
    "v_mfma_f32_16x16x32_bf16 v[208:211], a[60:63], v[188:191], v[208:211]\n"
    "v_mfma_f32_16x16x32_bf16 v[212:215], a[92:95], v[188:191], v[212:215]\n"
    "v_mfma_f32_16x16x32_bf16 v[216:219], a[124:127], v[188:191], v[216:219]\n"
    "v_mfma_f32_16x16x32_bf16 v[220:223], a[156:159], v[188:191], v[220:223]\n"
    "s_waitcnt lgkmcnt(9)\n"
    "v_mfma_f32_16x16x32_bf16 v[208:211], a[0:3], v[128:131], v[208:211]\n"
    "ds_read_b128 v[160:163], %2 offset:1024\n"
    "v_mfma_f32_16x16x32_bf16 v[212:215], a[32:35], v[128:131], v[212:215]\n"
    "ds_read_b128 v[164:167], %2 offset:1088\n"
    "v_mfma_f32_16x16x32_bf16 v[216:219], a[64:67], v[128:131], v[216:219]\n"
    "ds_read_b128 v[168:171], %2 offset:1152\n"
    "v_mfma_f32_16x16x32_bf16 v[220:223], a[96:99], v[128:131], v[220:223]\n"
    "ds_read_b128 v[172:175], %2 offset:1216\n"
    "s_waitcnt lgkmcnt(12)\n"
    "v_mfma_f32_16x16x32_bf16 v[208:211], a[4:7], v[132:135], v[208:211]\n"
    "ds_read_b128 v[176:179], %2 offset:1280\n"
    "v_mfma_f32_16x16x32_bf16 v[212:215], a[36:39], v[132:135], v[212:215]\n"
    "ds_read_b128 v[180:183], %2 offset:1344\n"
    "v_mfma_f32_16x16x32_bf16 v[216:219], a[68:71], v[132:135], v[216:219]\n"
    "ds_read_b128 v[184:187], %2 offset:1408\n"
    "v_mfma_f32_16x16x32_bf16 v[220:223], a[100:103], v[132:135], v[220:223]\n"
    "ds_read_b128 v[188:191], %2 offset:1472\n"
    "s_waitcnt lgkmcnt(15)\n"
    "v_mfma_f32_16x16x32_bf16 v[208:211], a[8:11], v[136:139], v[208:211]\n"
    "ds_read_b64 v[234:235], %8 offset:1024\n"
    "v_mfma_f32_16x16x32_bf16 v[212:215], a[40:43], v[136:139], v[212:215]\n"
    "v_and_b32 v240, 0xffff0000, v231\n"
    "v_mfma_f32_16x16x32_bf16 v[216:219], a[72:75], v[136:139], v[216:219]\n"
    "v_lshlrev_b32 v241, 16, v231\n"
    "v_mfma_f32_16x16x32_bf16 v[220:223], a[104:107], v[136:139], v[220:223]\n"
    "v_and_b32 v242, 0xffff0000, v230\n"
    "s_waitcnt lgkmcnt(15)\n"
    "v_mfma_f32_16x16x32_bf16 v[208:211], a[12:15], v[140:143], v[208:211]\n"
    "v_lshlrev_b32 v243, 16, v230\n"
    "v_mfma_f32_16x16x32_bf16 v[212:215], a[44:47], v[140:143], v[212:215]\n"
    "v_fma_f32 v204, v240, %4, v204\n"
    "v_mfma_f32_16x16x32_bf16 v[216:219], a[76:79], v[140:143], v[216:219]\n"
    "v_fma_f32 v205, v241, %4, v205\n"
    "v_mfma_f32_16x16x32_bf16 v[220:223], a[108:111], v[140:143], v[220:223]\n"
    "v_fma_f32 v206, v242, %4, v206\n"
    "s_waitcnt lgkmcnt(14)\n"
    "v_mfma_f32_16x16x32_bf16 v[208:211], a[16:19], v[144:147], v[208:211]\n"
    "v_fma_f32 v207, v243, %4, v207\n"
    "v_mfma_f32_16x16x32_bf16 v[212:215], a[48:51], v[144:147], v[212:215]\n"
    "v_cvt_pk_bf16_f32 v204, v204, v205\n"
    "v_mfma_f32_16x16x32_bf16 v[216:219], a[80:83], v[144:147], v[216:219]\n"
    "v_cvt_pk_bf16_f32 v205, v206, v207\n"
    "v_mfma_f32_16x16x32_bf16 v[220:223], a[112:115], v[144:147], v[220:223]\n"
    "ds_write_b64 %9, v[204:205] offset:1536\n"
    "s_waitcnt lgkmcnt(14)\n"
    "v_mfma_f32_16x16x32_bf16 v[208:211], a[20:23], v[148:151], v[208:211]\n"
    "v_mfma_f32_16x16x32_bf16 v[212:215], a[52:55], v[148:151], v[212:215]\n"
    "v_mfma_f32_16x16x32_bf16 v[216:219], a[84:87], v[148:151], v[216:219]\n"
    "v_mfma_f32_16x16x32_bf16 v[220:223], a[116:119], v[148:151], v[220:223]\n"
    "s_waitcnt lgkmcnt(13)\n"
    "v_mfma_f32_16x16x32_bf16 v[208:211], a[24:27], v[152:155], v[208:211]\n"
    "v_mfma_f32_16x16x32_bf16 v[212:215], a[56:59], v[152:155], v[212:215]\n"
    "v_mfma_f32_16x16x32_bf16 v[216:219], a[88:91], v[152:155], v[216:219]\n"
    "v_mfma_f32_16x16x32_bf16 v[220:223], a[120:123], v[152:155], v[220:223]\n"
    "s_waitcnt lgkmcnt(12)\n"
    "v_mfma_f32_16x16x32_bf16 v[208:211], a[28:31], v[156:159], v[208:211]\n"
    "v_mfma_f32_16x16x32_bf16 v[212:215], a[60:63], v[156:159], v[212:215]\n"
    "v_mfma_f32_16x16x32_bf16 v[216:219], a[92:95], v[156:159], v[216:219]\n"
    "v_mfma_f32_16x16x32_bf16 v[220:223], a[124:127], v[156:159], v[220:223]\n"
    "s_waitcnt lgkmcnt(9)\n"
    "v_mfma_f32_16x16x32_bf16 v[212:215], a[0:3], v[160:163], v[212:215]\n"
    "ds_read_b128 v[128:131], %2 offset:512\n"
    "v_mfma_f32_16x16x32_bf16 v[216:219], a[32:35], v[160:163], v[216:219]\n"
    "ds_read_b128 v[132:135], %2 offset:576\n"
    "v_mfma_f32_16x16x32_bf16 v[220:223], a[64:67], v[160:163], v[220:223]\n"
    "ds_read_b128 v[136:139], %2 offset:640\n"
    "s_waitcnt lgkmcnt(11)\n"
    "v_mfma_f32_16x16x32_bf16 v[212:215], a[4:7], v[164:167], v[212:215]\n"
    "ds_read_b128 v[140:143], %2 offset:704\n"
    "v_mfma_f32_16x16x32_bf16 v[216:219], a[36:39], v[164:167], v[216:219]\n"
    "ds_read_b128 v[144:147], %2 offset:768\n"
    "v_mfma_f32_16x16x32_bf16 v[220:223], a[68:71], v[164:167], v[220:223]\n"
    "ds_read_b128 v[148:151], %2 offset:832\n"
    "s_waitcnt lgkmcnt(13)\n"
    "v_mfma_f32_16x16x32_bf16 v[212:215], a[8:11], v[168:171], v[212:215]\n"
    "ds_read_b128 v[152:155], %2 offset:896\n"
    "v_mfma_f32_16x16x32_bf16 v[216:219], a[40:43], v[168:171], v[216:219]\n"
    "ds_read_b128 v[156:159], %2 offset:960\n"
    "v_mfma_f32_16x16x32_bf16 v[220:223], a[72:75], v[168:171], v[220:223]\n"
    "ds_read_b64 v[236:237], %8 offset:512\n"
    "s_waitcnt lgkmcnt(15)\n"
    "v_mfma_f32_16x16x32_bf16 v[212:215], a[12:15], v[172:175], v[212:215]\n"
    "v_and_b32 v240, 0xffff0000, v233\n"
    "v_mfma_f32_16x16x32_bf16 v[216:219], a[44:47], v[172:175], v[216:219]\n"
    "v_lshlrev_b32 v241, 16, v233\n"
    "v_mfma_f32_16x16x32_bf16 v[220:223], a[76:79], v[172:175], v[220:223]\n"
    "v_and_b32 v242, 0xffff0000, v232\n"
    "s_waitcnt lgkmcnt(14)\n"
    "v_mfma_f32_16x16x32_bf16 v[212:215], a[16:19], v[176:179], v[212:215]\n"
    "v_lshlrev_b32 v243, 16, v232\n"
    "v_mfma_f32_16x16x32_bf16 v[216:219], a[48:51], v[176:179], v[216:219]\n"
    "v_fma_f32 v208, v240, %4, v208\n"
    "v_mfma_f32_16x16x32_bf16 v[220:223], a[80:83], v[176:179], v[220:223]\n"
    "v_fma_f32 v209, v241, %4, v209\n"
    "s_waitcnt lgkmcnt(13)\n"
    "v_mfma_f32_16x16x32_bf16 v[212:215], a[20:23], v[180:183], v[212:215]\n"
    "v_fma_f32 v210, v242, %4, v210\n"
    "v_mfma_f32_16x16x32_bf16 v[216:219], a[52:55], v[180:183], v[216:219]\n"
    "v_fma_f32 v211, v243, %4, v211\n"
    "v_mfma_f32_16x16x32_bf16 v[220:223], a[84:87], v[180:183], v[220:223]\n"
    "v_cvt_pk_bf16_f32 v208, v208, v209\n"
    "s_waitcnt lgkmcnt(12)\n"
    "v_mfma_f32_16x16x32_bf16 v[212:215], a[24:27], v[184:187], v[212:215]\n"
    "v_cvt_pk_bf16_f32 v209, v210, v211\n"
    "v_mfma_f32_16x16x32_bf16 v[216:219], a[56:59], v[184:187], v[216:219]\n"
    "ds_write_b64 %9, v[208:209] offset:2048\n"
    "v_mfma_f32_16x16x32_bf16 v[220:223], a[88:91], v[184:187], v[220:223]\n"
    "s_waitcnt lgkmcnt(12)\n"
    "v_mfma_f32_16x16x32_bf16 v[212:215], a[28:31], v[188:191], v[212:215]\n"
    "v_mfma_f32_16x16x32_bf16 v[216:219], a[60:63], v[188:191], v[216:219]\n"
    "v_mfma_f32_16x16x32_bf16 v[220:223], a[92:95], v[188:191], v[220:223]\n"
    "s_waitcnt lgkmcnt(9)\n"
    "v_mfma_f32_16x16x32_bf16 v[216:219], a[0:3], v[128:131], v[216:219]\n"
    "ds_read_b128 v[160:163], %2 offset:0\n"
    "v_mfma_f32_16x16x32_bf16 v[220:223], a[32:35], v[128:131], v[220:223]\n"
    "ds_read_b128 v[164:167], %2 offset:64\n"
    "s_waitcnt lgkmcnt(10)\n"
    "v_mfma_f32_16x16x32_bf16 v[192:195], a[4:7], v[132:135], 0\n"
    "ds_read_b128 v[168:171], %2 offset:128\n"
    "v_mfma_f32_16x16x32_bf16 v[196:199], a[36:39], v[132:135], 0\n"
    "ds_read_b128 v[172:175], %2 offset:192\n"
    "s_waitcnt lgkmcnt(11)\n"
    "v_mfma_f32_16x16x32_bf16 v[216:219], a[8:11], v[136:139], v[216:219]\n"
    "ds_read_b128 v[176:179], %2 offset:256\n"
    "v_mfma_f32_16x16x32_bf16 v[220:223], a[40:43], v[136:139], v[220:223]\n"
    "ds_read_b128 v[180:183], %2 offset:320\n"
    "s_waitcnt lgkmcnt(12)\n"
    "v_mfma_f32_16x16x32_bf16 v[192:195], a[12:15], v[140:143], v[192:195]\n"
    "ds_read_b128 v[184:187], %2 offset:384\n"
    "v_mfma_f32_16x16x32_bf16 v[196:199], a[44:47], v[140:143], v[196:199]\n"
    "ds_read_b128 v[188:191], %2 offset:448\n"
    "s_waitcnt lgkmcnt(13)\n"
    "v_mfma_f32_16x16x32_bf16 v[216:219], a[16:19], v[144:147], v[216:219]\n"
    "ds_read_b64 v[238:239], %8 offset:0\n"
    "v_mfma_f32_16x16x32_bf16 v[220:223], a[48:51], v[144:147], v[220:223]\n"
    "v_and_b32 v240, 0xffff0000, v235\n"
    "s_waitcnt lgkmcnt(13)\n"
    "v_mfma_f32_16x16x32_bf16 v[192:195], a[20:23], v[148:151], v[192:195]\n"
    "v_lshlrev_b32 v241, 16, v235\n"
    "v_mfma_f32_16x16x32_bf16 v[196:199], a[52:55], v[148:151], v[196:199]\n"
    "v_and_b32 v242, 0xffff0000, v234\n"
    "s_waitcnt lgkmcnt(12)\n"
    "v_mfma_f32_16x16x32_bf16 v[216:219], a[24:27], v[152:155], v[216:219]\n"
    "v_lshlrev_b32 v243, 16, v234\n"
    "v_mfma_f32_16x16x32_bf16 v[220:223], a[56:59], v[152:155], v[220:223]\n"
    "v_fma_f32 v212, v240, %4, v212\n"
    "s_waitcnt lgkmcnt(11)\n"
    "v_mfma_f32_16x16x32_bf16 v[192:195], a[28:31], v[156:159], v[192:195]\n"
    "v_fma_f32 v213, v241, %4, v213\n"
    "v_mfma_f32_16x16x32_bf16 v[196:199], a[60:63], v[156:159], v[196:199]\n"
    "v_fma_f32 v214, v242, %4, v214\n"
    "v_fma_f32 v215, v243, %4, v215\n"
    "v_cvt_pk_bf16_f32 v212, v212, v213\n"
    "v_cvt_pk_bf16_f32 v213, v214, v215\n"
    "ds_write_b64 %9, v[212:213] offset:2560\n"
    "s_waitcnt lgkmcnt(9)\n"
    "v_mfma_f32_16x16x32_bf16 v[220:223], a[0:3], v[160:163], v[220:223]\n"
    "v_add_f32 v216, v216, v192\n"
    "s_waitcnt lgkmcnt(8)\n"
    "v_mfma_f32_16x16x32_bf16 v[196:199], a[4:7], v[164:167], v[196:199]\n"
    "v_add_f32 v217, v217, v193\n"
    "s_waitcnt lgkmcnt(7)\n"
    "v_mfma_f32_16x16x32_bf16 v[220:223], a[8:11], v[168:171], v[220:223]\n"
    "v_add_f32 v218, v218, v194\n"
    "s_waitcnt lgkmcnt(6)\n"
    "v_mfma_f32_16x16x32_bf16 v[196:199], a[12:15], v[172:175], v[196:199]\n"
    "v_add_f32 v219, v219, v195\n"
    "s_waitcnt lgkmcnt(5)\n"
    "v_mfma_f32_16x16x32_bf16 v[220:223], a[16:19], v[176:179], v[220:223]\n"
    "v_and_b32 v240, 0xffff0000, v237\n"
    "s_waitcnt lgkmcnt(4)\n"
    "v_mfma_f32_16x16x32_bf16 v[196:199], a[20:23], v[180:183], v[196:199]\n"
    "v_lshlrev_b32 v241, 16, v237\n"
    "s_waitcnt lgkmcnt(3)\n"
    "v_mfma_f32_16x16x32_bf16 v[220:223], a[24:27], v[184:187], v[220:223]\n"
    "v_and_b32 v242, 0xffff0000, v236\n"
    "s_waitcnt lgkmcnt(2)\n"
    "v_mfma_f32_16x16x32_bf16 v[196:199], a[28:31], v[188:191], v[196:199]\n"
    "v_lshlrev_b32 v243, 16, v236\n"
    "v_fma_f32 v216, v240, %4, v216\n"
    "v_fma_f32 v217, v241, %4, v217\n"
    "v_fma_f32 v218, v242, %4, v218\n"
    "v_fma_f32 v219, v243, %4, v219\n"
    "v_cvt_pk_bf16_f32 v216, v216, v217\n"
    "v_cvt_pk_bf16_f32 v217, v218, v219\n"
    "ds_write_b64 %9, v[216:217] offset:3072\n"
    "v_add_f32 v220, v220, v196\n"
    "v_add_f32 v221, v221, v197\n"
    "v_add_f32 v222, v222, v198\n"
    "v_add_f32 v223, v223, v199\n"
    "s_waitcnt lgkmcnt(2)\n"
    "v_and_b32 v240, 0xffff0000, v239\n"
    "v_lshlrev_b32 v241, 16, v239\n"
    "v_and_b32 v242, 0xffff0000, v238\n"
    "v_lshlrev_b32 v243, 16, v238\n"
    "v_fma_f32 v220, v240, %4, v220\n"
    "v_fma_f32 v221, v241, %4, v221\n"
    "v_fma_f32 v222, v242, %4, v222\n"
    "v_fma_f32 v223, v243, %4, v223\n"
    "v_cvt_pk_bf16_f32 v220, v220, v221\n"
    "v_cvt_pk_bf16_f32 v221, v222, v223\n"
    "ds_write_b64 %9, v[220:221] offset:3584\n"
    "s_waitcnt vmcnt(0) lgkmcnt(0)\n"
    :: "s"(W), "v"((unsigned)(uintptr_t)ka), "v"((unsigned)(uintptr_t)ub - 3584), "v"(OFF_ACK + (8 * (p >> 2) + 4 + (p & 3)) * 128), "v"(dv), "v"(lane * 16),
       "s"(L_UR + wave * 4624), "s"(OFF_UT + gw * 4096), "v"((unsigned)(uintptr_t)ue - 3584), "v"((unsigned)(uintptr_t)&yo[wave][16 * c + 4 * g]),
       "s"(pw), "v"(po), "s"(part == 0 ? 8192 : 262144)
    : "v128", "v129", "v130", "v131", "v132", "v133", "v134", "v135", "v136", "v137", "v138", "v139", "v140", "v141", "v142", "v143", "v144", "v145", "v146", "v147", "v148", "v149", "v150", "v151", "v152", "v153", "v154", "v155", "v156", "v157", "v158", "v159", "v160", "v161", "v162", "v163", "v164", "v165", "v166", "v167", "v168", "v169", "v170", "v171", "v172", "v173", "v174", "v175", "v176", "v177", "v178", "v179", "v180", "v181", "v182", "v183", "v184", "v185", "v186", "v187", "v188", "v189", "v190", "v191", "v192", "v193", "v194", "v195", "v196", "v197", "v198", "v199", "v200", "v201", "v202", "v203", "v204", "v205", "v206", "v207", "v208", "v209", "v210", "v211", "v212", "v213", "v214", "v215", "v216", "v217", "v218", "v219", "v220", "v221", "v222", "v223", "v224", "v225", "v226", "v227", "v228", "v229", "v230", "v231", "v232", "v233", "v234", "v235", "v236", "v237", "v238", "v239", "v240", "v241", "v242", "v243", "v244", "v245", "a0", "a1", "a2", "a3", "a4", "a5", "a6", "a7", "a8", "a9", "a10", "a11", "a12", "a13", "a14", "a15", "a16", "a17", "a18", "a19", "a20", "a21", "a22", "a23", "a24", "a25", "a26", "a27", "a28", "a29", "a30", "a31", "a32", "a33", "a34", "a35", "a36", "a37", "a38", "a39", "a40", "a41", "a42", "a43", "a44", "a45", "a46", "a47", "a48", "a49", "a50", "a51", "a52", "a53", "a54", "a55", "a56", "a57", "a58", "a59", "a60", "a61", "a62", "a63", "a64", "a65", "a66", "a67", "a68", "a69", "a70", "a71", "a72", "a73", "a74", "a75", "a76", "a77", "a78", "a79", "a80", "a81", "a82", "a83", "a84", "a85", "a86", "a87", "a88", "a89", "a90", "a91", "a92", "a93", "a94", "a95", "a96", "a97", "a98", "a99", "a100", "a101", "a102", "a103", "a104", "a105", "a106", "a107", "a108", "a109", "a110", "a111", "a112", "a113", "a114", "a115", "a116", "a117", "a118", "a119", "a120", "a121", "a122", "a123", "a124", "a125", "a126", "a127", "a128", "a129", "a130", "a131", "a132", "a133", "a134", "a135", "a136", "a137", "a138", "a139", "a140", "a141", "a142", "a143", "a144", "a145", "a146", "a147", "a148", "a149", "a150", "a151", "a152", "a153", "a154", "a155", "a156", "a157", "a158", "a159", "a160", "a161", "a162", "a163", "a164", "a165", "a166", "a167", "a168", "a169", "a170", "a171", "a172", "a173", "a174", "a175", "a176", "a177", "a178", "a179", "a180", "a181", "a182", "a183", "a184", "a185", "a186", "a187", "a188", "a189", "a190", "a191", "a192", "a193", "a194", "a195", "a196", "a197", "a198", "a199", "a200", "a201", "a202", "a203", "a204", "a205", "a206", "a207", "a208", "a209", "a210", "a211", "a212", "a213", "a214", "a215", "a216", "a217", "a218", "a219", "a220", "a221", "a222", "a223", "a224", "a225", "a226", "a227", "a228", "a229", "a230", "a231", "a232", "a233", "a234", "a235", "a236", "a237", "a238", "a239", "a240", "a241", "a242", "a243", "a244", "a245", "a246", "a247", "a248", "a249", "a250", "a251", "a252", "a253", "a254", "a255", "s90", "vcc", "scc", "m0", "memory");
  LBAR();
#pragma unroll
  for (int q = 0; q < 4; ++q) if (q < 3 || tid < 128) put(256 + 512 * q + 2 * tid);
  ack(W, 3);
  return h0;
}

DEVI void pre_rest(const Args& A, rsrc_t W, int i, int lane, int wave, unsigned h0) {
  extern __shared__ __attribute__((aligned(16))) unsigned char lds[];
  const unsigned pol = beat_state(W, h0, 2) & 1;
  if (i >= 4) {
    const int t0 = 64 * i + 16 * wave;
    unsigned char* yl = lds + wave * 16384;
    const rsrc_t Q = __builtin_amdgcn_make_buffer_rsrc((void*)A.q, 0, S * D * 2, 0x00020000);
#pragma unroll
    for (int q = 0; q < 16; ++q) __builtin_amdgcn_raw_ptr_buffer_load_lds(Q, (LAS void*)(yl + 65536 + 1024 * q), 16, lane * 16, (t0 + q) * 1024, 0, AUXNT);
    wait_conv(W, lane, 3);
#pragma unroll
    for (int q = 0; q < 16; ++q) DMA16C(W, yl + 1024 * q, (lane >> 3) * 16384 + ((lane ^ (4 * q + (lane >> 4))) & 7) * 16, OFF_YB + q * 8 * 16384 + t0 * 8);
    DRAIN();
#pragma unroll 1
    for (int j = 0; j < 8; ++j) {
      const v4u c0 = *(const v4u*)(yl + 65536 + j * 2048 + lane * 16), c1 = *(const v4u*)(yl + 65536 + 1024 + j * 2048 + lane * 16), ya = *(const v4u*)(yl + lane * 256 + ((lane ^ j) & 7) * 16), yb = *(const v4u*)(yl + lane * 256 + 128 + ((lane ^ j) & 7) * 16);
      const int t = t0 + 2 * j;
      ln_token<false>(W, t, lane, pol, c0, (v4u){ya.x, ya.y, yb.x, yb.y});
      ln_token<false>(W, t + 1, lane, pol, c1, (v4u){ya.z, ya.w, yb.z, yb.w});
    }
  }
  const unsigned fl = D_OP + U_FH * 16384 + ((pol ^ 1) << 13) + (lane & 1) * 4096 + i * 128;
  for (unsigned n = 0; n < kSpinCap; ++n) {
    const unsigned a = LD4C(W, fl, 0); CFENCE();
    if (!__builtin_amdgcn_ballot_w64(a < 7)) break;
  }
  DRAIN(); LBAR();
  if (threadIdx.x < 2) ST4C(64u, W, fl, 0);
}

constexpr int LDS_BYTES = 160 * 1024;

__global__ void __launch_bounds__(256, 1) k_main(const Args A) {
  const int wave = RFL(threadIdx.x >> 6), lane = threadIdx.x & 63;
  const int vb = vblk(), x = vb & 3, i = vb >> 3, layer = (vb >> 2) & 1;
  const rsrc_t W = __builtin_amdgcn_make_buffer_rsrc(A.ws, 0, WS_USED, 0x00020000);
  asm volatile(
    "s_getpc_b64 s[92:93]\n"
    ".Lcode_here:\n"
    "s_and_b32 s93, s93, 0xffff\n"
    "s_mov_b32 s94, .Lcode_end-.Lcode_here\n"
    "s_mov_b32 s95, 0x20000\n"
    "s_mov_b32 m0, %1\n"
    "s_nop 0\n"
    "buffer_load_dwordx4 %0, s[92:95], 0 offen lds\n"
    :: "v"(i * 4096 + threadIdx.x * 16), "s"(LDS_BYTES - 1024) : "s92", "s93", "s94", "s95", "memory");
  unsigned ids = layer | wave << 1 | vb << 16, h0 = 0;
  if (layer) h0 = pre_conv(A, W, 32 * x + i, 4 * i + x, lane, wave);
  if (x == 3) {
    if (layer) pre_rest(A, W, i, lane, wave, h0); else h0 = pre_first(A, W, i, lane, wave);
    ids |= i << 8 | 64;
  } else {
    const int n = 32 * x + i, id = n / 3, par = n - 3 * id;
    ids |= id << 8;
    if (par) ids |= (par - 1) << 3 | (x == 2 ? 0 : x + 1) << 4 | 128;
  }
  asm volatile(
    "s_bitcmp1_b32 %1, 6\n"
    "s_cbranch_scc1 ROLE_O_%=\n"
    "s_bitcmp1_b32 %1, 7\n"
    "s_cbranch_scc0 ROLE_R_%=\n"
    "s_load_dwordx16 s[4:19], %0, 0\n"
    "s_bfe_u32 s24, %1, 0x10000\n"
    "s_bfe_u32 s25, %1, 0x10003\n"
    "s_bfe_u32 s26, %1, 0x80008\n"
    "s_bfe_u32 s27, %1, 0x20001\n"
    "s_bfe_u32 s28, %1, 0x80010\n"
    "s_bfe_u32 s89, %1, 0x20004\n"
    "s_lshl_b32 s89, s89, 14\n"
    "s_mov_b32 s47, 0x3e0293ee\n"
    "s_mov_b32 s91, 0\n"
    "v_mbcnt_lo_u32_b32 v0, -1, 0\n"
    "v_mbcnt_hi_u32_b32 v0, -1, v0\n"
    "s_lshl_b32 s66, s26, 8\n"
    "s_lshl_b32 s45, s27, 17\n"
    "s_add_u32 s66, s66, s45\n"
    "s_lshl_b32 s67, s27, 12\n"
    "v_lshrrev_b32 v4, 4, v0\n"
    "v_and_b32 v5, 15, v0\n"
    "v_lshlrev_b32 v6, 1, v4\n"
    "v_xor_b32 v5, v5, v6\n"
    "v_lshlrev_b32 v5, 4, v5\n"
    "v_lshl_add_u32 v22, v4, 13, v5\n"
    "v_xor_b32 v23, 128, v22\n"
    "v_bfe_u32 v5, v0, 2, 2\n"
    "v_lshl_add_u32 v6, v4, 3, v5\n"
    "v_and_b32 v4, 1, v4\n"
    "v_lshl_add_u32 v4, v4, 2, v5\n"
    "v_lshlrev_b32 v4, 5, v4\n"
    "v_and_b32 v5, 3, v0\n"
    "v_lshlrev_b32 v5, 3, v5\n"
    "v_mov_b32 v7, s27\n"
    "v_lshl_add_u32 v5, v7, 6, v5\n"
    "v_xor_b32 v4, v4, v5\n"
    "v_lshl_add_u32 v24, v6, 8, v4\n"
    "v_xor_b32 v25, 32, v24\n"
    "s_waitcnt lgkmcnt(0)\n"
    "s_mov_b32 s20, s12\n"
    "s_and_b32 s21, s13, 0xffff\n"
    "s_mov_b32 s22, 85983232\n"
    "s_mov_b32 s23, 0x20000\n"
    "s_and_b32 s31, s28, 7\n"
    "s_lshl_b32 s31, s31, 7\n"
    "s_add_u32 s31, s31, 81788928\n"
    "buffer_load_dwordx2 v[20:21], off, s[20:23], s31 sc1\n"
    "s_mov_b64 s[76:77], s[6:7]\n"
    "s_and_b32 s77, s77, 0xffff\n"
    "s_mov_b32 s78, 4194304\n"
    "s_mov_b32 s79, 0x20000\n"
    "s_add_u32 m0, s67, 0\n"
    "s_add_u32 s80, s66, 0\n"
    "buffer_load_dwordx4 v22, s[76:79], s80 offen offset:0  lds\n"
    "s_add_u32 s80, s66, 31744\n"
    "buffer_load_dwordx4 v22, s[76:79], s80 offen offset:1024  lds\n"
    "s_add_u32 s80, s66, 63488\n"
    "buffer_load_dwordx4 v23, s[76:79], s80 offen offset:2048  lds\n"
    "s_add_u32 s80, s66, 95232\n"
    "buffer_load_dwordx4 v23, s[76:79], s80 offen offset:3072  lds\n"
    "s_add_u32 m0, s67, 16384\n"
    "s_add_u32 s80, s66, 524288\n"
    "buffer_load_dwordx4 v22, s[76:79], s80 offen offset:0  lds\n"
    "s_add_u32 s80, s66, 556032\n"
    "buffer_load_dwordx4 v22, s[76:79], s80 offen offset:1024  lds\n"
    "s_add_u32 s80, s66, 587776\n"
    "buffer_load_dwordx4 v23, s[76:79], s80 offen offset:2048  lds\n"
    "s_add_u32 s80, s66, 619520\n"
    "buffer_load_dwordx4 v23, s[76:79], s80 offen offset:3072  lds\n"
    "s_add_u32 m0, s67, 32768\n"
    "s_add_u32 s80, s66, 1048576\n"
    "buffer_load_dwordx4 v22, s[76:79], s80 offen offset:0  lds\n"
    "s_add_u32 s80, s66, 1080320\n"
    "buffer_load_dwordx4 v22, s[76:79], s80 offen offset:1024  lds\n"
    "s_add_u32 s80, s66, 1112064\n"
    "buffer_load_dwordx4 v23, s[76:79], s80 offen offset:2048  lds\n"
    "s_add_u32 s80, s66, 1143808\n"
    "buffer_load_dwordx4 v23, s[76:79], s80 offen offset:3072  lds\n"
    "s_add_u32 m0, s67, 49152\n"
    "s_add_u32 s80, s66, 1572864\n"
    "buffer_load_dwordx4 v22, s[76:79], s80 offen offset:0  lds\n"
    "s_add_u32 s80, s66, 1604608\n"
    "buffer_load_dwordx4 v22, s[76:79], s80 offen offset:1024  lds\n"
    "s_add_u32 s80, s66, 1636352\n"
    "buffer_load_dwordx4 v23, s[76:79], s80 offen offset:2048  lds\n"
    "s_add_u32 s80, s66, 1668096\n"
    "buffer_load_dwordx4 v23, s[76:79], s80 offen offset:3072  lds\n"
    "s_add_u32 m0, s67, 65536\n"
    "s_add_u32 s80, s66, 2097152\n"
    "buffer_load_dwordx4 v22, s[76:79], s80 offen offset:0  lds\n"
    "s_add_u32 s80, s66, 2128896\n"
    "buffer_load_dwordx4 v22, s[76:79], s80 offen offset:1024  lds\n"
    "s_add_u32 s80, s66, 2160640\n"
    "buffer_load_dwordx4 v23, s[76:79], s80 offen offset:2048  lds\n"
    "s_add_u32 s80, s66, 2192384\n"
    "buffer_load_dwordx4 v23, s[76:79], s80 offen offset:3072  lds\n"
    "s_add_u32 m0, s67, 81920\n"
    "s_add_u32 s80, s66, 2621440\n"
    "buffer_load_dwordx4 v22, s[76:79], s80 offen offset:0  lds\n"
    "s_add_u32 s80, s66, 2653184\n"
    "buffer_load_dwordx4 v22, s[76:79], s80 offen offset:1024  lds\n"
    "s_add_u32 s80, s66, 2684928\n"
    "buffer_load_dwordx4 v23, s[76:79], s80 offen offset:2048  lds\n"
    "s_add_u32 s80, s66, 2716672\n"
    "buffer_load_dwordx4 v23, s[76:79], s80 offen offset:3072  lds\n"
    "s_add_u32 m0, s67, 98304\n"
    "s_add_u32 s80, s66, 3145728\n"
    "buffer_load_dwordx4 v22, s[76:79], s80 offen offset:0  lds\n"
    "s_add_u32 s80, s66, 3177472\n"
    "buffer_load_dwordx4 v22, s[76:79], s80 offen offset:1024  lds\n"
    "s_add_u32 s80, s66, 3209216\n"
    "buffer_load_dwordx4 v23, s[76:79], s80 offen offset:2048  lds\n"
    "s_add_u32 s80, s66, 3240960\n"
    "buffer_load_dwordx4 v23, s[76:79], s80 offen offset:3072  lds\n"
    "s_add_u32 m0, s67, 114688\n"
    "s_add_u32 s80, s66, 3670016\n"
    "buffer_load_dwordx4 v22, s[76:79], s80 offen offset:0  lds\n"
    "s_add_u32 s80, s66, 3701760\n"
    "buffer_load_dwordx4 v22, s[76:79], s80 offen offset:1024  lds\n"
    "s_add_u32 s80, s66, 3733504\n"
    "buffer_load_dwordx4 v23, s[76:79], s80 offen offset:2048  lds\n"
    "s_add_u32 s80, s66, 3765248\n"
    "buffer_load_dwordx4 v23, s[76:79], s80 offen offset:3072  lds\n"
    "v_lshlrev_b32 v101, 4, v0\n"
    "v_lshlrev_b32 v111, 2, v0\n"
    "s_lshr_b32 s68, s27, 1\n"
    "s_and_b32 s69, s27, 1\n"
    "s_lshl_b32 s35, s26, 1\n"
    "s_add_u32 s35, s35, s68\n"
    "s_lshl_b32 s35, s35, 14\n"
    "s_lshl_b32 s46, s69, 12\n"
    "s_add_u32 s35, s35, s46\n"
    "s_add_u32 s35, s35, 16777216\n"
    "s_mov_b32 s44, 2105344\n"
    "s_andn2_b32 s40, s25, s24\n"
    "s_lshl_b32 s40, s40, 12\n"
    "s_add_u32 s40, s40, s44\n"
    "s_add_u32 s40, s40, 1196032\n"
    "s_add_u32 s89, s89, s44\n"
    "s_add_u32 s89, s89, 1212416\n"
    "s_lshl_b32 s46, s25, 7\n"
    "s_add_u32 s89, s89, s46\n"
    "s_lshr_b32 s46, s26, 3\n"
    "s_lshl_b32 s46, s46, 14\n"
    "s_add_u32 s44, s44, s46\n"
    "s_and_b32 s46, s26, 7\n"
    "s_lshl_b32 s46, s46, 1\n"
    "s_add_u32 s46, s46, s25\n"
    "s_lshl_b32 s46, s46, 2\n"
    "s_add_u32 s46, s46, s27\n"
    "s_lshl_b32 s46, s46, 7\n"
    "s_add_u32 s44, s44, s46\n"
    "s_add_u32 s38, s44, 1048576\n"
    "s_add_u32 s39, s44, 1114112\n"
    "s_lshl_b32 s34, s25, 2\n"
    "s_add_u32 s34, s34, s27\n"
    "s_lshl_b32 s34, s34, 14\n"
    "s_add_u32 s34, s34, 4194304\n"
    "s_lshl_b32 s36, s25, 14\n"
    "s_lshl_b32 s44, s27, 11\n"
    "s_add_u32 s36, s36, s44\n"
    "s_add_u32 s36, s36, 0\n"
    "v_add_u32 v124, s44, v101\n"
    "v_add_u32 v124, 107520, v124\n"
    "s_lshl_b32 s37, s25, 5\n"
    "s_add_u32 s37, s37, s26\n"
    "s_lshl_b32 s37, s37, 14\n"
    "s_lshl_b32 s44, s68, 12\n"
    "s_add_u32 s37, s37, s44\n"
    "s_lshl_b32 s44, s69, 10\n"
    "s_add_u32 s37, s37, s44\n"
    "s_add_u32 s37, s37, 25165824\n"
    "s_mul_i32 s43, s27, 8448\n"
    "s_add_u32 s32, s25, 1\n"
    "s_mov_b32 s33, s32\n"
    "s_mov_b32 s48, s32\n"
    "s_mov_b32 s29, 0\n"
    "s_mov_b32 s93, 0x7f000000\n"
    "s_mov_b32 s41, 33792\n"
    "s_mov_b32 s42, 33792\n"
    "s_mul_i32 s49, s28, 2176\n"
    "s_add_u32 s49, s49, 84934656\n"
    "s_cmp_eq_u32 s27, 0\n"
    "s_cselect_b32 s49, s49, 0x7f000000\n"
    "v_and_b32 v1, 15, v0\n"
    "v_lshrrev_b32 v2, 4, v0\n"
    "v_and_b32 v3, 7, v0\n"
    "v_bfe_u32 v4, v0, 3, 1\n"
    "v_mov_b32 v5, 0x4444\n"
    "v_mov_b32 v6, 0xeeee\n"
    "v_cmp_eq_u32 vcc, 1, v4\n"
    "s_nop 1\n"
    "v_cndmask_b32 v100, v5, v6, vcc\n"
    "v_mul_u32_u24 v5, 1056, v1\n"
    "v_lshl_add_u32 v102, v2, 4, v5\n"
    "v_and_b32 v5, 31, v0\n"
    "v_lshl_add_u32 v6, s27, 3, v3\n"
    "s_cmp_eq_u32 s24, 9\n"
    "s_cselect_b64 vcc, -1, 0\n"
    "v_cndmask_b32 v5, v5, v6, vcc\n"
    "v_lshlrev_b32 v107, 7, v5\n"
    "v_lshl_add_u32 v5, v2, 3, v3\n"
    "v_lshlrev_b32 v5, 4, v5\n"
    "v_add_u32 v106, 119808, v5\n"
    "v_lshl_add_u32 v5, v4, 3, v106\n"
    "s_lshl_b32 s44, s27, 9\n"
    "v_add_u32 v105, s44, v5\n"
    "v_mov_b32 v110, 0xd028965f\n"
    "v_and_b32 v7, 1, v2\n"
    "v_bfe_u32 v9, v0, 2, 1\n"
    "v_and_b32 v10, 1, v0\n"
    "v_lshlrev_b32 v10, 4, v10\n"
    "v_mov_b32 v11, 0x3f80\n"
    "v_lshlrev_b32 v11, v10, v11\n"
    "v_cmp_eq_u32 vcc, v9, v7\n"
    "s_nop 1\n"
    "v_cndmask_b32 v12, 0, v11, vcc\n"
    "v_bfe_u32 v13, v0, 1, 1\n"
    "v_lshl_add_u32 v13, v4, 1, v13\n"
    "v_cmp_eq_u32 vcc, 0, v13\n"
    "s_nop 1\n"
    "v_cndmask_b32 v116, 0, v12, vcc\n"
    "v_mov_b32 v120, 0x3f803f80\n"
    "v_cmp_eq_u32 vcc, 1, v13\n"
    "s_nop 1\n"
    "v_cndmask_b32 v117, 0, v12, vcc\n"
    "v_mov_b32 v121, 0x3f803f80\n"
    "v_cmp_eq_u32 vcc, 2, v13\n"
    "s_nop 1\n"
    "v_cndmask_b32 v118, 0, v12, vcc\n"
    "v_mov_b32 v122, 0x3f803f80\n"
    "v_cmp_eq_u32 vcc, 3, v13\n"
    "s_nop 1\n"
    "v_cndmask_b32 v119, 0, v12, vcc\n"
    "v_mov_b32 v123, 0x3f803f80\n"
    "v_mov_b32 v114, 0x3f003f00\n"
    "v_mov_b32 v115, 0x3f003f00\n"
    "s_lshl_b32 s44, s69, 4\n"
    "v_add_u32 v5, s44, v1\n"
    "v_lshlrev_b32 v6, 2, v2\n"
    "v_sub_u32 v5, v5, v6\n"
    "v_cmp_le_i32 s[50:51], 0, v5\n"
    "v_cmp_le_i32 s[52:53], 1, v5\n"
    "v_cmp_le_i32 s[54:55], 2, v5\n"
    "v_cmp_le_i32 s[56:57], 3, v5\n"
    "v_cmp_le_i32 s[58:59], 16, v5\n"
    "v_cmp_le_i32 s[60:61], 17, v5\n"
    "v_cmp_le_i32 s[62:63], 18, v5\n"
    "v_cmp_le_i32 s[64:65], 19, v5\n"
    "s_lshl_b32 s70, s69, 10\n"
    "s_mul_i32 s71, s68, 12288\n"
    "s_waitcnt vmcnt(28) lgkmcnt(0)\n"
    "s_barrier\n"
    "ds_read_b64_tr_b16 a[0:1], v24 offset:0\n"
    "ds_read_b64_tr_b16 a[64:65], v25 offset:0\n"
    "ds_read_b64_tr_b16 a[2:3], v24 offset:1024\n"
    "ds_read_b64_tr_b16 a[66:67], v25 offset:1024\n"
    "ds_read_b64_tr_b16 a[4:5], v24 offset:8192\n"
    "ds_read_b64_tr_b16 a[68:69], v25 offset:8192\n"
    "ds_read_b64_tr_b16 a[6:7], v24 offset:9216\n"
    "ds_read_b64_tr_b16 a[70:71], v25 offset:9216\n"
    "s_waitcnt vmcnt(24) lgkmcnt(0)\n"
    "s_barrier\n"
    "s_mov_b64 s[76:77], s[8:9]\n"
    "s_and_b32 s77, s77, 0xffff\n"
    "s_mov_b32 s78, 4194304\n"
    "s_mov_b32 s79, 0x20000\n"
    "s_add_u32 m0, s67, 0\n"
    "s_add_u32 s80, s66, 0\n"
    "buffer_load_dwordx4 v22, s[76:79], s80 offen offset:0  lds\n"
    "s_add_u32 s80, s66, 31744\n"
    "buffer_load_dwordx4 v22, s[76:79], s80 offen offset:1024  lds\n"
    "s_add_u32 s80, s66, 63488\n"
    "buffer_load_dwordx4 v23, s[76:79], s80 offen offset:2048  lds\n"
    "s_add_u32 s80, s66, 95232\n"
    "buffer_load_dwordx4 v23, s[76:79], s80 offen offset:3072  lds\n"
    "ds_read_b64_tr_b16 a[8:9], v24 offset:16384\n"
    "ds_read_b64_tr_b16 a[72:73], v25 offset:16384\n"
    "ds_read_b64_tr_b16 a[10:11], v24 offset:17408\n"
    "ds_read_b64_tr_b16 a[74:75], v25 offset:17408\n"
    "ds_read_b64_tr_b16 a[12:13], v24 offset:24576\n"
    "ds_read_b64_tr_b16 a[76:77], v25 offset:24576\n"
    "ds_read_b64_tr_b16 a[14:15], v24 offset:25600\n"
    "ds_read_b64_tr_b16 a[78:79], v25 offset:25600\n"
    "s_waitcnt vmcnt(24) lgkmcnt(0)\n"
    "s_barrier\n"
    "s_add_u32 m0, s67, 16384\n"
    "s_add_u32 s80, s66, 524288\n"
    "buffer_load_dwordx4 v22, s[76:79], s80 offen offset:0  lds\n"
    "s_add_u32 s80, s66, 556032\n"
    "buffer_load_dwordx4 v22, s[76:79], s80 offen offset:1024  lds\n"
    "s_add_u32 s80, s66, 587776\n"
    "buffer_load_dwordx4 v23, s[76:79], s80 offen offset:2048  lds\n"
    "s_add_u32 s80, s66, 619520\n"
    "buffer_load_dwordx4 v23, s[76:79], s80 offen offset:3072  lds\n"
    "ds_read_b64_tr_b16 a[16:17], v24 offset:32768\n"
    "ds_read_b64_tr_b16 a[80:81], v25 offset:32768\n"
    "ds_read_b64_tr_b16 a[18:19], v24 offset:33792\n"
    "ds_read_b64_tr_b16 a[82:83], v25 offset:33792\n"
    "ds_read_b64_tr_b16 a[20:21], v24 offset:40960\n"
    "ds_read_b64_tr_b16 a[84:85], v25 offset:40960\n"
    "ds_read_b64_tr_b16 a[22:23], v24 offset:41984\n"
    "ds_read_b64_tr_b16 a[86:87], v25 offset:41984\n"
    "s_waitcnt vmcnt(24) lgkmcnt(0)\n"
    "s_barrier\n"
    "s_add_u32 m0, s67, 32768\n"
    "s_add_u32 s80, s66, 1048576\n"
    "buffer_load_dwordx4 v22, s[76:79], s80 offen offset:0  lds\n"
    "s_add_u32 s80, s66, 1080320\n"
    "buffer_load_dwordx4 v22, s[76:79], s80 offen offset:1024  lds\n"
    "s_add_u32 s80, s66, 1112064\n"
    "buffer_load_dwordx4 v23, s[76:79], s80 offen offset:2048  lds\n"
    "s_add_u32 s80, s66, 1143808\n"
    "buffer_load_dwordx4 v23, s[76:79], s80 offen offset:3072  lds\n"
    "ds_read_b64_tr_b16 a[24:25], v24 offset:49152\n"
    "ds_read_b64_tr_b16 a[88:89], v25 offset:49152\n"
    "ds_read_b64_tr_b16 a[26:27], v24 offset:50176\n"
    "ds_read_b64_tr_b16 a[90:91], v25 offset:50176\n"
    "ds_read_b64_tr_b16 a[28:29], v24 offset:57344\n"
    "ds_read_b64_tr_b16 a[92:93], v25 offset:57344\n"
    "ds_read_b64_tr_b16 a[30:31], v24 offset:58368\n"
    "ds_read_b64_tr_b16 a[94:95], v25 offset:58368\n"
    "s_waitcnt vmcnt(24) lgkmcnt(0)\n"
    "s_barrier\n"
    "s_add_u32 m0, s67, 49152\n"
    "s_add_u32 s80, s66, 1572864\n"
    "buffer_load_dwordx4 v22, s[76:79], s80 offen offset:0  lds\n"
    "s_add_u32 s80, s66, 1604608\n"
    "buffer_load_dwordx4 v22, s[76:79], s80 offen offset:1024  lds\n"
    "s_add_u32 s80, s66, 1636352\n"
    "buffer_load_dwordx4 v23, s[76:79], s80 offen offset:2048  lds\n"
    "s_add_u32 s80, s66, 1668096\n"
    "buffer_load_dwordx4 v23, s[76:79], s80 offen offset:3072  lds\n"
    "v_add_u32 v24, 65536, v24\n"
    "v_add_u32 v25, 65536, v25\n"
    "ds_read_b64_tr_b16 a[32:33], v24 offset:0\n"
    "ds_read_b64_tr_b16 a[96:97], v25 offset:0\n"
    "ds_read_b64_tr_b16 a[34:35], v24 offset:1024\n"
    "ds_read_b64_tr_b16 a[98:99], v25 offset:1024\n"
    "ds_read_b64_tr_b16 a[36:37], v24 offset:8192\n"
    "ds_read_b64_tr_b16 a[100:101], v25 offset:8192\n"
    "ds_read_b64_tr_b16 a[38:39], v24 offset:9216\n"
    "ds_read_b64_tr_b16 a[102:103], v25 offset:9216\n"
    "s_waitcnt vmcnt(24) lgkmcnt(0)\n"
    "s_barrier\n"
    "s_add_u32 m0, s67, 65536\n"
    "s_add_u32 s80, s66, 2097152\n"
    "buffer_load_dwordx4 v22, s[76:79], s80 offen offset:0  lds\n"
    "s_add_u32 s80, s66, 2128896\n"
    "buffer_load_dwordx4 v22, s[76:79], s80 offen offset:1024  lds\n"
    "s_add_u32 s80, s66, 2160640\n"
    "buffer_load_dwordx4 v23, s[76:79], s80 offen offset:2048  lds\n"
    "s_add_u32 s80, s66, 2192384\n"
    "buffer_load_dwordx4 v23, s[76:79], s80 offen offset:3072  lds\n"
    "ds_read_b64_tr_b16 a[40:41], v24 offset:16384\n"
    "ds_read_b64_tr_b16 a[104:105], v25 offset:16384\n"
    "ds_read_b64_tr_b16 a[42:43], v24 offset:17408\n"
    "ds_read_b64_tr_b16 a[106:107], v25 offset:17408\n"
    "ds_read_b64_tr_b16 a[44:45], v24 offset:24576\n"
    "ds_read_b64_tr_b16 a[108:109], v25 offset:24576\n"
    "ds_read_b64_tr_b16 a[46:47], v24 offset:25600\n"
    "ds_read_b64_tr_b16 a[110:111], v25 offset:25600\n"
    "s_waitcnt vmcnt(24) lgkmcnt(0)\n"
    "s_barrier\n"
    "s_add_u32 m0, s67, 81920\n"
    "s_add_u32 s80, s66, 2621440\n"
    "buffer_load_dwordx4 v22, s[76:79], s80 offen offset:0  lds\n"
    "s_add_u32 s80, s66, 2653184\n"
    "buffer_load_dwordx4 v22, s[76:79], s80 offen offset:1024  lds\n"
    "s_add_u32 s80, s66, 2684928\n"
    "buffer_load_dwordx4 v23, s[76:79], s80 offen offset:2048  lds\n"
    "s_add_u32 s80, s66, 2716672\n"
    "buffer_load_dwordx4 v23, s[76:79], s80 offen offset:3072  lds\n"
    "buffer_load_dwordx2 v[26:27], off, s[20:23], s31 sc1\n"
    "ds_read_b64_tr_b16 a[48:49], v24 offset:32768\n"
    "ds_read_b64_tr_b16 a[112:113], v25 offset:32768\n"
    "ds_read_b64_tr_b16 a[50:51], v24 offset:33792\n"
    "ds_read_b64_tr_b16 a[114:115], v25 offset:33792\n"
    "ds_read_b64_tr_b16 a[52:53], v24 offset:40960\n"
    "ds_read_b64_tr_b16 a[116:117], v25 offset:40960\n"
    "ds_read_b64_tr_b16 a[54:55], v24 offset:41984\n"
    "ds_read_b64_tr_b16 a[118:119], v25 offset:41984\n"
    "s_waitcnt vmcnt(25) lgkmcnt(0)\n"
    "s_barrier\n"
    "s_add_u32 m0, s67, 98304\n"
    "s_add_u32 s80, s66, 3145728\n"
    "buffer_load_dwordx4 v22, s[76:79], s80 offen offset:0  lds\n"
    "s_add_u32 s80, s66, 3177472\n"
    "buffer_load_dwordx4 v22, s[76:79], s80 offen offset:1024  lds\n"
    "s_add_u32 s80, s66, 3209216\n"
    "buffer_load_dwordx4 v23, s[76:79], s80 offen offset:2048  lds\n"
    "s_add_u32 s80, s66, 3240960\n"
    "buffer_load_dwordx4 v23, s[76:79], s80 offen offset:3072  lds\n"
    "ds_read_b64_tr_b16 a[56:57], v24 offset:49152\n"
    "ds_read_b64_tr_b16 a[120:121], v25 offset:49152\n"
    "ds_read_b64_tr_b16 a[58:59], v24 offset:50176\n"
    "ds_read_b64_tr_b16 a[122:123], v25 offset:50176\n"
    "ds_read_b64_tr_b16 a[60:61], v24 offset:57344\n"
    "ds_read_b64_tr_b16 a[124:125], v25 offset:57344\n"
    "ds_read_b64_tr_b16 a[62:63], v24 offset:58368\n"
    "ds_read_b64_tr_b16 a[126:127], v25 offset:58368\n"
    "s_waitcnt vmcnt(25) lgkmcnt(0)\n"
    "s_barrier\n"
    "s_add_u32 m0, s67, 114688\n"
    "s_add_u32 s80, s66, 3670016\n"
    "buffer_load_dwordx4 v22, s[76:79], s80 offen offset:0  lds\n"
    "s_add_u32 s80, s66, 3701760\n"
    "buffer_load_dwordx4 v22, s[76:79], s80 offen offset:1024  lds\n"
    "s_add_u32 s80, s66, 3733504\n"
    "buffer_load_dwordx4 v23, s[76:79], s80 offen offset:2048  lds\n"
    "s_add_u32 s80, s66, 3765248\n"
    "buffer_load_dwordx4 v23, s[76:79], s80 offen offset:3072  lds\n"
    "v_add_u32 v24, 4294901760, v24\n"
    "v_add_u32 v25, 4294901760, v25\n"
    "ds_read_b64_tr_b16 a[128:129], v24 offset:0\n"
    "ds_read_b64_tr_b16 a[192:193], v25 offset:0\n"
    "ds_read_b64_tr_b16 a[130:131], v24 offset:1024\n"
    "ds_read_b64_tr_b16 a[194:195], v25 offset:1024\n"
    "ds_read_b64_tr_b16 a[132:133], v24 offset:8192\n"
    "ds_read_b64_tr_b16 a[196:197], v25 offset:8192\n"
    "ds_read_b64_tr_b16 a[134:135], v24 offset:9216\n"
    "ds_read_b64_tr_b16 a[198:199], v25 offset:9216\n"
    "s_waitcnt vmcnt(25) lgkmcnt(0)\n"
    "s_barrier\n"
    "s_mov_b64 s[76:77], s[4:5]\n"
    "s_and_b32 s77, s77, 0xffff\n"
    "s_mov_b32 s78, 4194304\n"
    "s_mov_b32 s79, 0x20000\n"
    "s_add_u32 m0, s67, 0\n"
    "s_add_u32 s80, s66, 0\n"
    "buffer_load_dwordx4 v22, s[76:79], s80 offen offset:0  lds\n"
    "s_add_u32 s80, s66, 31744\n"
    "buffer_load_dwordx4 v22, s[76:79], s80 offen offset:1024  lds\n"
    "s_add_u32 s80, s66, 63488\n"
    "buffer_load_dwordx4 v23, s[76:79], s80 offen offset:2048  lds\n"
    "s_add_u32 s80, s66, 95232\n"
    "buffer_load_dwordx4 v23, s[76:79], s80 offen offset:3072  lds\n"
    "ds_read_b64_tr_b16 a[136:137], v24 offset:16384\n"
    "ds_read_b64_tr_b16 a[200:201], v25 offset:16384\n"
    "ds_read_b64_tr_b16 a[138:139], v24 offset:17408\n"
    "ds_read_b64_tr_b16 a[202:203], v25 offset:17408\n"
    "ds_read_b64_tr_b16 a[140:141], v24 offset:24576\n"
    "ds_read_b64_tr_b16 a[204:205], v25 offset:24576\n"
    "ds_read_b64_tr_b16 a[142:143], v24 offset:25600\n"
    "ds_read_b64_tr_b16 a[206:207], v25 offset:25600\n"
    "s_waitcnt vmcnt(25) lgkmcnt(0)\n"
    "s_barrier\n"
    "s_add_u32 m0, s67, 16384\n"
    "s_add_u32 s80, s66, 524288\n"
    "buffer_load_dwordx4 v22, s[76:79], s80 offen offset:0  lds\n"
    "s_add_u32 s80, s66, 556032\n"
    "buffer_load_dwordx4 v22, s[76:79], s80 offen offset:1024  lds\n"
    "s_add_u32 s80, s66, 587776\n"
    "buffer_load_dwordx4 v23, s[76:79], s80 offen offset:2048  lds\n"
    "s_add_u32 s80, s66, 619520\n"
    "buffer_load_dwordx4 v23, s[76:79], s80 offen offset:3072  lds\n"
    "ds_read_b64_tr_b16 a[144:145], v24 offset:32768\n"
    "ds_read_b64_tr_b16 a[208:209], v25 offset:32768\n"
    "ds_read_b64_tr_b16 a[146:147], v24 offset:33792\n"
    "ds_read_b64_tr_b16 a[210:211], v25 offset:33792\n"
    "ds_read_b64_tr_b16 a[148:149], v24 offset:40960\n"
    "ds_read_b64_tr_b16 a[212:213], v25 offset:40960\n"
    "ds_read_b64_tr_b16 a[150:151], v24 offset:41984\n"
    "ds_read_b64_tr_b16 a[214:215], v25 offset:41984\n"
    "s_waitcnt vmcnt(25) lgkmcnt(0)\n"
    "s_barrier\n"
    "s_add_u32 m0, s67, 32768\n"
    "s_add_u32 s80, s66, 1048576\n"
    "buffer_load_dwordx4 v22, s[76:79], s80 offen offset:0  lds\n"
    "s_add_u32 s80, s66, 1080320\n"
    "buffer_load_dwordx4 v22, s[76:79], s80 offen offset:1024  lds\n"
    "s_add_u32 s80, s66, 1112064\n"
    "buffer_load_dwordx4 v23, s[76:79], s80 offen offset:2048  lds\n"
    "s_add_u32 s80, s66, 1143808\n"
    "buffer_load_dwordx4 v23, s[76:79], s80 offen offset:3072  lds\n"
    "ds_read_b64_tr_b16 a[152:153], v24 offset:49152\n"
    "ds_read_b64_tr_b16 a[216:217], v25 offset:49152\n"
    "ds_read_b64_tr_b16 a[154:155], v24 offset:50176\n"
    "ds_read_b64_tr_b16 a[218:219], v25 offset:50176\n"
    "ds_read_b64_tr_b16 a[156:157], v24 offset:57344\n"
    "ds_read_b64_tr_b16 a[220:221], v25 offset:57344\n"
    "ds_read_b64_tr_b16 a[158:159], v24 offset:58368\n"
    "ds_read_b64_tr_b16 a[222:223], v25 offset:58368\n"
    "s_waitcnt vmcnt(25) lgkmcnt(0)\n"
    "s_barrier\n"
    "s_add_u32 m0, s67, 49152\n"
    "s_add_u32 s80, s66, 1572864\n"
    "buffer_load_dwordx4 v22, s[76:79], s80 offen offset:0  lds\n"
    "s_add_u32 s80, s66, 1604608\n"
    "buffer_load_dwordx4 v22, s[76:79], s80 offen offset:1024  lds\n"
    "s_add_u32 s80, s66, 1636352\n"
    "buffer_load_dwordx4 v23, s[76:79], s80 offen offset:2048  lds\n"
    "s_add_u32 s80, s66, 1668096\n"
    "buffer_load_dwordx4 v23, s[76:79], s80 offen offset:3072  lds\n"
    "v_add_u32 v24, 65536, v24\n"
    "v_add_u32 v25, 65536, v25\n"
    "ds_read_b64_tr_b16 a[160:161], v24 offset:0\n"
    "ds_read_b64_tr_b16 a[224:225], v25 offset:0\n"
    "ds_read_b64_tr_b16 a[162:163], v24 offset:1024\n"
    "ds_read_b64_tr_b16 a[226:227], v25 offset:1024\n"
    "ds_read_b64_tr_b16 a[164:165], v24 offset:8192\n"
    "ds_read_b64_tr_b16 a[228:229], v25 offset:8192\n"
    "ds_read_b64_tr_b16 a[166:167], v24 offset:9216\n"
    "ds_read_b64_tr_b16 a[230:231], v25 offset:9216\n"
    "s_waitcnt vmcnt(25) lgkmcnt(0)\n"
    "s_barrier\n"
    "s_add_u32 m0, s67, 65536\n"
    "s_add_u32 s80, s66, 2097152\n"
    "buffer_load_dwordx4 v22, s[76:79], s80 offen offset:0  lds\n"
    "s_add_u32 s80, s66, 2128896\n"
    "buffer_load_dwordx4 v22, s[76:79], s80 offen offset:1024  lds\n"
    "s_add_u32 s80, s66, 2160640\n"
    "buffer_load_dwordx4 v23, s[76:79], s80 offen offset:2048  lds\n"
    "s_add_u32 s80, s66, 2192384\n"
    "buffer_load_dwordx4 v23, s[76:79], s80 offen offset:3072  lds\n"
    "ds_read_b64_tr_b16 a[168:169], v24 offset:16384\n"
    "ds_read_b64_tr_b16 a[232:233], v25 offset:16384\n"
    "ds_read_b64_tr_b16 a[170:171], v24 offset:17408\n"
    "ds_read_b64_tr_b16 a[234:235], v25 offset:17408\n"
    "ds_read_b64_tr_b16 a[172:173], v24 offset:24576\n"
    "ds_read_b64_tr_b16 a[236:237], v25 offset:24576\n"
    "ds_read_b64_tr_b16 a[174:175], v24 offset:25600\n"
    "ds_read_b64_tr_b16 a[238:239], v25 offset:25600\n"
    "s_waitcnt vmcnt(24) lgkmcnt(0)\n"
    "s_barrier\n"
    "s_add_u32 m0, s67, 81920\n"
    "s_add_u32 s80, s66, 2621440\n"
    "buffer_load_dwordx4 v22, s[76:79], s80 offen offset:0  lds\n"
    "s_add_u32 s80, s66, 2653184\n"
    "buffer_load_dwordx4 v22, s[76:79], s80 offen offset:1024  lds\n"
    "s_add_u32 s80, s66, 2684928\n"
    "buffer_load_dwordx4 v23, s[76:79], s80 offen offset:2048  lds\n"
    "s_add_u32 s80, s66, 2716672\n"
    "buffer_load_dwordx4 v23, s[76:79], s80 offen offset:3072  lds\n"
    "v_readfirstlane_b32 s45, v21\n"
    "v_readfirstlane_b32 s92, v27\n"
    "s_cmp_lg_u32 s24, 0\n"
    "s_cselect_b32 s45, %2, s45\n"
    "s_cmp_lg_u32 s45, s92\n"
    "s_cselect_b32 s92, -1, 0\n"
    "s_lshl_b32 s45, s28, 7\n"
    "s_add_u32 s45, s45, 81793024\n"
    "s_cmp_lg_u32 s24, 0\n"
    "s_cselect_b32 s45, 0x7f000000, s45\n"
    "s_cmp_eq_u32 s92, 0\n"
    "s_cselect_b32 s45, 0x7f000000, s45\n"
    "v_mov_b32 v28, 1\n"
    "s_mov_b64 exec, 1\n"
    "buffer_store_dword v28, off, s[20:23], s45 sc1\n"
    "s_mov_b64 exec, -1\n"
    "ds_read_b64_tr_b16 a[176:177], v24 offset:32768\n"
    "ds_read_b64_tr_b16 a[240:241], v25 offset:32768\n"
    "ds_read_b64_tr_b16 a[178:179], v24 offset:33792\n"
    "ds_read_b64_tr_b16 a[242:243], v25 offset:33792\n"
    "ds_read_b64_tr_b16 a[180:181], v24 offset:40960\n"
    "ds_read_b64_tr_b16 a[244:245], v25 offset:40960\n"
    "ds_read_b64_tr_b16 a[182:183], v24 offset:41984\n"
    "ds_read_b64_tr_b16 a[246:247], v25 offset:41984\n"
    "s_waitcnt vmcnt(25) lgkmcnt(0)\n"
    "s_barrier\n"
    "s_add_u32 m0, s67, 98304\n"
    "s_add_u32 s80, s66, 3145728\n"
    "buffer_load_dwordx4 v22, s[76:79], s80 offen offset:0  lds\n"
    "s_add_u32 s80, s66, 3177472\n"
    "buffer_load_dwordx4 v22, s[76:79], s80 offen offset:1024  lds\n"
    "s_add_u32 s80, s66, 3209216\n"
    "buffer_load_dwordx4 v23, s[76:79], s80 offen offset:2048  lds\n"
    "s_add_u32 s80, s66, 3240960\n"
    "buffer_load_dwordx4 v23, s[76:79], s80 offen offset:3072  lds\n"
    "ds_read_b64_tr_b16 a[184:185], v24 offset:49152\n"
    "ds_read_b64_tr_b16 a[248:249], v25 offset:49152\n"
    "ds_read_b64_tr_b16 a[186:187], v24 offset:50176\n"
    "ds_read_b64_tr_b16 a[250:251], v25 offset:50176\n"
    "ds_read_b64_tr_b16 a[188:189], v24 offset:57344\n"
    "ds_read_b64_tr_b16 a[252:253], v25 offset:57344\n"
    "ds_read_b64_tr_b16 a[190:191], v24 offset:58368\n"
    "ds_read_b64_tr_b16 a[254:255], v25 offset:58368\n"
    "s_waitcnt vmcnt(25) lgkmcnt(0)\n"
    "s_barrier\n"
    "s_add_u32 m0, s67, 114688\n"
    "s_add_u32 s80, s66, 3670016\n"
    "buffer_load_dwordx4 v22, s[76:79], s80 offen offset:0  lds\n"
    "s_add_u32 s80, s66, 3701760\n"
    "buffer_load_dwordx4 v22, s[76:79], s80 offen offset:1024  lds\n"
    "s_add_u32 s80, s66, 3733504\n"
    "buffer_load_dwordx4 v23, s[76:79], s80 offen offset:2048  lds\n"
    "s_add_u32 s80, s66, 3765248\n"
    "buffer_load_dwordx4 v23, s[76:79], s80 offen offset:3072  lds\n"
    "v_add_u32 v24, 4294901760, v24\n"
    "v_add_u32 v25, 4294901760, v25\n"
    "ds_read_b64_tr_b16 v[128:129], v24 offset:0\n"
    "ds_read_b64_tr_b16 v[192:193], v25 offset:0\n"
    "ds_read_b64_tr_b16 v[130:131], v24 offset:1024\n"
    "ds_read_b64_tr_b16 v[194:195], v25 offset:1024\n"
    "ds_read_b64_tr_b16 v[132:133], v24 offset:8192\n"
    "ds_read_b64_tr_b16 v[196:197], v25 offset:8192\n"
    "ds_read_b64_tr_b16 v[134:135], v24 offset:9216\n"
    "ds_read_b64_tr_b16 v[198:199], v25 offset:9216\n"
    "s_waitcnt vmcnt(25) lgkmcnt(0)\n"
    "s_barrier\n"
    "ds_read_b64_tr_b16 v[136:137], v24 offset:16384\n"
    "ds_read_b64_tr_b16 v[200:201], v25 offset:16384\n"
    "ds_read_b64_tr_b16 v[138:139], v24 offset:17408\n"
    "ds_read_b64_tr_b16 v[202:203], v25 offset:17408\n"
    "ds_read_b64_tr_b16 v[140:141], v24 offset:24576\n"
    "ds_read_b64_tr_b16 v[204:205], v25 offset:24576\n"
    "ds_read_b64_tr_b16 v[142:143], v24 offset:25600\n"
    "ds_read_b64_tr_b16 v[206:207], v25 offset:25600\n"
    "s_waitcnt vmcnt(21) lgkmcnt(0)\n"
    "s_barrier\n"
    "ds_read_b64_tr_b16 v[144:145], v24 offset:32768\n"
    "ds_read_b64_tr_b16 v[208:209], v25 offset:32768\n"
    "ds_read_b64_tr_b16 v[146:147], v24 offset:33792\n"
    "ds_read_b64_tr_b16 v[210:211], v25 offset:33792\n"
    "ds_read_b64_tr_b16 v[148:149], v24 offset:40960\n"
    "ds_read_b64_tr_b16 v[212:213], v25 offset:40960\n"
    "ds_read_b64_tr_b16 v[150:151], v24 offset:41984\n"
    "ds_read_b64_tr_b16 v[214:215], v25 offset:41984\n"
    "s_waitcnt vmcnt(17) lgkmcnt(0)\n"
    "s_barrier\n"
    "ds_read_b64_tr_b16 v[152:153], v24 offset:49152\n"
    "ds_read_b64_tr_b16 v[216:217], v25 offset:49152\n"
    "ds_read_b64_tr_b16 v[154:155], v24 offset:50176\n"
    "ds_read_b64_tr_b16 v[218:219], v25 offset:50176\n"
    "ds_read_b64_tr_b16 v[156:157], v24 offset:57344\n"
    "ds_read_b64_tr_b16 v[220:221], v25 offset:57344\n"
    "ds_read_b64_tr_b16 v[158:159], v24 offset:58368\n"
    "ds_read_b64_tr_b16 v[222:223], v25 offset:58368\n"
    "s_waitcnt vmcnt(13) lgkmcnt(0)\n"
    "s_barrier\n"
    "s_getpc_b64 s[94:95]\n"
    "HCA20_%=:\n"
    "v_lshlrev_b32 v29, 7, v0\n"
    "v_min_u32 v29, .Lcode_end-HCA20_%=-4, v29\n"
    "global_load_dword v29, v29, s[94:95]\n"
    "v_add_u32 v24, 65536, v24\n"
    "v_add_u32 v25, 65536, v25\n"
    "ds_read_b64_tr_b16 v[160:161], v24 offset:0\n"
    "ds_read_b64_tr_b16 v[224:225], v25 offset:0\n"
    "ds_read_b64_tr_b16 v[162:163], v24 offset:1024\n"
    "ds_read_b64_tr_b16 v[226:227], v25 offset:1024\n"
    "ds_read_b64_tr_b16 v[164:165], v24 offset:8192\n"
    "ds_read_b64_tr_b16 v[228:229], v25 offset:8192\n"
    "ds_read_b64_tr_b16 v[166:167], v24 offset:9216\n"
    "ds_read_b64_tr_b16 v[230:231], v25 offset:9216\n"
    "s_waitcnt vmcnt(10) lgkmcnt(0)\n"
    "s_barrier\n"
    "buffer_load_dwordx2 v[26:27], off, s[20:23], s31 sc1\n"
    "s_xor_b32 s45, s40, 0x2000\n"
    "buffer_load_dword v125, v107, s[20:23], s40 offen sc1\n"
    "buffer_load_dword v126, v107, s[20:23], s45 offen sc1\n"
    "ds_read_b64_tr_b16 v[168:169], v24 offset:16384\n"
    "ds_read_b64_tr_b16 v[232:233], v25 offset:16384\n"
    "ds_read_b64_tr_b16 v[170:171], v24 offset:17408\n"
    "ds_read_b64_tr_b16 v[234:235], v25 offset:17408\n"
    "ds_read_b64_tr_b16 v[172:173], v24 offset:24576\n"
    "ds_read_b64_tr_b16 v[236:237], v25 offset:24576\n"
    "ds_read_b64_tr_b16 v[174:175], v24 offset:25600\n"
    "ds_read_b64_tr_b16 v[238:239], v25 offset:25600\n"
    "s_waitcnt vmcnt(8) lgkmcnt(0)\n"
    "s_barrier\n"
    "ds_read_b64_tr_b16 v[176:177], v24 offset:32768\n"
    "ds_read_b64_tr_b16 v[240:241], v25 offset:32768\n"
    "ds_read_b64_tr_b16 v[178:179], v24 offset:33792\n"
    "ds_read_b64_tr_b16 v[242:243], v25 offset:33792\n"
    "ds_read_b64_tr_b16 v[180:181], v24 offset:40960\n"
    "ds_read_b64_tr_b16 v[244:245], v25 offset:40960\n"
    "ds_read_b64_tr_b16 v[182:183], v24 offset:41984\n"
    "ds_read_b64_tr_b16 v[246:247], v25 offset:41984\n"
    "s_waitcnt vmcnt(4) lgkmcnt(0)\n"
    "s_barrier\n"
    "ds_read_b64_tr_b16 v[184:185], v24 offset:49152\n"
    "ds_read_b64_tr_b16 v[248:249], v25 offset:49152\n"
    "ds_read_b64_tr_b16 v[186:187], v24 offset:50176\n"
    "ds_read_b64_tr_b16 v[250:251], v25 offset:50176\n"
    "ds_read_b64_tr_b16 v[188:189], v24 offset:57344\n"
    "ds_read_b64_tr_b16 v[252:253], v25 offset:57344\n"
    "ds_read_b64_tr_b16 v[190:191], v24 offset:58368\n"
    "ds_read_b64_tr_b16 v[254:255], v25 offset:58368\n"
    "s_waitcnt lgkmcnt(0)\n"
    "s_barrier\n"
    "s_waitcnt vmcnt(2)\n"
    "v_readfirstlane_b32 s88, v26\n"
    "s_bitcmp1_b32 s88, 1\n"
    "s_cselect_b32 s92, s92, 0\n"
    "s_xor_b32 s88, s88, s24\n"
    "s_and_b32 s88, s88, 1\n"
    "s_lshl_b32 s88, s88, 13\n"
    "s_cmp_lg_u32 s88, 0\n"
    "s_cselect_b64 vcc, -1, 0\n"
    "s_waitcnt vmcnt(0)\n"
    "v_cndmask_b32 v125, v125, v126, vcc\n"
    "s_cmp_lg_u32 s92, 0\n"
    "s_cbranch_scc1 HF9_%=\n"
    "v_readfirstlane_b32 s45, v21\n"
    "s_cmp_lg_u32 s24, 0\n"
    "s_cselect_b32 s45, %2, s45\n"
    "s_mov_b32 s44, 0\n"
    "HBW_%=:\n"
    "buffer_load_dwordx2 v[20:21], off, s[20:23], s31 sc1\n"
    "s_add_u32 s44, s44, 1\n"
    "s_cmp_gt_u32 s44, 262144\n"
    "s_cbranch_scc1 HEND_%=\n"
    "s_waitcnt vmcnt(0)\n"
    "v_readfirstlane_b32 s88, v20\n"
    "v_cmp_eq_u32 vcc, s45, v21\n"
    "s_cbranch_vccnz HBW_%=\n"
    "s_bitcmp1_b32 s88, 1\n"
    "s_cbranch_scc0 HBW_%=\n"
    "s_xor_b32 s88, s88, s24\n"
    "s_and_b32 s88, s88, 1\n"
    "s_lshl_b32 s88, s88, 13\n"
    "s_lshl_b32 s45, s28, 7\n"
    "s_add_u32 s45, s45, 81793024\n"
    "s_cmp_lg_u32 s24, 0\n"
    "s_cselect_b32 s45, 0x7f000000, s45\n"
    "v_mov_b32 v20, 1\n"
    "s_mov_b64 exec, 1\n"
    "buffer_store_dword v20, off, s[20:23], s45 sc1\n"
    "s_mov_b64 exec, -1\n"
    "v_mov_b32 v125, 0\n"
    "HF9_%=:\n"
    "s_xor_b32 s35, s35, s88\n"
    "s_xor_b32 s40, s40, s88\n"
    "s_xor_b32 s89, s89, s88\n"
    "s_xor_b32 s38, s38, s88\n"
    "s_xor_b32 s39, s39, s88\n"
    "s_xor_b32 s34, s34, s88\n"
    "s_xor_b32 s36, s36, s88\n"
    "s_xor_b32 s37, s37, s88\n"
    "s_lshl_b32 s28, s26, 3\n"
    "s_add_u32 s28, s28, s27\n"
    "s_lshl_b32 s28, s28, 11\n"
    "s_add_u32 s28, s28, s88\n"
    "s_mul_i32 s45, s25, 3670016\n"
    "s_add_u32 s45, s45, 67633152\n"
    "s_add_u32 s28, s28, s45\n"
    "s_mul_i32 s66, s27, 6144\n"
    "s_mov_b32 s67, 0x7f000000\n"
    "s_mov_b32 s69, 0x7f000000\n"
    "s_cmp_lg_u32 s91, 0\n"
    "s_cbranch_scc1 HF0_%=c\n"
    "v_cmp_gt_u32 vcc, s48, v125\n"
    "s_cbranch_vccz HF0_%=d\n"
    "s_mov_b32 s44, 0\n"
    "s_branch HF0_%=b\n"
    "HF0_%=:\n"
    "s_sleep 1\n"
    "s_add_u32 s44, s44, 1\n"
    "s_cmp_gt_u32 s44, 262144\n"
    "s_cbranch_scc1 HEND_%=\n"
    "HF0_%=b:\n"
    "buffer_load_dword v125, v107, s[20:23], s40 offen sc1\n"
    "s_waitcnt vmcnt(0)\n"
    "v_cmp_gt_u32 vcc, s48, v125\n"
    "s_cbranch_vccnz HF0_%=\n"
    "s_cmp_eq_u32 s24, 0\n"
    "s_cbranch_scc1 HF0_%=d\n"
    "s_sleep 6\n"
    "HF0_%=d:\n"
    "v_cmp_gt_u32 vcc, 64, v125\n"
    "s_cmp_eq_u64 vcc, 0\n"
    "s_cselect_b32 s91, 1, 0\n"
    "HF0_%=c:\n"
    "s_lshr_b32 s45, s34, 8\n"
    "s_and_b32 s45, s45, 0x2000\n"
    "s_xor_b32 s90, s34, s45\n"
    "s_mov_b32 m0, s43\n"
    "s_add_u32 s45, s90, 4096\n"
    "buffer_load_dwordx4 v101, s[20:23], s90 offen offset:0 sc1 lds\n"
    "s_add_u32 m0, m0, 32\n"
    "s_nop 0\n"
    "buffer_load_dwordx4 v101, s[20:23], s90 offen offset:1024 sc1 lds\n"
    "s_add_u32 m0, m0, 32\n"
    "s_nop 0\n"
    "buffer_load_dwordx4 v101, s[20:23], s90 offen offset:2048 sc1 lds\n"
    "s_add_u32 m0, m0, 32\n"
    "s_nop 0\n"
    "buffer_load_dwordx4 v101, s[20:23], s90 offen offset:3072 sc1 lds\n"
    "s_add_u32 m0, m0, 4128\n"
    "s_nop 0\n"
    "buffer_load_dwordx4 v101, s[20:23], s45 offen offset:0 sc1 lds\n"
    "s_add_u32 m0, m0, 32\n"
    "s_nop 0\n"
    "buffer_load_dwordx4 v101, s[20:23], s45 offen offset:1024 sc1 lds\n"
    "s_add_u32 m0, m0, 32\n"
    "s_nop 0\n"
    "buffer_load_dwordx4 v101, s[20:23], s45 offen offset:2048 sc1 lds\n"
    "s_add_u32 m0, m0, 32\n"
    "s_nop 0\n"
    "buffer_load_dwordx4 v101, s[20:23], s45 offen offset:3072 sc1 lds\n"
    "s_add_u32 s34, s34, 131072\n"
    "s_add_u32 s48, s48, 2\n"
    "s_or_b32 s92, s92, 2\n"
    "s_mov_b32 s44, 0\n"
    ".p2align 6\n"
    "HLP_%=:\n"
    "s_add_u32 s45, s41, s66\n"
    "v_add_u32 v103, s45, v101\n"
    "s_add_u32 s41, s41, 24576\n"
    "s_cmp_eq_u32 s41, 107520\n"
    "s_cselect_b32 s41, 33792, s41\n"
    "s_sub_u32 s45, s32, 1\n"
    "s_and_b32 s45, s45, 7\n"
    "s_lshl_b32 s45, s45, 20\n"
    "s_add_u32 s46, s45, s35\n"
    "s_lshr_b32 s45, s46, 8\n"
    "s_and_b32 s45, s45, 0x2000\n"
    "s_xor_b32 s46, s46, s45\n"
    "v_mov_b32 v108, s32\n"
    "s_waitcnt vmcnt(0)\n"
    "s_cmp_lg_u32 s93, 0x7f000000\n"
    "s_cbranch_scc1 HR2_%=\n"
    "HR1_%=:\n"
    "s_waitcnt vmcnt(0)\n"
    "HR2_%=:\n"
    "s_barrier\n"
    "ds_read_b128 v[0:3], v102 offset:0\n"
    "ds_read_b128 v[4:7], v102 offset:16896\n"
    "ds_read_b128 v[8:11], v102 offset:64\n"
    "ds_read_b128 v[12:15], v102 offset:16960\n"
    "s_waitcnt lgkmcnt(3)\n"
    "v_mfma_f32_16x16x32_bf16 v[32:35], a[0:3], v[0:3], 0\n"
    "ds_read_b128 v[16:19], v102 offset:128\n"
    "v_mfma_f32_16x16x32_bf16 v[36:39], a[64:67], v[0:3], 0\n"
    "ds_read_b128 v[20:23], v102 offset:17024\n"
    "s_waitcnt lgkmcnt(4)\n"
    "v_mfma_f32_16x16x32_bf16 v[40:43], a[0:3], v[4:7], 0\n"
    "v_max_u32 v109, v0, v2\n"
    "v_mfma_f32_16x16x32_bf16 v[44:47], a[64:67], v[4:7], 0\n"
    "v_max3_u32 v109, v109, v4, v6\n"
    "v_mfma_f32_16x16x32_bf16 v[48:51], v[0:3], a[128:131], 0\n"
    "v_mfma_f32_16x16x32_bf16 v[52:55], v[4:7], a[128:131], 0\n"
    "v_mfma_f32_16x16x32_bf16 v[56:59], v[0:3], a[192:195], 0\n"
    "v_mfma_f32_16x16x32_bf16 v[60:63], v[4:7], a[192:195], 0\n"
    "s_waitcnt lgkmcnt(3)\n"
    "v_mfma_f32_16x16x32_bf16 v[32:35], a[4:7], v[8:11], v[32:35]\n"
    "ds_read_b128 v[0:3], v102 offset:192\n"
    "v_mfma_f32_16x16x32_bf16 v[36:39], a[68:71], v[8:11], v[36:39]\n"
    "ds_read_b128 v[4:7], v102 offset:17088\n"
    "s_waitcnt lgkmcnt(4)\n"
    "v_mfma_f32_16x16x32_bf16 v[40:43], a[4:7], v[12:15], v[40:43]\n"
    "v_max3_u32 v109, v109, v8, v10\n"
    "v_mfma_f32_16x16x32_bf16 v[44:47], a[68:71], v[12:15], v[44:47]\n"
    "v_max3_u32 v109, v109, v12, v14\n"
    "v_mfma_f32_16x16x32_bf16 v[48:51], v[8:11], a[132:135], v[48:51]\n"
    "v_mfma_f32_16x16x32_bf16 v[52:55], v[12:15], a[132:135], v[52:55]\n"
    "v_mfma_f32_16x16x32_bf16 v[56:59], v[8:11], a[196:199], v[56:59]\n"
    "v_mfma_f32_16x16x32_bf16 v[60:63], v[12:15], a[196:199], v[60:63]\n"
    "s_waitcnt lgkmcnt(3)\n"
    "v_mfma_f32_16x16x32_bf16 v[32:35], a[8:11], v[16:19], v[32:35]\n"
    "ds_read_b128 v[8:11], v102 offset:256\n"
    "v_mfma_f32_16x16x32_bf16 v[36:39], a[72:75], v[16:19], v[36:39]\n"
    "ds_read_b128 v[12:15], v102 offset:17152\n"
    "s_waitcnt lgkmcnt(4)\n"
    "v_mfma_f32_16x16x32_bf16 v[40:43], a[8:11], v[20:23], v[40:43]\n"
    "v_max3_u32 v109, v109, v16, v18\n"
    "v_mfma_f32_16x16x32_bf16 v[44:47], a[72:75], v[20:23], v[44:47]\n"
    "v_max3_u32 v109, v109, v20, v22\n"
    "v_mfma_f32_16x16x32_bf16 v[48:51], v[16:19], a[136:139], v[48:51]\n"
    "v_mfma_f32_16x16x32_bf16 v[52:55], v[20:23], a[136:139], v[52:55]\n"
    "v_mfma_f32_16x16x32_bf16 v[56:59], v[16:19], a[200:203], v[56:59]\n"
    "v_mfma_f32_16x16x32_bf16 v[60:63], v[20:23], a[200:203], v[60:63]\n"
    "s_waitcnt lgkmcnt(3)\n"
    "v_mfma_f32_16x16x32_bf16 v[32:35], a[12:15], v[0:3], v[32:35]\n"
    "ds_read_b128 v[16:19], v102 offset:320\n"
    "v_mfma_f32_16x16x32_bf16 v[36:39], a[76:79], v[0:3], v[36:39]\n"
    "ds_read_b128 v[20:23], v102 offset:17216\n"
    "s_waitcnt lgkmcnt(4)\n"
    "v_mfma_f32_16x16x32_bf16 v[40:43], a[12:15], v[4:7], v[40:43]\n"
    "v_max3_u32 v109, v109, v0, v2\n"
    "v_mfma_f32_16x16x32_bf16 v[44:47], a[76:79], v[4:7], v[44:47]\n"
    "v_max3_u32 v109, v109, v4, v6\n"
    "v_mfma_f32_16x16x32_bf16 v[48:51], v[0:3], a[140:143], v[48:51]\n"
    "v_mfma_f32_16x16x32_bf16 v[52:55], v[4:7], a[140:143], v[52:55]\n"
    "v_mfma_f32_16x16x32_bf16 v[56:59], v[0:3], a[204:207], v[56:59]\n"
    "v_mfma_f32_16x16x32_bf16 v[60:63], v[4:7], a[204:207], v[60:63]\n"
    "s_waitcnt lgkmcnt(3)\n"
    "v_mfma_f32_16x16x32_bf16 v[32:35], a[16:19], v[8:11], v[32:35]\n"
    "ds_read_b128 v[0:3], v102 offset:384\n"
    "v_mfma_f32_16x16x32_bf16 v[36:39], a[80:83], v[8:11], v[36:39]\n"
    "ds_read_b128 v[4:7], v102 offset:17280\n"
    "s_waitcnt lgkmcnt(4)\n"
    "v_mfma_f32_16x16x32_bf16 v[40:43], a[16:19], v[12:15], v[40:43]\n"
    "v_max3_u32 v109, v109, v8, v10\n"
    "v_mfma_f32_16x16x32_bf16 v[44:47], a[80:83], v[12:15], v[44:47]\n"
    "v_max3_u32 v109, v109, v12, v14\n"
    "v_mfma_f32_16x16x32_bf16 v[48:51], v[8:11], a[144:147], v[48:51]\n"
    "v_mfma_f32_16x16x32_bf16 v[52:55], v[12:15], a[144:147], v[52:55]\n"
    "v_mfma_f32_16x16x32_bf16 v[56:59], v[8:11], a[208:211], v[56:59]\n"
    "v_mfma_f32_16x16x32_bf16 v[60:63], v[12:15], a[208:211], v[60:63]\n"
    "buffer_store_dwordx4 v[64:67], v101, s[20:23], s67 offen offset:0 sc1\n"
    "buffer_store_dwordx4 v[72:75], v101, s[20:23], s67 offen offset:2048 sc1\n"
    "s_mov_b32 s67, 0x7f000000\n"
    "s_waitcnt lgkmcnt(3)\n"
    "v_mfma_f32_16x16x32_bf16 v[32:35], a[20:23], v[16:19], v[32:35]\n"
    "ds_read_b128 v[8:11], v102 offset:448\n"
    "v_mfma_f32_16x16x32_bf16 v[36:39], a[84:87], v[16:19], v[36:39]\n"
    "ds_read_b128 v[12:15], v102 offset:17344\n"
    "s_waitcnt lgkmcnt(4)\n"
    "v_mfma_f32_16x16x32_bf16 v[40:43], a[20:23], v[20:23], v[40:43]\n"
    "v_max3_u32 v109, v109, v16, v18\n"
    "v_mfma_f32_16x16x32_bf16 v[44:47], a[84:87], v[20:23], v[44:47]\n"
    "v_max3_u32 v109, v109, v20, v22\n"
    "v_mfma_f32_16x16x32_bf16 v[48:51], v[16:19], a[148:151], v[48:51]\n"
    "v_mfma_f32_16x16x32_bf16 v[52:55], v[20:23], a[148:151], v[52:55]\n"
    "v_mfma_f32_16x16x32_bf16 v[56:59], v[16:19], a[212:215], v[56:59]\n"
    "v_mfma_f32_16x16x32_bf16 v[60:63], v[20:23], a[212:215], v[60:63]\n"
    "s_waitcnt lgkmcnt(3)\n"
    "v_mfma_f32_16x16x32_bf16 v[32:35], a[24:27], v[0:3], v[32:35]\n"
    "ds_read_b128 v[16:19], v102 offset:512\n"
    "v_mfma_f32_16x16x32_bf16 v[36:39], a[88:91], v[0:3], v[36:39]\n"
    "ds_read_b128 v[20:23], v102 offset:17408\n"
    "s_waitcnt lgkmcnt(4)\n"
    "v_mfma_f32_16x16x32_bf16 v[40:43], a[24:27], v[4:7], v[40:43]\n"
    "v_max3_u32 v109, v109, v0, v2\n"
    "v_mfma_f32_16x16x32_bf16 v[44:47], a[88:91], v[4:7], v[44:47]\n"
    "v_max3_u32 v109, v109, v4, v6\n"
    "v_mfma_f32_16x16x32_bf16 v[48:51], v[0:3], a[152:155], v[48:51]\n"
    "v_mfma_f32_16x16x32_bf16 v[52:55], v[4:7], a[152:155], v[52:55]\n"
    "v_mfma_f32_16x16x32_bf16 v[56:59], v[0:3], a[216:219], v[56:59]\n"
    "v_mfma_f32_16x16x32_bf16 v[60:63], v[4:7], a[216:219], v[60:63]\n"
    "buffer_store_dwordx4 v[64:67], v101, s[20:23], s69 offen offset:0 sc1\n"
    "buffer_store_dwordx4 v[72:75], v101, s[20:23], s69 offen offset:2048 sc1\n"
    "s_mov_b32 s69, 0x7f000000\n"
    "s_waitcnt lgkmcnt(3)\n"
    "v_mfma_f32_16x16x32_bf16 v[32:35], a[28:31], v[8:11], v[32:35]\n"
    "ds_read_b128 v[0:3], v102 offset:576\n"
    "v_mfma_f32_16x16x32_bf16 v[36:39], a[92:95], v[8:11], v[36:39]\n"
    "ds_read_b128 v[4:7], v102 offset:17472\n"
    "s_waitcnt lgkmcnt(4)\n"
    "v_mfma_f32_16x16x32_bf16 v[40:43], a[28:31], v[12:15], v[40:43]\n"
    "v_max3_u32 v109, v109, v8, v10\n"
    "v_mfma_f32_16x16x32_bf16 v[44:47], a[92:95], v[12:15], v[44:47]\n"
    "v_max3_u32 v109, v109, v12, v14\n"
    "v_mfma_f32_16x16x32_bf16 v[48:51], v[8:11], a[156:159], v[48:51]\n"
    "v_mfma_f32_16x16x32_bf16 v[52:55], v[12:15], a[156:159], v[52:55]\n"
    "v_mfma_f32_16x16x32_bf16 v[56:59], v[8:11], a[220:223], v[56:59]\n"
    "v_mfma_f32_16x16x32_bf16 v[60:63], v[12:15], a[220:223], v[60:63]\n"
    "s_waitcnt lgkmcnt(3)\n"
    "v_mfma_f32_16x16x32_bf16 v[32:35], a[32:35], v[16:19], v[32:35]\n"
    "ds_read_b128 v[8:11], v102 offset:640\n"
    "v_mfma_f32_16x16x32_bf16 v[36:39], a[96:99], v[16:19], v[36:39]\n"
    "ds_read_b128 v[12:15], v102 offset:17536\n"
    "s_waitcnt lgkmcnt(4)\n"
    "v_mfma_f32_16x16x32_bf16 v[40:43], a[32:35], v[20:23], v[40:43]\n"
    "v_max3_u32 v109, v109, v16, v18\n"
    "v_mfma_f32_16x16x32_bf16 v[44:47], a[96:99], v[20:23], v[44:47]\n"
    "v_max3_u32 v109, v109, v20, v22\n"
    "v_mfma_f32_16x16x32_bf16 v[48:51], v[16:19], a[160:163], v[48:51]\n"
    "v_mfma_f32_16x16x32_bf16 v[52:55], v[20:23], a[160:163], v[52:55]\n"
    "v_mfma_f32_16x16x32_bf16 v[56:59], v[16:19], a[224:227], v[56:59]\n"
    "v_mfma_f32_16x16x32_bf16 v[60:63], v[20:23], a[224:227], v[60:63]\n"
    "s_waitcnt lgkmcnt(3)\n"
    "v_mfma_f32_16x16x32_bf16 v[32:35], a[36:39], v[0:3], v[32:35]\n"
    "ds_read_b128 v[16:19], v102 offset:704\n"
    "v_mfma_f32_16x16x32_bf16 v[36:39], a[100:103], v[0:3], v[36:39]\n"
    "ds_read_b128 v[20:23], v102 offset:17600\n"
    "s_waitcnt lgkmcnt(4)\n"
    "v_mfma_f32_16x16x32_bf16 v[40:43], a[36:39], v[4:7], v[40:43]\n"
    "v_max3_u32 v109, v109, v0, v2\n"
    "v_mfma_f32_16x16x32_bf16 v[44:47], a[100:103], v[4:7], v[44:47]\n"
    "v_max3_u32 v109, v109, v4, v6\n"
    "v_mfma_f32_16x16x32_bf16 v[48:51], v[0:3], a[164:167], v[48:51]\n"
    "v_mfma_f32_16x16x32_bf16 v[52:55], v[4:7], a[164:167], v[52:55]\n"
    "v_mfma_f32_16x16x32_bf16 v[56:59], v[0:3], a[228:231], v[56:59]\n"
    "v_mfma_f32_16x16x32_bf16 v[60:63], v[4:7], a[228:231], v[60:63]\n"
    "s_waitcnt lgkmcnt(3)\n"
    "v_mfma_f32_16x16x32_bf16 v[32:35], a[40:43], v[8:11], v[32:35]\n"
    "ds_read_b128 v[0:3], v102 offset:768\n"
    "v_mfma_f32_16x16x32_bf16 v[36:39], a[104:107], v[8:11], v[36:39]\n"
    "ds_read_b128 v[4:7], v102 offset:17664\n"
    "s_waitcnt lgkmcnt(4)\n"
    "v_mfma_f32_16x16x32_bf16 v[40:43], a[40:43], v[12:15], v[40:43]\n"
    "v_max3_u32 v109, v109, v8, v10\n"
    "v_mfma_f32_16x16x32_bf16 v[44:47], a[104:107], v[12:15], v[44:47]\n"
    "v_max3_u32 v109, v109, v12, v14\n"
    "v_mfma_f32_16x16x32_bf16 v[48:51], v[8:11], a[168:171], v[48:51]\n"
    "v_mfma_f32_16x16x32_bf16 v[52:55], v[12:15], a[168:171], v[52:55]\n"
    "v_mfma_f32_16x16x32_bf16 v[56:59], v[8:11], a[232:235], v[56:59]\n"
    "v_mfma_f32_16x16x32_bf16 v[60:63], v[12:15], a[232:235], v[60:63]\n"
    "s_waitcnt vmcnt(0)\n"
    "s_mov_b64 exec, 1\n"
    "buffer_store_dword v127, off, s[20:23], s93 sc1\n"
    "s_mov_b64 exec, -1\n"
    "s_mov_b32 s93, 0x7f000000\n"
    "s_waitcnt lgkmcnt(3)\n"
    "v_mfma_f32_16x16x32_bf16 v[32:35], a[44:47], v[16:19], v[32:35]\n"
    "ds_read_b128 v[8:11], v102 offset:832\n"
    "v_mfma_f32_16x16x32_bf16 v[36:39], a[108:111], v[16:19], v[36:39]\n"
    "ds_read_b128 v[12:15], v102 offset:17728\n"
    "s_waitcnt lgkmcnt(4)\n"
    "v_mfma_f32_16x16x32_bf16 v[40:43], a[44:47], v[20:23], v[40:43]\n"
    "v_max3_u32 v109, v109, v16, v18\n"
    "v_mfma_f32_16x16x32_bf16 v[44:47], a[108:111], v[20:23], v[44:47]\n"
    "v_max3_u32 v109, v109, v20, v22\n"
    "v_mfma_f32_16x16x32_bf16 v[48:51], v[16:19], a[172:175], v[48:51]\n"
    "v_mfma_f32_16x16x32_bf16 v[52:55], v[20:23], a[172:175], v[52:55]\n"
    "v_mfma_f32_16x16x32_bf16 v[56:59], v[16:19], a[236:239], v[56:59]\n"
    "v_mfma_f32_16x16x32_bf16 v[60:63], v[20:23], a[236:239], v[60:63]\n"
    "s_waitcnt lgkmcnt(3)\n"
    "v_mfma_f32_16x16x32_bf16 v[32:35], a[48:51], v[0:3], v[32:35]\n"
    "ds_read_b128 v[16:19], v102 offset:896\n"
    "v_mfma_f32_16x16x32_bf16 v[36:39], a[112:115], v[0:3], v[36:39]\n"
    "ds_read_b128 v[20:23], v102 offset:17792\n"
    "s_waitcnt lgkmcnt(4)\n"
    "v_mfma_f32_16x16x32_bf16 v[40:43], a[48:51], v[4:7], v[40:43]\n"
    "v_max3_u32 v109, v109, v0, v2\n"
    "v_mfma_f32_16x16x32_bf16 v[44:47], a[112:115], v[4:7], v[44:47]\n"
    "v_max3_u32 v109, v109, v4, v6\n"
    "v_mfma_f32_16x16x32_bf16 v[48:51], v[0:3], a[176:179], v[48:51]\n"
    "v_mfma_f32_16x16x32_bf16 v[52:55], v[4:7], a[176:179], v[52:55]\n"
    "v_mfma_f32_16x16x32_bf16 v[56:59], v[0:3], a[240:243], v[56:59]\n"
    "v_mfma_f32_16x16x32_bf16 v[60:63], v[4:7], a[240:243], v[60:63]\n"
    "s_cmp_lg_u32 s91, 0\n"
    "s_cselect_b32 s45, 0x7f000000, s40\n"
    "buffer_load_dword v125, v107, s[20:23], s45 offen sc1\n"
    "s_waitcnt lgkmcnt(3)\n"
    "v_mfma_f32_16x16x32_bf16 v[32:35], a[52:55], v[8:11], v[32:35]\n"
    "ds_read_b128 v[0:3], v102 offset:960\n"
    "v_mfma_f32_16x16x32_bf16 v[36:39], a[116:119], v[8:11], v[36:39]\n"
    "ds_read_b128 v[4:7], v102 offset:17856\n"
    "s_waitcnt lgkmcnt(4)\n"
    "v_mfma_f32_16x16x32_bf16 v[40:43], a[52:55], v[12:15], v[40:43]\n"
    "v_max3_u32 v109, v109, v8, v10\n"
    "v_mfma_f32_16x16x32_bf16 v[44:47], a[116:119], v[12:15], v[44:47]\n"
    "v_max3_u32 v109, v109, v12, v14\n"
    "v_mfma_f32_16x16x32_bf16 v[48:51], v[8:11], a[180:183], v[48:51]\n"
    "v_mfma_f32_16x16x32_bf16 v[52:55], v[12:15], a[180:183], v[52:55]\n"
    "v_mfma_f32_16x16x32_bf16 v[56:59], v[8:11], a[244:247], v[56:59]\n"
    "v_mfma_f32_16x16x32_bf16 v[60:63], v[12:15], a[244:247], v[60:63]\n"
    "buffer_store_dwordx4 v[120:123], v101, s[20:23], s28 offen offset:0 sc1\n"
    "s_waitcnt lgkmcnt(3)\n"
    "v_mfma_f32_16x16x32_bf16 v[32:35], a[56:59], v[16:19], v[32:35]\n"
    "ds_read_b128 v[8:11], v102 offset:0\n"
    "v_mfma_f32_16x16x32_bf16 v[36:39], a[120:123], v[16:19], v[36:39]\n"
    "ds_read_b128 v[12:15], v102 offset:16896\n"
    "s_waitcnt lgkmcnt(4)\n"
    "v_mfma_f32_16x16x32_bf16 v[40:43], a[56:59], v[20:23], v[40:43]\n"
    "v_max3_u32 v109, v109, v16, v18\n"
    "v_mfma_f32_16x16x32_bf16 v[44:47], a[120:123], v[20:23], v[44:47]\n"
    "v_max3_u32 v109, v109, v20, v22\n"
    "v_mfma_f32_16x16x32_bf16 v[48:51], v[16:19], a[184:187], v[48:51]\n"
    "v_mfma_f32_16x16x32_bf16 v[52:55], v[20:23], a[184:187], v[52:55]\n"
    "v_mfma_f32_16x16x32_bf16 v[56:59], v[16:19], a[248:251], v[56:59]\n"
    "v_mfma_f32_16x16x32_bf16 v[60:63], v[20:23], a[248:251], v[60:63]\n"
    "buffer_store_dwordx4 v[120:123], v101, s[20:23], s28 offen offset:1024 sc1\n"
    "s_waitcnt lgkmcnt(3)\n"
    "v_mfma_f32_16x16x32_bf16 v[32:35], a[60:63], v[0:3], v[32:35]\n"
    "ds_read_b128 v[16:19], v102 offset:64\n"
    "v_mfma_f32_16x16x32_bf16 v[36:39], a[124:127], v[0:3], v[36:39]\n"
    "ds_read_b128 v[20:23], v102 offset:16960\n"
    "s_waitcnt lgkmcnt(4)\n"
    "v_mfma_f32_16x16x32_bf16 v[40:43], a[60:63], v[4:7], v[40:43]\n"
    "v_max3_u32 v109, v109, v0, v2\n"
    "v_mfma_f32_16x16x32_bf16 v[44:47], a[124:127], v[4:7], v[44:47]\n"
    "v_max3_u32 v109, v109, v4, v6\n"
    "v_mfma_f32_16x16x32_bf16 v[48:51], v[0:3], a[188:191], v[48:51]\n"
    "v_cmp_eq_u32 vcc, -1, v109\n"
    "v_mfma_f32_16x16x32_bf16 v[52:55], v[4:7], a[188:191], v[52:55]\n"
    "v_mfma_f32_16x16x32_bf16 v[56:59], v[0:3], a[252:255], v[56:59]\n"
    "v_mfma_f32_16x16x32_bf16 v[60:63], v[4:7], a[252:255], v[60:63]\n"
    "s_cbranch_vccnz HRT_%=\n"
    "s_waitcnt lgkmcnt(3)\n"
    "v_mfma_f32_16x16x32_bf16 v[64:67], v[128:131], v[8:11], 0\n"
    "ds_read_b128 v[0:3], v102 offset:128\n"
    "v_cvt_pk_bf16_f32 v32, v32, v33\n"
    "v_mfma_f32_16x16x32_bf16 v[68:71], v[192:195], v[8:11], 0\n"
    "ds_read_b128 v[4:7], v102 offset:17024\n"
    "v_cvt_pk_bf16_f32 v33, v34, v35\n"
    "s_waitcnt lgkmcnt(4)\n"
    "v_mfma_f32_16x16x32_bf16 v[72:75], v[128:131], v[12:15], 0\n"
    "v_cvt_pk_bf16_f32 v34, v36, v37\n"
    "v_cvt_pk_bf16_f32 v35, v38, v39\n"
    "v_mfma_f32_16x16x32_bf16 v[76:79], v[192:195], v[12:15], 0\n"
    "buffer_store_dwordx4 v[32:35], v101, s[20:23], s46 offen offset:0 sc1\n"
    "s_waitcnt lgkmcnt(3)\n"
    "v_mfma_f32_16x16x32_bf16 v[64:67], v[132:135], v[16:19], v[64:67]\n"
    "ds_read_b128 v[8:11], v102 offset:192\n"
    "v_cvt_pk_bf16_f32 v40, v40, v41\n"
    "v_mfma_f32_16x16x32_bf16 v[68:71], v[196:199], v[16:19], v[68:71]\n"
    "ds_read_b128 v[12:15], v102 offset:17088\n"
    "v_cvt_pk_bf16_f32 v41, v42, v43\n"
    "s_waitcnt lgkmcnt(4)\n"
    "v_mfma_f32_16x16x32_bf16 v[72:75], v[132:135], v[20:23], v[72:75]\n"
    "v_cvt_pk_bf16_f32 v42, v44, v45\n"
    "v_cvt_pk_bf16_f32 v43, v46, v47\n"
    "v_mfma_f32_16x16x32_bf16 v[76:79], v[196:199], v[20:23], v[76:79]\n"
    "buffer_store_dwordx4 v[40:43], v101, s[20:23], s46 offen offset:1024 sc1\n"
    "s_waitcnt lgkmcnt(3)\n"
    "v_mfma_f32_16x16x32_bf16 v[64:67], v[136:139], v[0:3], v[64:67]\n"
    "ds_read_b128 v[16:19], v102 offset:256\n"
    "v_cvt_pk_bf16_f32 v51, v50, v51\n"
    "v_mfma_f32_16x16x32_bf16 v[68:71], v[200:203], v[0:3], v[68:71]\n"
    "ds_read_b128 v[20:23], v102 offset:17152\n"
    "v_cvt_pk_bf16_f32 v50, v48, v49\n"
    "s_waitcnt lgkmcnt(4)\n"
    "v_mfma_f32_16x16x32_bf16 v[72:75], v[136:139], v[4:7], v[72:75]\n"
    "v_cvt_pk_bf16_f32 v48, v52, v53\n"
    "v_cvt_pk_bf16_f32 v49, v54, v55\n"
    "v_mfma_f32_16x16x32_bf16 v[76:79], v[200:203], v[4:7], v[76:79]\n"
    "buffer_store_dwordx4 v[48:51], v101, s[20:23], s46 offen offset:2048 sc1\n"
    "s_waitcnt lgkmcnt(3)\n"
    "v_mfma_f32_16x16x32_bf16 v[64:67], v[140:143], v[8:11], v[64:67]\n"
    "ds_read_b128 v[0:3], v102 offset:320\n"
    "v_cvt_pk_bf16_f32 v56, v56, v57\n"
    "v_mfma_f32_16x16x32_bf16 v[68:71], v[204:207], v[8:11], v[68:71]\n"
    "ds_read_b128 v[4:7], v102 offset:17216\n"
    "v_cvt_pk_bf16_f32 v57, v58, v59\n"
    "s_waitcnt lgkmcnt(4)\n"
    "v_mfma_f32_16x16x32_bf16 v[72:75], v[140:143], v[12:15], v[72:75]\n"
    "v_cvt_pk_bf16_f32 v58, v60, v61\n"
    "v_cvt_pk_bf16_f32 v59, v62, v63\n"
    "v_mfma_f32_16x16x32_bf16 v[76:79], v[204:207], v[12:15], v[76:79]\n"
    "buffer_store_dwordx4 v[56:59], v101, s[20:23], s46 offen offset:3072 sc1\n"
    "s_waitcnt lgkmcnt(3)\n"
    "v_mfma_f32_16x16x32_bf16 v[64:67], v[144:147], v[16:19], v[64:67]\n"
    "ds_read_b128 v[8:11], v102 offset:384\n"
    "v_cvt_pk_bf16_f32 v52, v52, v53\n"
    "v_mfma_f32_16x16x32_bf16 v[68:71], v[208:211], v[16:19], v[68:71]\n"
    "ds_read_b128 v[12:15], v102 offset:17280\n"
    "v_cvt_pk_bf16_f32 v53, v54, v55\n"
    "s_waitcnt lgkmcnt(4)\n"
    "v_mfma_f32_16x16x32_bf16 v[72:75], v[144:147], v[20:23], v[72:75]\n"
    "ds_write_b128 v103, v[32:35] offset:0\n"
    "v_mfma_f32_16x16x32_bf16 v[76:79], v[208:211], v[20:23], v[76:79]\n"
    "ds_write_b128 v103, v[40:43] offset:1024\n"
    "s_waitcnt lgkmcnt(5)\n"
    "v_mfma_f32_16x16x32_bf16 v[64:67], v[148:151], v[0:3], v[64:67]\n"
    "ds_read_b128 v[16:19], v102 offset:448\n"
    "v_mfma_f32_16x16x32_bf16 v[68:71], v[212:215], v[0:3], v[68:71]\n"
    "ds_read_b128 v[20:23], v102 offset:17344\n"
    "s_waitcnt lgkmcnt(6)\n"
    "v_mfma_f32_16x16x32_bf16 v[72:75], v[148:151], v[4:7], v[72:75]\n"
    "ds_write_b128 v103, v[50:53] offset:2048\n"
    "v_mfma_f32_16x16x32_bf16 v[76:79], v[212:215], v[4:7], v[76:79]\n"
    "ds_write_b128 v103, v[56:59] offset:3072\n"
    "s_waitcnt lgkmcnt(7)\n"
    "v_mfma_f32_16x16x32_bf16 v[64:67], v[152:155], v[8:11], v[64:67]\n"
    "ds_read_b128 v[0:3], v102 offset:512\n"
    "v_mfma_f32_16x16x32_bf16 v[68:71], v[216:219], v[8:11], v[68:71]\n"
    "ds_read_b128 v[4:7], v102 offset:17408\n"
    "s_waitcnt lgkmcnt(8)\n"
    "v_mfma_f32_16x16x32_bf16 v[72:75], v[152:155], v[12:15], v[72:75]\n"
    "v_mfma_f32_16x16x32_bf16 v[76:79], v[216:219], v[12:15], v[76:79]\n"
    "s_waitcnt lgkmcnt(5)\n"
    "v_mfma_f32_16x16x32_bf16 v[64:67], v[156:159], v[16:19], v[64:67]\n"
    "ds_read_b128 v[8:11], v102 offset:576\n"
    "v_mfma_f32_16x16x32_bf16 v[68:71], v[220:223], v[16:19], v[68:71]\n"
    "ds_read_b128 v[12:15], v102 offset:17472\n"
    "s_waitcnt lgkmcnt(6)\n"
    "v_mfma_f32_16x16x32_bf16 v[72:75], v[156:159], v[20:23], v[72:75]\n"
    "v_mfma_f32_16x16x32_bf16 v[76:79], v[220:223], v[20:23], v[76:79]\n"
    "s_waitcnt lgkmcnt(3)\n"
    "v_mfma_f32_16x16x32_bf16 v[64:67], v[160:163], v[0:3], v[64:67]\n"
    "ds_read_b128 v[16:19], v102 offset:640\n"
    "v_mfma_f32_16x16x32_bf16 v[68:71], v[224:227], v[0:3], v[68:71]\n"
    "ds_read_b128 v[20:23], v102 offset:17536\n"
    "s_waitcnt lgkmcnt(4)\n"
    "v_mfma_f32_16x16x32_bf16 v[72:75], v[160:163], v[4:7], v[72:75]\n"
    "v_mfma_f32_16x16x32_bf16 v[76:79], v[224:227], v[4:7], v[76:79]\n"
    "s_waitcnt lgkmcnt(3)\n"
    "v_mfma_f32_16x16x32_bf16 v[64:67], v[164:167], v[8:11], v[64:67]\n"
    "ds_read_b128 v[0:3], v102 offset:704\n"
    "v_mfma_f32_16x16x32_bf16 v[68:71], v[228:231], v[8:11], v[68:71]\n"
    "ds_read_b128 v[4:7], v102 offset:17600\n"
    "s_waitcnt lgkmcnt(4)\n"
    "v_mfma_f32_16x16x32_bf16 v[72:75], v[164:167], v[12:15], v[72:75]\n"
    "v_mfma_f32_16x16x32_bf16 v[76:79], v[228:231], v[12:15], v[76:79]\n"
    "s_waitcnt vmcnt(0)\n"
    "s_mov_b64 exec, 1\n"
    "buffer_store_dword v108, off, s[20:23], s38 sc1\n"
    "s_mov_b64 exec, -1\n"
    "s_cmp_gt_u32 s33, 2\n"
    "s_cselect_b32 s45, 128, 0\n"
    "s_sub_u32 s30, s89, s45\n"
    "buffer_load_dword v126, off, s[20:23], s30 sc1\n"
    "s_waitcnt lgkmcnt(3)\n"
    "v_mfma_f32_16x16x32_bf16 v[64:67], v[168:171], v[16:19], v[64:67]\n"
    "ds_read_b128 v[8:11], v102 offset:768\n"
    "v_mfma_f32_16x16x32_bf16 v[68:71], v[232:235], v[16:19], v[68:71]\n"
    "ds_read_b128 v[12:15], v102 offset:17664\n"
    "s_waitcnt lgkmcnt(4)\n"
    "v_mfma_f32_16x16x32_bf16 v[72:75], v[168:171], v[20:23], v[72:75]\n"
    "v_mfma_f32_16x16x32_bf16 v[76:79], v[232:235], v[20:23], v[76:79]\n"
    "s_waitcnt lgkmcnt(3)\n"
    "v_mfma_f32_16x16x32_bf16 v[64:67], v[172:175], v[0:3], v[64:67]\n"
    "ds_read_b128 v[16:19], v102 offset:832\n"
    "v_mfma_f32_16x16x32_bf16 v[68:71], v[236:239], v[0:3], v[68:71]\n"
    "ds_read_b128 v[20:23], v102 offset:17728\n"
    "s_waitcnt lgkmcnt(4)\n"
    "v_mfma_f32_16x16x32_bf16 v[72:75], v[172:175], v[4:7], v[72:75]\n"
    "v_mfma_f32_16x16x32_bf16 v[76:79], v[236:239], v[4:7], v[76:79]\n"
    "s_waitcnt lgkmcnt(3)\n"
    "v_mfma_f32_16x16x32_bf16 v[64:67], v[176:179], v[8:11], v[64:67]\n"
    "ds_read_b128 v[0:3], v102 offset:896\n"
    "v_mfma_f32_16x16x32_bf16 v[68:71], v[240:243], v[8:11], v[68:71]\n"
    "ds_read_b128 v[4:7], v102 offset:17792\n"
    "s_waitcnt lgkmcnt(4)\n"
    "v_mfma_f32_16x16x32_bf16 v[72:75], v[176:179], v[12:15], v[72:75]\n"
    "v_mfma_f32_16x16x32_bf16 v[76:79], v[240:243], v[12:15], v[76:79]\n"
    "s_waitcnt lgkmcnt(3)\n"
    "v_mfma_f32_16x16x32_bf16 v[64:67], v[180:183], v[16:19], v[64:67]\n"
    "ds_read_b128 v[8:11], v102 offset:960\n"
    "v_mfma_f32_16x16x32_bf16 v[68:71], v[244:247], v[16:19], v[68:71]\n"
    "ds_read_b128 v[12:15], v102 offset:17856\n"
    "s_waitcnt lgkmcnt(4)\n"
    "v_mfma_f32_16x16x32_bf16 v[72:75], v[180:183], v[20:23], v[72:75]\n"
    "v_mfma_f32_16x16x32_bf16 v[76:79], v[244:247], v[20:23], v[76:79]\n"
    "s_waitcnt lgkmcnt(3)\n"
    "v_mfma_f32_16x16x32_bf16 v[64:67], v[184:187], v[0:3], v[64:67]\n"
    "v_mfma_f32_16x16x32_bf16 v[68:71], v[248:251], v[0:3], v[68:71]\n"
    "s_waitcnt lgkmcnt(2)\n"
    "v_mfma_f32_16x16x32_bf16 v[72:75], v[184:187], v[4:7], v[72:75]\n"
    "v_mfma_f32_16x16x32_bf16 v[76:79], v[248:251], v[4:7], v[76:79]\n"
    "s_waitcnt lgkmcnt(1)\n"
    "v_mfma_f32_16x16x32_bf16 v[64:67], v[188:191], v[8:11], v[64:67]\n"
    "v_mfma_f32_16x16x32_bf16 v[68:71], v[252:255], v[8:11], v[68:71]\n"
    "s_waitcnt lgkmcnt(0)\n"
    "v_mfma_f32_16x16x32_bf16 v[72:75], v[188:191], v[12:15], v[72:75]\n"
    "s_nop 2\n"
    "v_mfma_f32_16x16x32_bf16 v[76:79], v[252:255], v[12:15], v[76:79]\n"
    "s_nop 0\n"
    "v_cvt_pk_bf16_f32 v64, v64, v65\n"
    "v_cvt_pk_bf16_f32 v65, v66, v67\n"
    "v_cvt_pk_bf16_f32 v66, v68, v69\n"
    "v_cvt_pk_bf16_f32 v67, v70, v71\n"
    "ds_write_b128 v103, v[64:67] offset:4096\n"
    "v_cvt_pk_bf16_f32 v72, v72, v73\n"
    "v_cvt_pk_bf16_f32 v73, v74, v75\n"
    "v_cvt_pk_bf16_f32 v74, v76, v77\n"
    "v_cvt_pk_bf16_f32 v75, v78, v79\n"
    "ds_write_b128 v103, v[72:75] offset:5120\n"
    "s_add_u32 s32, s32, 2\n"
    "s_xor_b32 s28, s28, 4194304\n"
    "s_waitcnt lgkmcnt(0)\n"
    "s_barrier\n"
    "s_cmp_gt_u32 s29, 30\n"
    "s_cselect_b32 s92, 4, 0\n"
    "s_cbranch_scc1 HP2_%=\n"
    "s_cmp_lg_u32 s91, 0\n"
    "s_cbranch_scc1 HF1_%=c\n"
    "v_cmp_gt_u32 vcc, s48, v125\n"
    "s_cbranch_vccnz HP2_%=\n"
    "v_cmp_gt_u32 vcc, 64, v125\n"
    "s_cmp_eq_u64 vcc, 0\n"
    "s_cselect_b32 s91, 1, 0\n"
    "HF1_%=c:\n"
    "s_lshr_b32 s45, s34, 8\n"
    "s_and_b32 s45, s45, 0x2000\n"
    "s_xor_b32 s90, s34, s45\n"
    "s_mov_b32 m0, s43\n"
    "s_add_u32 s45, s90, 4096\n"
    "buffer_load_dwordx4 v101, s[20:23], s90 offen offset:0 sc1 lds\n"
    "s_add_u32 m0, m0, 32\n"
    "s_nop 0\n"
    "buffer_load_dwordx4 v101, s[20:23], s90 offen offset:1024 sc1 lds\n"
    "s_add_u32 m0, m0, 32\n"
    "s_nop 0\n"
    "buffer_load_dwordx4 v101, s[20:23], s90 offen offset:2048 sc1 lds\n"
    "s_add_u32 m0, m0, 32\n"
    "s_nop 0\n"
    "buffer_load_dwordx4 v101, s[20:23], s90 offen offset:3072 sc1 lds\n"
    "s_add_u32 m0, m0, 4128\n"
    "s_nop 0\n"
    "buffer_load_dwordx4 v101, s[20:23], s45 offen offset:0 sc1 lds\n"
    "s_add_u32 m0, m0, 32\n"
    "s_nop 0\n"
    "buffer_load_dwordx4 v101, s[20:23], s45 offen offset:1024 sc1 lds\n"
    "s_add_u32 m0, m0, 32\n"
    "s_nop 0\n"
    "buffer_load_dwordx4 v101, s[20:23], s45 offen offset:2048 sc1 lds\n"
    "s_add_u32 m0, m0, 32\n"
    "s_nop 0\n"
    "buffer_load_dwordx4 v101, s[20:23], s45 offen offset:3072 sc1 lds\n"
    "s_add_u32 s34, s34, 131072\n"
    "s_add_u32 s48, s48, 2\n"
    "s_or_b32 s92, s92, 2\n"
    "s_mov_b32 s44, 0\n"
    "HP2_%=:\n"
    "v_add_u32 v104, s42, v101\n"
    "v_add_u32 v112, s70, v104\n"
    "v_add_u32 v113, s71, v104\n"
    "s_add_u32 s42, s42, 24576\n"
    "s_cmp_eq_u32 s42, 107520\n"
    "s_cselect_b32 s42, 33792, s42\n"
    "ds_read_b128 v[32:35], v112 offset:4096\n"
    "ds_read_b128 v[0:3], v104 offset:0\n"
    "ds_read_b128 v[16:19], v104 offset:1024\n"
    "ds_read_b128 v[36:39], v112 offset:10240\n"
    "ds_read_b128 v[4:7], v104 offset:6144\n"
    "ds_read_b128 v[20:23], v104 offset:7168\n"
    "ds_read_b128 v[40:43], v112 offset:16384\n"
    "ds_read_b128 v[8:11], v104 offset:12288\n"
    "ds_read_b128 v[24:27], v104 offset:13312\n"
    "ds_read_b128 v[44:47], v112 offset:22528\n"
    "ds_read_b128 v[12:15], v104 offset:18432\n"
    "ds_read_b128 v[28:31], v104 offset:19456\n"
    "v_mov_b64 v[64:65], 0\n"
    "v_mov_b64 v[66:67], 0\n"
    "v_mov_b64 v[68:69], 0\n"
    "v_mov_b64 v[70:71], 0\n"
    "v_mov_b64 v[72:73], 0\n"
    "v_mov_b64 v[74:75], 0\n"
    "v_mov_b64 v[76:77], 0\n"
    "v_mov_b64 v[78:79], 0\n"
    "s_waitcnt lgkmcnt(10)\n"
    "v_mfma_f32_16x16x32_bf16 v[80:83], v[0:3], v[32:35], 0\n"
    "s_waitcnt lgkmcnt(9)\n"
    "v_mfma_f32_16x16x32_bf16 v[84:87], v[16:19], v[32:35], 0\n"
    "ds_read_b128 v[48:51], v113 offset:2048\n"
    "s_waitcnt lgkmcnt(8)\n"
    "v_mfma_f32_16x16x32_bf16 v[80:83], v[4:7], v[36:39], v[80:83]\n"
    "s_waitcnt lgkmcnt(7)\n"
    "v_mfma_f32_16x16x32_bf16 v[84:87], v[20:23], v[36:39], v[84:87]\n"
    "ds_read_b128 v[52:55], v113 offset:3072\n"
    "s_waitcnt lgkmcnt(6)\n"
    "v_mfma_f32_16x16x32_bf16 v[80:83], v[8:11], v[40:43], v[80:83]\n"
    "s_waitcnt lgkmcnt(5)\n"
    "v_mfma_f32_16x16x32_bf16 v[84:87], v[24:27], v[40:43], v[84:87]\n"
    "ds_read_b128 v[56:59], v113 offset:8192\n"
    "s_waitcnt lgkmcnt(4)\n"
    "v_mfma_f32_16x16x32_bf16 v[80:83], v[12:15], v[44:47], v[80:83]\n"
    "s_nop 1\n"
    "s_waitcnt lgkmcnt(3)\n"
    "v_mfma_f32_16x16x32_bf16 v[84:87], v[28:31], v[44:47], v[84:87]\n"
    "ds_read_b128 v[60:63], v113 offset:9216\n"
    "s_nop 2\n"
    "v_cndmask_b32 v80, v110, v80, s[50:51]\n"
    "v_cndmask_b32 v81, v110, v81, s[52:53]\n"
    "v_cndmask_b32 v82, v110, v82, s[54:55]\n"
    "v_cndmask_b32 v83, v110, v83, s[56:57]\n"
    "v_cndmask_b32 v84, v110, v84, s[58:59]\n"
    "v_cndmask_b32 v85, v110, v85, s[60:61]\n"
    "v_cndmask_b32 v86, v110, v86, s[62:63]\n"
    "v_cndmask_b32 v87, v110, v87, s[64:65]\n"
    "v_max3_f32 v109, v80, v81, v82\n"
    "v_max3_f32 v109, v109, v83, v84\n"
    "v_max3_f32 v109, v109, v85, v86\n"
    "v_max_f32 v109, v109, v87\n"
    "v_add_u32 v104, 107520, v101\n"
    "s_lshl_b32 s45, s27, 10\n"
    "v_add_u32 v112, s45, v104\n"
    "s_lshl_b32 s45, s68, 11\n"
    "v_add_u32 v113, s45, v104\n"
    "s_waitcnt vmcnt(8) lgkmcnt(0)\n"
    "s_cmp_eq_u32 s92, 2\n"
    "s_cbranch_scc1 HHc_%=\n"
    "s_waitcnt vmcnt(0)\n"
    "s_cmp_eq_u32 s92, 4\n"
    "s_cbranch_scc1 HHc_%=\n"
    "HL4_%=:\n"
    "HHc_%=:\n"
    "s_mov_b32 s44, 0\n"
    "s_branch HHb_%=\n"
    "HM_%=:\n"
    "s_add_u32 s31, s31, 56\n"
    "s_xor_b32 s36, s36, 1048576\n"
    "s_add_u32 s30, s30, 64\n"
    "s_cmp_lt_u32 s30, s89\n"
    "s_cbranch_scc1 HU_%=\n"
    "s_mov_b32 s30, s89\n"
    "s_bitset0_b32 s36, 20\n"
    "HH_%=:\n"
    "s_sleep 2\n"
    "buffer_load_dword v126, off, s[20:23], s30 sc1\n"
    "s_add_u32 s44, s44, 1\n"
    "s_cmp_gt_u32 s44, 262144\n"
    "s_cbranch_scc1 HEND_%=\n"
    "s_waitcnt vmcnt(0)\n"
    "HHb_%=:\n"
    "v_readfirstlane_b32 s31, v126\n"
    "s_cmp_eq_u32 s31, 0\n"
    "s_cbranch_scc1 HH_%=\n"
    "s_cmp_eq_u32 s30, s89\n"
    "s_cbranch_scc1 HMb_%=\n"
    "s_add_u32 s31, s31, s18\n"
    "HU_%=:\n"
    "s_memrealtime s[94:95]\n"
    "s_waitcnt lgkmcnt(0)\n"
    "s_sub_u32 s45, s94, s31\n"
    "s_cmp_lt_i32 s45, -1\n"
    "s_cbranch_scc1 HU_%=\n"
    "HMb_%=:\n"
    "buffer_load_dwordx4 v[88:91], v101, s[20:23], s36 offen sc1\n"
    "buffer_load_dwordx4 v[92:95], v101, s[20:23], s36 offen offset:1024 sc1\n"
    "s_lshr_b32 s45, s37, 8\n"
    "s_and_b32 s45, s45, 0x2000\n"
    "s_xor_b32 s46, s37, s45\n"
    "s_waitcnt vmcnt(0)\n"
    "v_max_u32 v108, v88, v90\n"
    "ds_write_b128 v124, v[88:91]\n"
    "v_max3_u32 v108, v108, v92, v94\n"
    "ds_write_b128 v124, v[92:95] offset:1024\n"
    "v_cmp_eq_u32 vcc, -1, v108\n"
    "s_cbranch_vccnz HM_%=\n"
    "s_bitset0_b32 s36, 20\n"
    "s_add_u32 s36, s36, 32768\n"
    "s_add_u32 s89, s89, 256\n"
    "s_cmp_lg_u32 s92, 0\n"
    "s_cselect_b32 s45, 0x7f000000, s40\n"
    "buffer_load_dword v125, v107, s[20:23], s45 offen sc1\n"
    "s_waitcnt lgkmcnt(0)\n"
    "s_barrier\n"
    "ds_read_b128 v[0:3], v104 offset:0\n"
    "ds_read_b128 v[4:7], v104 offset:1024\n"
    "ds_read_b128 v[8:11], v104 offset:2048\n"
    "ds_read_b128 v[12:15], v104 offset:3072\n"
    "s_waitcnt lgkmcnt(3)\n"
    "v_smfmac_f32_16x16x64_bf16 v[64:67], v[0:3], a[0:7], v100\n"
    "ds_read_b128 v[16:19], v104 offset:4096\n"
    "v_smfmac_f32_16x16x64_bf16 v[68:71], v[0:3], a[64:71], v100\n"
    "s_waitcnt lgkmcnt(3)\n"
    "v_smfmac_f32_16x16x64_bf16 v[64:67], v[4:7], a[8:15], v100\n"
    "ds_read_b128 v[20:23], v104 offset:5120\n"
    "v_smfmac_f32_16x16x64_bf16 v[68:71], v[4:7], a[72:79], v100\n"
    "s_waitcnt lgkmcnt(3)\n"
    "v_smfmac_f32_16x16x64_bf16 v[64:67], v[8:11], a[16:23], v100\n"
    "ds_read_b128 v[24:27], v104 offset:6144\n"
    "v_smfmac_f32_16x16x64_bf16 v[68:71], v[8:11], a[80:87], v100\n"
    "s_waitcnt lgkmcnt(3)\n"
    "v_smfmac_f32_16x16x64_bf16 v[64:67], v[12:15], a[24:31], v100\n"
    "ds_read_b128 v[28:31], v104 offset:7168\n"
    "v_smfmac_f32_16x16x64_bf16 v[68:71], v[12:15], a[88:95], v100\n"
    "s_waitcnt lgkmcnt(3)\n"
    "v_smfmac_f32_16x16x64_bf16 v[64:67], v[16:19], a[32:39], v100\n"
    "v_smfmac_f32_16x16x64_bf16 v[68:71], v[16:19], a[96:103], v100\n"
    "s_waitcnt lgkmcnt(2)\n"
    "v_smfmac_f32_16x16x64_bf16 v[64:67], v[20:23], a[40:47], v100\n"
    "v_smfmac_f32_16x16x64_bf16 v[68:71], v[20:23], a[104:111], v100\n"
    "s_waitcnt lgkmcnt(1)\n"
    "v_smfmac_f32_16x16x64_bf16 v[64:67], v[24:27], a[48:55], v100\n"
    "v_smfmac_f32_16x16x64_bf16 v[68:71], v[24:27], a[112:119], v100\n"
    "s_waitcnt lgkmcnt(0)\n"
    "v_smfmac_f32_16x16x64_bf16 v[64:67], v[28:31], a[56:63], v100\n"
    "v_smfmac_f32_16x16x64_bf16 v[68:71], v[28:31], a[120:127], v100\n"
    "v_smfmac_f32_16x16x64_bf16 v[72:75], v[0:3], a[128:135], v100\n"
    "s_nop 2\n"
    "v_smfmac_f32_16x16x64_bf16 v[76:79], v[0:3], a[192:199], v100\n"
    "s_nop 1\n"
    "v_cvt_pk_bf16_f32 v64, v64, v65\n"
    "v_smfmac_f32_16x16x64_bf16 v[72:75], v[4:7], a[136:143], v100\n"
    "v_cvt_pk_bf16_f32 v65, v66, v67\n"
    "v_smfmac_f32_16x16x64_bf16 v[76:79], v[4:7], a[200:207], v100\n"
    "v_cvt_pk_bf16_f32 v66, v68, v69\n"
    "v_smfmac_f32_16x16x64_bf16 v[72:75], v[8:11], a[144:151], v100\n"
    "v_cvt_pk_bf16_f32 v67, v70, v71\n"
    "v_smfmac_f32_16x16x64_bf16 v[76:79], v[8:11], a[208:215], v100\n"
    "s_nop 0\n"
    "v_mfma_f32_16x16x32_bf16 v[68:71], v[64:67], v[116:119], 0\n"
    "v_smfmac_f32_16x16x64_bf16 v[72:75], v[12:15], a[152:159], v100\n"
    "v_smfmac_f32_16x16x64_bf16 v[76:79], v[12:15], a[216:223], v100\n"
    "s_nop 2\n"
    "v_smfmac_f32_16x16x64_bf16 v[72:75], v[16:19], a[160:167], v100\n"
    "s_nop 1\n"
    "v_cvt_pk_bf16_f32 v64, v68, v69\n"
    "v_smfmac_f32_16x16x64_bf16 v[76:79], v[16:19], a[224:231], v100\n"
    "v_cvt_pk_bf16_f32 v65, v70, v71\n"
    "v_smfmac_f32_16x16x64_bf16 v[72:75], v[20:23], a[168:175], v100\n"
    "ds_write_b64 v105, v[64:65]\n"
    "v_smfmac_f32_16x16x64_bf16 v[76:79], v[20:23], a[232:239], v100\n"
    "v_smfmac_f32_16x16x64_bf16 v[72:75], v[24:27], a[176:183], v100\n"
    "v_smfmac_f32_16x16x64_bf16 v[76:79], v[24:27], a[240:247], v100\n"
    "v_smfmac_f32_16x16x64_bf16 v[72:75], v[28:31], a[184:191], v100\n"
    "s_nop 2\n"
    "v_smfmac_f32_16x16x64_bf16 v[76:79], v[28:31], a[248:255], v100\n"
    "s_nop 1\n"
    "s_nop 1\n"
    "v_cvt_pk_bf16_f32 v72, v72, v73\n"
    "v_cvt_pk_bf16_f32 v73, v74, v75\n"
    "s_nop 1\n"
    "v_cvt_pk_bf16_f32 v74, v76, v77\n"
    "v_cvt_pk_bf16_f32 v75, v78, v79\n"
    "ds_write_b128 v112, v[72:75] offset:8192\n"
    "s_cmp_lg_u32 s92, 0\n"
    "s_cbranch_scc1 HN3_%=\n"
    "s_waitcnt vmcnt(0)\n"
    "s_cmp_lg_u32 s91, 0\n"
    "s_cbranch_scc1 HF3_%=c\n"
    "v_cmp_gt_u32 vcc, s48, v125\n"
    "s_cbranch_vccnz HN3_%=\n"
    "v_cmp_gt_u32 vcc, 64, v125\n"
    "s_cmp_eq_u64 vcc, 0\n"
    "s_cselect_b32 s91, 1, 0\n"
    "HF3_%=c:\n"
    "s_lshr_b32 s45, s34, 8\n"
    "s_and_b32 s45, s45, 0x2000\n"
    "s_xor_b32 s90, s34, s45\n"
    "s_waitcnt lgkmcnt(0)\n"
    "s_barrier\n"
    "ds_read_b128 v[0:3], v106 offset:0\n"
    "ds_read_b128 v[4:7], v106 offset:512\n"
    "ds_read_b128 v[8:11], v106 offset:1024\n"
    "ds_read_b128 v[12:15], v106 offset:1536\n"
    "ds_read_b128 v[16:19], v113 offset:8192\n"
    "ds_read_b128 v[20:23], v113 offset:9216\n"
    "s_mov_b32 m0, s43\n"
    "s_add_u32 s45, s90, 4096\n"
    "buffer_load_dwordx4 v101, s[20:23], s90 offen offset:0 sc1 lds\n"
    "s_waitcnt lgkmcnt(5)\n"
    "v_mfma_f32_16x16x32_bf16 v[88:91], v[0:3], v[32:35], 0\n"
    "s_waitcnt lgkmcnt(4)\n"
    "v_mfma_f32_16x16x32_bf16 v[88:91], v[4:7], v[36:39], v[88:91]\n"
    "s_waitcnt lgkmcnt(3)\n"
    "v_mfma_f32_16x16x32_bf16 v[88:91], v[8:11], v[40:43], v[88:91]\n"
    "s_waitcnt lgkmcnt(2)\n"
    "v_mfma_f32_16x16x32_bf16 v[88:91], v[12:15], v[44:47], v[88:91]\n"
    "s_nop 0\n"
    "s_nop 6\n"
    "v_max3_f32 v109, v109, v88, v89\n"
    "v_max3_f32 v108, v109, v90, v91\n"
    "v_max3_f32 v109, v109, v90, v91\n"
    "s_nop 1\n"
    "v_permlane16_swap_b32 v109, v108\n"
    "s_add_u32 m0, m0, 32\n"
    "s_nop 0\n"
    "buffer_load_dwordx4 v101, s[20:23], s90 offen offset:1024 sc1 lds\n"
    "v_max_f32 v96, v109, v108\n"
    "v_max_f32 v109, v109, v108\n"
    "s_nop 1\n"
    "v_permlane32_swap_b32 v109, v96\n"
    "v_max_f32 v109, v109, v96\n"
    "v_mul_f32 v109, s47, v109\n"
    "v_fma_f32 v80, v80, s47, -v109\n"
    "v_fma_f32 v81, v81, s47, -v109\n"
    "v_fma_f32 v82, v82, s47, -v109\n"
    "s_add_u32 m0, m0, 32\n"
    "s_nop 0\n"
    "buffer_load_dwordx4 v101, s[20:23], s90 offen offset:2048 sc1 lds\n"
    "v_fma_f32 v83, v83, s47, -v109\n"
    "v_fma_f32 v84, v84, s47, -v109\n"
    "v_fma_f32 v85, v85, s47, -v109\n"
    "v_fma_f32 v86, v86, s47, -v109\n"
    "v_fma_f32 v87, v87, s47, -v109\n"
    "v_fma_f32 v88, v88, s47, -v109\n"
    "v_fma_f32 v89, v89, s47, -v109\n"
    "v_fma_f32 v90, v90, s47, -v109\n"
    "s_add_u32 m0, m0, 32\n"
    "s_nop 0\n"
    "buffer_load_dwordx4 v101, s[20:23], s90 offen offset:3072 sc1 lds\n"
    "v_fma_f32 v91, v91, s47, -v109\n"
    "v_exp_f32 v80, v80\n"
    "v_exp_f32 v81, v81\n"
    "v_exp_f32 v82, v82\n"
    "v_exp_f32 v83, v83\n"
    "v_exp_f32 v84, v84\n"
    "v_exp_f32 v85, v85\n"
    "v_exp_f32 v86, v86\n"
    "s_add_u32 m0, m0, 4128\n"
    "s_nop 0\n"
    "buffer_load_dwordx4 v101, s[20:23], s45 offen offset:0 sc1 lds\n"
    "v_exp_f32 v87, v87\n"
    "v_cvt_pk_bf16_f32 v80, v80, v81\n"
    "v_cvt_pk_bf16_f32 v81, v82, v83\n"
    "v_cvt_pk_bf16_f32 v82, v84, v85\n"
    "v_cvt_pk_bf16_f32 v83, v86, v87\n"
    "s_nop 1\n"
    "v_mfma_f32_16x16x32_bf16 v[96:99], v[120:123], v[80:83], 0\n"
    "v_exp_f32 v88, v88\n"
    "v_mfma_f32_16x16x32_bf16 v[64:67], v[48:51], v[80:83], 0\n"
    "s_add_u32 m0, m0, 32\n"
    "s_nop 0\n"
    "buffer_load_dwordx4 v101, s[20:23], s45 offen offset:1024 sc1 lds\n"
    "v_exp_f32 v89, v89\n"
    "v_mfma_f32_16x16x32_bf16 v[68:71], v[52:55], v[80:83], 0\n"
    "v_exp_f32 v90, v90\n"
    "v_mfma_f32_16x16x32_bf16 v[72:75], v[56:59], v[80:83], 0\n"
    "v_exp_f32 v91, v91\n"
    "v_mfma_f32_16x16x32_bf16 v[76:79], v[60:63], v[80:83], 0\n"
    "v_cvt_pk_bf16_f32 v92, v88, v89\n"
    "v_cvt_pk_bf16_f32 v93, v90, v91\n"
    "s_add_u32 m0, m0, 32\n"
    "s_nop 0\n"
    "buffer_load_dwordx4 v101, s[20:23], s45 offen offset:2048 sc1 lds\n"
    "v_mfma_f32_16x16x16_bf16 v[96:99], v[114:115], v[92:93], v[96:99]\n"
    "s_waitcnt lgkmcnt(1)\n"
    "v_mfma_f32_16x16x16_bf16 v[64:67], v[16:17], v[92:93], v[64:67]\n"
    "v_mfma_f32_16x16x16_bf16 v[68:71], v[18:19], v[92:93], v[68:71]\n"
    "s_waitcnt lgkmcnt(0)\n"
    "v_mfma_f32_16x16x16_bf16 v[72:75], v[20:21], v[92:93], v[72:75]\n"
    "s_nop 0\n"
    "v_mfma_f32_16x16x16_bf16 v[76:79], v[22:23], v[92:93], v[76:79]\n"
    "s_nop 0\n"
    "v_rcp_f32 v96, v96\n"
    "v_mov_b32 v127, s33\n"
    "v_mul_f32 v64, v64, v96\n"
    "s_add_u32 m0, m0, 32\n"
    "s_nop 0\n"
    "buffer_load_dwordx4 v101, s[20:23], s45 offen offset:3072 sc1 lds\n"
    "v_mul_f32 v65, v65, v96\n"
    "v_mul_f32 v66, v66, v96\n"
    "v_mul_f32 v67, v67, v96\n"
    "v_mul_f32 v68, v68, v96\n"
    "v_mul_f32 v69, v69, v96\n"
    "v_mul_f32 v70, v70, v96\n"
    "v_mul_f32 v71, v71, v96\n"
    "v_cvt_pk_bf16_f32 v64, v64, v65\n"
    "v_cvt_pk_bf16_f32 v65, v66, v67\n"
    "v_cvt_pk_bf16_f32 v66, v68, v69\n"
    "v_cvt_pk_bf16_f32 v67, v70, v71\n"
    "v_mul_f32 v72, v72, v96\n"
    "v_mul_f32 v73, v73, v96\n"
    "v_mul_f32 v74, v74, v96\n"
    "v_mul_f32 v75, v75, v96\n"
    "v_mul_f32 v76, v76, v96\n"
    "v_mul_f32 v77, v77, v96\n"
    "v_mul_f32 v78, v78, v96\n"
    "v_mul_f32 v79, v79, v96\n"
    "v_cvt_pk_bf16_f32 v72, v72, v73\n"
    "v_cvt_pk_bf16_f32 v73, v74, v75\n"
    "v_cvt_pk_bf16_f32 v74, v76, v77\n"
    "v_cvt_pk_bf16_f32 v75, v78, v79\n"
    "s_add_u32 s34, s34, 131072\n"
    "s_add_u32 s48, s48, 2\n"
    "s_or_b32 s92, s92, 2\n"
    "s_mov_b32 s44, 0\n"
    "s_mov_b32 s67, s46\n"
    "s_mov_b32 s46, s69\n"
    "s_branch HJ3_%=\n"
    "HN3_%=:\n"
    "s_cmp_lg_u32 s92, 0\n"
    "s_cselect_b32 s45, 0x7f000000, s40\n"
    "buffer_load_dword v125, v107, s[20:23], s45 offen sc1\n"
    "s_waitcnt lgkmcnt(0)\n"
    "s_barrier\n"
    "ds_read_b128 v[0:3], v106 offset:0\n"
    "ds_read_b128 v[4:7], v106 offset:512\n"
    "ds_read_b128 v[8:11], v106 offset:1024\n"
    "ds_read_b128 v[12:15], v106 offset:1536\n"
    "ds_read_b128 v[16:19], v113 offset:8192\n"
    "ds_read_b128 v[20:23], v113 offset:9216\n"
    "s_waitcnt lgkmcnt(5)\n"
    "v_mfma_f32_16x16x32_bf16 v[88:91], v[0:3], v[32:35], 0\n"
    "s_waitcnt lgkmcnt(4)\n"
    "v_mfma_f32_16x16x32_bf16 v[88:91], v[4:7], v[36:39], v[88:91]\n"
    "s_waitcnt lgkmcnt(3)\n"
    "v_mfma_f32_16x16x32_bf16 v[88:91], v[8:11], v[40:43], v[88:91]\n"
    "s_waitcnt lgkmcnt(2)\n"
    "v_mfma_f32_16x16x32_bf16 v[88:91], v[12:15], v[44:47], v[88:91]\n"
    "s_nop 0\n"
    "s_nop 6\n"
    "v_max3_f32 v109, v109, v88, v89\n"
    "v_max3_f32 v108, v109, v90, v91\n"
    "v_max3_f32 v109, v109, v90, v91\n"
    "s_nop 1\n"
    "v_permlane16_swap_b32 v109, v108\n"
    "v_max_f32 v96, v109, v108\n"
    "v_max_f32 v109, v109, v108\n"
    "s_nop 1\n"
    "v_permlane32_swap_b32 v109, v96\n"
    "v_max_f32 v109, v109, v96\n"
    "v_mul_f32 v109, s47, v109\n"
    "v_fma_f32 v80, v80, s47, -v109\n"
    "v_fma_f32 v81, v81, s47, -v109\n"
    "v_fma_f32 v82, v82, s47, -v109\n"
    "v_fma_f32 v83, v83, s47, -v109\n"
    "v_fma_f32 v84, v84, s47, -v109\n"
    "v_fma_f32 v85, v85, s47, -v109\n"
    "v_fma_f32 v86, v86, s47, -v109\n"
    "v_fma_f32 v87, v87, s47, -v109\n"
    "v_fma_f32 v88, v88, s47, -v109\n"
    "v_fma_f32 v89, v89, s47, -v109\n"
    "v_fma_f32 v90, v90, s47, -v109\n"
    "v_fma_f32 v91, v91, s47, -v109\n"
    "v_exp_f32 v80, v80\n"
    "v_exp_f32 v81, v81\n"
    "v_exp_f32 v82, v82\n"
    "v_exp_f32 v83, v83\n"
    "v_exp_f32 v84, v84\n"
    "v_exp_f32 v85, v85\n"
    "v_exp_f32 v86, v86\n"
    "v_exp_f32 v87, v87\n"
    "v_cvt_pk_bf16_f32 v80, v80, v81\n"
    "v_cvt_pk_bf16_f32 v81, v82, v83\n"
    "v_cvt_pk_bf16_f32 v82, v84, v85\n"
    "v_cvt_pk_bf16_f32 v83, v86, v87\n"
    "s_nop 1\n"
    "v_mfma_f32_16x16x32_bf16 v[96:99], v[120:123], v[80:83], 0\n"
    "v_exp_f32 v88, v88\n"
    "v_mfma_f32_16x16x32_bf16 v[64:67], v[48:51], v[80:83], 0\n"
    "v_exp_f32 v89, v89\n"
    "v_mfma_f32_16x16x32_bf16 v[68:71], v[52:55], v[80:83], 0\n"
    "v_exp_f32 v90, v90\n"
    "v_mfma_f32_16x16x32_bf16 v[72:75], v[56:59], v[80:83], 0\n"
    "v_exp_f32 v91, v91\n"
    "v_mfma_f32_16x16x32_bf16 v[76:79], v[60:63], v[80:83], 0\n"
    "v_cvt_pk_bf16_f32 v92, v88, v89\n"
    "v_cvt_pk_bf16_f32 v93, v90, v91\n"
    "s_nop 1\n"
    "v_mfma_f32_16x16x16_bf16 v[96:99], v[114:115], v[92:93], v[96:99]\n"
    "s_waitcnt lgkmcnt(1)\n"
    "v_mfma_f32_16x16x16_bf16 v[64:67], v[16:17], v[92:93], v[64:67]\n"
    "v_mfma_f32_16x16x16_bf16 v[68:71], v[18:19], v[92:93], v[68:71]\n"
    "s_waitcnt lgkmcnt(0)\n"
    "v_mfma_f32_16x16x16_bf16 v[72:75], v[20:21], v[92:93], v[72:75]\n"
    "s_nop 0\n"
    "v_mfma_f32_16x16x16_bf16 v[76:79], v[22:23], v[92:93], v[76:79]\n"
    "s_nop 0\n"
    "v_rcp_f32 v96, v96\n"
    "v_mov_b32 v127, s33\n"
    "v_mul_f32 v64, v64, v96\n"
    "v_mul_f32 v65, v65, v96\n"
    "v_mul_f32 v66, v66, v96\n"
    "v_mul_f32 v67, v67, v96\n"
    "v_mul_f32 v68, v68, v96\n"
    "v_mul_f32 v69, v69, v96\n"
    "v_mul_f32 v70, v70, v96\n"
    "v_mul_f32 v71, v71, v96\n"
    "v_cvt_pk_bf16_f32 v64, v64, v65\n"
    "v_cvt_pk_bf16_f32 v65, v66, v67\n"
    "v_cvt_pk_bf16_f32 v66, v68, v69\n"
    "v_cvt_pk_bf16_f32 v67, v70, v71\n"
    "v_mul_f32 v72, v72, v96\n"
    "v_mul_f32 v73, v73, v96\n"
    "v_mul_f32 v74, v74, v96\n"
    "v_mul_f32 v75, v75, v96\n"
    "v_mul_f32 v76, v76, v96\n"
    "v_mul_f32 v77, v77, v96\n"
    "v_mul_f32 v78, v78, v96\n"
    "v_mul_f32 v79, v79, v96\n"
    "v_cvt_pk_bf16_f32 v72, v72, v73\n"
    "v_cvt_pk_bf16_f32 v73, v74, v75\n"
    "v_cvt_pk_bf16_f32 v74, v76, v77\n"
    "v_cvt_pk_bf16_f32 v75, v78, v79\n"
    "s_cmp_lg_u32 s92, 0\n"
    "s_cbranch_scc1 HJ3_%=\n"
    "s_waitcnt vmcnt(0)\n"
    "s_cmp_lg_u32 s91, 0\n"
    "s_cbranch_scc1 HF5_%=c\n"
    "v_cmp_gt_u32 vcc, s48, v125\n"
    "s_cbranch_vccnz HJ3_%=\n"
    "v_cmp_gt_u32 vcc, 64, v125\n"
    "s_cmp_eq_u64 vcc, 0\n"
    "s_cselect_b32 s91, 1, 0\n"
    "HF5_%=c:\n"
    "s_lshr_b32 s45, s34, 8\n"
    "s_and_b32 s45, s45, 0x2000\n"
    "s_xor_b32 s90, s34, s45\n"
    "s_mov_b32 m0, s43\n"
    "s_add_u32 s45, s90, 4096\n"
    "buffer_load_dwordx4 v101, s[20:23], s90 offen offset:0 sc1 lds\n"
    "s_add_u32 m0, m0, 32\n"
    "s_nop 0\n"
    "buffer_load_dwordx4 v101, s[20:23], s90 offen offset:1024 sc1 lds\n"
    "s_add_u32 m0, m0, 32\n"
    "s_nop 0\n"
    "buffer_load_dwordx4 v101, s[20:23], s90 offen offset:2048 sc1 lds\n"
    "s_add_u32 m0, m0, 32\n"
    "s_nop 0\n"
    "buffer_load_dwordx4 v101, s[20:23], s90 offen offset:3072 sc1 lds\n"
    "s_add_u32 m0, m0, 4128\n"
    "s_nop 0\n"
    "buffer_load_dwordx4 v101, s[20:23], s45 offen offset:0 sc1 lds\n"
    "s_add_u32 m0, m0, 32\n"
    "s_nop 0\n"
    "buffer_load_dwordx4 v101, s[20:23], s45 offen offset:1024 sc1 lds\n"
    "s_add_u32 m0, m0, 32\n"
    "s_nop 0\n"
    "buffer_load_dwordx4 v101, s[20:23], s45 offen offset:2048 sc1 lds\n"
    "s_add_u32 m0, m0, 32\n"
    "s_nop 0\n"
    "buffer_load_dwordx4 v101, s[20:23], s45 offen offset:3072 sc1 lds\n"
    "s_add_u32 s34, s34, 131072\n"
    "s_add_u32 s48, s48, 2\n"
    "s_or_b32 s92, s92, 2\n"
    "s_mov_b32 s44, 0\n"
    "buffer_store_dwordx4 v[64:67], v101, s[20:23], s46 offen offset:0 sc1\n"
    "buffer_store_dwordx4 v[72:75], v101, s[20:23], s46 offen offset:2048 sc1\n"
    "s_mov_b32 s46, 0x7f000000\n"
    "HJ3_%=:\n"
    "s_add_u32 s37, s37, 1048576\n"
    "s_cmp_ge_u32 s37, 58720256\n"
    "s_cselect_b32 s45, 33554432, 0\n"
    "s_sub_u32 s37, s37, s45\n"
    "s_add_u32 s33, s33, 2\n"
    "s_bitcmp1_b32 s92, 1\n"
    "s_cselect_b32 s93, s39, s93\n"
    "s_cselect_b32 s69, s46, s69\n"
    "s_cbranch_scc1 HN2_%=\n"
    "buffer_store_dwordx4 v[64:67], v101, s[20:23], s46 offen offset:0 sc1\n"
    "buffer_store_dwordx4 v[72:75], v101, s[20:23], s46 offen offset:2048 sc1\n"
    "buffer_load_dword v125, v107, s[20:23], s40 offen sc1\n"
    "s_waitcnt vmcnt(0)\n"
    "s_mov_b64 exec, 1\n"
    "buffer_store_dword v127, off, s[20:23], s39 sc1\n"
    "s_mov_b64 exec, -1\n"
    "s_cmp_lg_u32 s92, 0\n"
    "s_cbranch_scc1 HN2_%=\n"
    "s_cmp_lg_u32 s91, 0\n"
    "s_cbranch_scc1 HF2_%=c\n"
    "v_cmp_gt_u32 vcc, s48, v125\n"
    "s_cbranch_vccz HF2_%=d\n"
    "s_mov_b32 s44, 0\n"
    "s_branch HF2_%=b\n"
    "HF2_%=:\n"
    "s_sleep 1\n"
    "s_add_u32 s44, s44, 1\n"
    "s_cmp_gt_u32 s44, 262144\n"
    "s_cbranch_scc1 HEND_%=\n"
    "HF2_%=b:\n"
    "buffer_load_dword v125, v107, s[20:23], s40 offen sc1\n"
    "s_waitcnt vmcnt(0)\n"
    "v_cmp_gt_u32 vcc, s48, v125\n"
    "s_cbranch_vccnz HF2_%=\n"
    "s_cmp_eq_u32 s24, 0\n"
    "s_cbranch_scc1 HF2_%=d\n"
    "s_sleep 6\n"
    "HF2_%=d:\n"
    "v_cmp_gt_u32 vcc, 64, v125\n"
    "s_cmp_eq_u64 vcc, 0\n"
    "s_cselect_b32 s91, 1, 0\n"
    "HF2_%=c:\n"
    "s_lshr_b32 s45, s34, 8\n"
    "s_and_b32 s45, s45, 0x2000\n"
    "s_xor_b32 s90, s34, s45\n"
    "s_mov_b32 m0, s43\n"
    "s_add_u32 s45, s90, 4096\n"
    "buffer_load_dwordx4 v101, s[20:23], s90 offen offset:0 sc1 lds\n"
    "s_add_u32 m0, m0, 32\n"
    "s_nop 0\n"
    "buffer_load_dwordx4 v101, s[20:23], s90 offen offset:1024 sc1 lds\n"
    "s_add_u32 m0, m0, 32\n"
    "s_nop 0\n"
    "buffer_load_dwordx4 v101, s[20:23], s90 offen offset:2048 sc1 lds\n"
    "s_add_u32 m0, m0, 32\n"
    "s_nop 0\n"
    "buffer_load_dwordx4 v101, s[20:23], s90 offen offset:3072 sc1 lds\n"
    "s_add_u32 m0, m0, 4128\n"
    "s_nop 0\n"
    "buffer_load_dwordx4 v101, s[20:23], s45 offen offset:0 sc1 lds\n"
    "s_add_u32 m0, m0, 32\n"
    "s_nop 0\n"
    "buffer_load_dwordx4 v101, s[20:23], s45 offen offset:1024 sc1 lds\n"
    "s_add_u32 m0, m0, 32\n"
    "s_nop 0\n"
    "buffer_load_dwordx4 v101, s[20:23], s45 offen offset:2048 sc1 lds\n"
    "s_add_u32 m0, m0, 32\n"
    "s_nop 0\n"
    "buffer_load_dwordx4 v101, s[20:23], s45 offen offset:3072 sc1 lds\n"
    "s_add_u32 s34, s34, 131072\n"
    "s_add_u32 s48, s48, 2\n"
    "s_or_b32 s92, s92, 2\n"
    "s_mov_b32 s44, 0\n"
    "HN2_%=:\n"
    "s_add_u32 s29, s29, 1\n"
    "s_cmp_lt_u32 s29, 32\n"
    "s_cbranch_scc1 HLP_%=\n"
    "s_branch HEND_%=\n"
    "HRT_%=:\n"
    "s_waitcnt lgkmcnt(0)\n"
    "s_barrier\n"
    "s_add_u32 s44, s44, 1\n"
    "s_cmp_gt_u32 s44, 262144\n"
    "s_cbranch_scc1 HEND_%=\n"
    "s_mov_b32 m0, s43\n"
    "s_add_u32 s45, s90, 4096\n"
    "buffer_load_dwordx4 v101, s[20:23], s90 offen offset:0 sc1 lds\n"
    "s_add_u32 m0, m0, 32\n"
    "s_nop 0\n"
    "buffer_load_dwordx4 v101, s[20:23], s90 offen offset:1024 sc1 lds\n"
    "s_add_u32 m0, m0, 32\n"
    "s_nop 0\n"
    "buffer_load_dwordx4 v101, s[20:23], s90 offen offset:2048 sc1 lds\n"
    "s_add_u32 m0, m0, 32\n"
    "s_nop 0\n"
    "buffer_load_dwordx4 v101, s[20:23], s90 offen offset:3072 sc1 lds\n"
    "s_add_u32 m0, m0, 4128\n"
    "s_nop 0\n"
    "buffer_load_dwordx4 v101, s[20:23], s45 offen offset:0 sc1 lds\n"
    "s_add_u32 m0, m0, 32\n"
    "s_nop 0\n"
    "buffer_load_dwordx4 v101, s[20:23], s45 offen offset:1024 sc1 lds\n"
    "s_add_u32 m0, m0, 32\n"
    "s_nop 0\n"
    "buffer_load_dwordx4 v101, s[20:23], s45 offen offset:2048 sc1 lds\n"
    "s_add_u32 m0, m0, 32\n"
    "s_nop 0\n"
    "buffer_load_dwordx4 v101, s[20:23], s45 offen offset:3072 sc1 lds\n"
    "s_branch HR1_%=\n"
    "HEND_%=:\n"
    "s_endpgm\n"
    "ROLE_O_%=:\n"
    "s_load_dwordx16 s[4:19], %0, 0\n"
    "s_waitcnt lgkmcnt(0)\n"
    "s_mov_b32 s20, s12\n"
    "s_and_b32 s21, s13, 0xffff\n"
    "s_mov_b32 s22, 85983232\n"
    "s_mov_b32 s23, 0x20000\n"
    "s_bfe_u32 s24, %1, 0x10000\n"
    "s_bfe_u32 s25, %1, 0x80008\n"
    "s_bfe_u32 s26, %1, 0x20001\n"
    "s_bfe_u32 s27, %1, 0x80010\n"
    "s_mov_b32 s28, s19\n"
    "s_mov_b32 s44, s14\n"
    "s_and_b32 s45, s15, 0xffff\n"
    "s_mov_b32 s46, 2097152\n"
    "s_mov_b32 s47, 0x20000\n"
    "v_mbcnt_lo_u32_b32 v0, -1, 0\n"
    "v_mbcnt_hi_u32_b32 v0, -1, v0\n"
    "v_lshlrev_b32 v230, 4, v0\n"
    "v_lshlrev_b32 v231, 2, v0\n"
    "s_lshr_b32 s52, s25, 2\n"
    "s_lshl_b32 s52, s52, 19\n"
    "s_and_b32 s40, s25, 3\n"
    "s_lshl_b32 s40, s40, 8\n"
    "s_add_u32 s52, s52, s40\n"
    "s_lshl_b32 s40, s26, 14\n"
    "s_add_u32 s52, s52, s40\n"
    "s_lshl_b32 s53, s26, 12\n"
    "v_lshrrev_b32 v2, 4, v0\n"
    "v_and_b32 v1, 15, v0\n"
    "v_lshlrev_b32 v3, 2, v2\n"
    "v_xor_b32 v3, v3, v1\n"
    "v_lshlrev_b32 v3, 4, v3\n"
    "v_lshl_add_u32 v22, v2, 10, v3\n"
    "s_lshr_b32 s40, s26, 1\n"
    "s_lshl_b32 s40, s40, 7\n"
    "v_lshlrev_b32 v3, 4, v1\n"
    "v_xor_b32 v3, s40, v3\n"
    "v_lshrrev_b32 v4, 2, v0\n"
    "v_lshl_add_u32 v24, v4, 8, v3\n"
    "v_xor_b32 v25, 64, v24\n"
    "s_getpc_b64 s[94:95]\n"
    "OCA_%=:\n"
    "v_lshlrev_b32 v30, 7, v0\n"
    "v_min_u32 v30, .Lcode_end-OCA_%=-4, v30\n"
    "global_load_dword v30, v30, s[94:95]\n"
    "s_barrier\n"
    "s_and_b32 s61, s27, 7\n"
    "s_lshl_b32 s61, s61, 7\n"
    "s_add_u32 s61, s61, 81788928\n"
    "buffer_load_dwordx2 v[20:21], off, s[20:23], s61 sc1\n"
    "v_lshrrev_b32 v26, 2, v0\n"
    "v_and_b32 v27, 3, v0\n"
    "v_lshl_add_u32 v26, v26, 3, v27\n"
    "v_lshlrev_b32 v26, 7, v26\n"
    "s_mov_b32 s40, 81793536\n"
    "buffer_load_dword v27, v26, s[20:23], s40 offen sc1\n"
    "s_mov_b32 s40, 81809920\n"
    "buffer_load_dword v28, v26, s[20:23], s40 offen sc1\n"
    "s_cmp_eq_u32 s24, 0\n"
    "s_cbranch_scc1 OW_%=\n"
    "s_mov_b64 s[76:77], s[10:11]\n"
    "s_and_b32 s77, s77, 0xffff\n"
    "s_mov_b32 s78, 4194304\n"
    "s_mov_b32 s79, 0x20000\n"
    "s_add_u32 m0, s53, 0\n"
    "s_add_u32 s80, s52, 0\n"
    "buffer_load_dwordx4 v22, s[76:79], s80 offen offset:0  lds\n"
    "s_add_u32 s80, s52, 3072\n"
    "buffer_load_dwordx4 v22, s[76:79], s80 offen offset:1024  lds\n"
    "s_add_u32 s80, s52, 6144\n"
    "buffer_load_dwordx4 v22, s[76:79], s80 offen offset:2048  lds\n"
    "s_add_u32 s80, s52, 9216\n"
    "buffer_load_dwordx4 v22, s[76:79], s80 offen offset:3072  lds\n"
    "s_add_u32 m0, s53, 16384\n"
    "s_add_u32 s80, s52, 65536\n"
    "buffer_load_dwordx4 v22, s[76:79], s80 offen offset:0  lds\n"
    "s_add_u32 s80, s52, 68608\n"
    "buffer_load_dwordx4 v22, s[76:79], s80 offen offset:1024  lds\n"
    "s_add_u32 s80, s52, 71680\n"
    "buffer_load_dwordx4 v22, s[76:79], s80 offen offset:2048  lds\n"
    "s_add_u32 s80, s52, 74752\n"
    "buffer_load_dwordx4 v22, s[76:79], s80 offen offset:3072  lds\n"
    "s_add_u32 m0, s53, 32768\n"
    "s_add_u32 s80, s52, 131072\n"
    "buffer_load_dwordx4 v22, s[76:79], s80 offen offset:0  lds\n"
    "s_add_u32 s80, s52, 134144\n"
    "buffer_load_dwordx4 v22, s[76:79], s80 offen offset:1024  lds\n"
    "s_add_u32 s80, s52, 137216\n"
    "buffer_load_dwordx4 v22, s[76:79], s80 offen offset:2048  lds\n"
    "s_add_u32 s80, s52, 140288\n"
    "buffer_load_dwordx4 v22, s[76:79], s80 offen offset:3072  lds\n"
    "s_add_u32 m0, s53, 49152\n"
    "s_add_u32 s80, s52, 196608\n"
    "buffer_load_dwordx4 v22, s[76:79], s80 offen offset:0  lds\n"
    "s_add_u32 s80, s52, 199680\n"
    "buffer_load_dwordx4 v22, s[76:79], s80 offen offset:1024  lds\n"
    "s_add_u32 s80, s52, 202752\n"
    "buffer_load_dwordx4 v22, s[76:79], s80 offen offset:2048  lds\n"
    "s_add_u32 s80, s52, 205824\n"
    "buffer_load_dwordx4 v22, s[76:79], s80 offen offset:3072  lds\n"
    "s_add_u32 m0, s53, 65536\n"
    "s_add_u32 s80, s52, 262144\n"
    "buffer_load_dwordx4 v22, s[76:79], s80 offen offset:0  lds\n"
    "s_add_u32 s80, s52, 265216\n"
    "buffer_load_dwordx4 v22, s[76:79], s80 offen offset:1024  lds\n"
    "s_add_u32 s80, s52, 268288\n"
    "buffer_load_dwordx4 v22, s[76:79], s80 offen offset:2048  lds\n"
    "s_add_u32 s80, s52, 271360\n"
    "buffer_load_dwordx4 v22, s[76:79], s80 offen offset:3072  lds\n"
    "s_add_u32 m0, s53, 81920\n"
    "s_add_u32 s80, s52, 327680\n"
    "buffer_load_dwordx4 v22, s[76:79], s80 offen offset:0  lds\n"
    "s_add_u32 s80, s52, 330752\n"
    "buffer_load_dwordx4 v22, s[76:79], s80 offen offset:1024  lds\n"
    "s_add_u32 s80, s52, 333824\n"
    "buffer_load_dwordx4 v22, s[76:79], s80 offen offset:2048  lds\n"
    "s_add_u32 s80, s52, 336896\n"
    "buffer_load_dwordx4 v22, s[76:79], s80 offen offset:3072  lds\n"
    "s_add_u32 m0, s53, 98304\n"
    "s_add_u32 s80, s52, 393216\n"
    "buffer_load_dwordx4 v22, s[76:79], s80 offen offset:0  lds\n"
    "s_add_u32 s80, s52, 396288\n"
    "buffer_load_dwordx4 v22, s[76:79], s80 offen offset:1024  lds\n"
    "s_add_u32 s80, s52, 399360\n"
    "buffer_load_dwordx4 v22, s[76:79], s80 offen offset:2048  lds\n"
    "s_add_u32 s80, s52, 402432\n"
    "buffer_load_dwordx4 v22, s[76:79], s80 offen offset:3072  lds\n"
    "s_add_u32 m0, s53, 114688\n"
    "s_add_u32 s80, s52, 458752\n"
    "buffer_load_dwordx4 v22, s[76:79], s80 offen offset:0  lds\n"
    "s_add_u32 s80, s52, 461824\n"
    "buffer_load_dwordx4 v22, s[76:79], s80 offen offset:1024  lds\n"
    "s_add_u32 s80, s52, 464896\n"
    "buffer_load_dwordx4 v22, s[76:79], s80 offen offset:2048  lds\n"
    "s_add_u32 s80, s52, 467968\n"
    "buffer_load_dwordx4 v22, s[76:79], s80 offen offset:3072  lds\n"
    "OW_%=:\n"
    "s_waitcnt vmcnt(28) lgkmcnt(0)\n"
    "s_barrier\n"
    "ds_read_b64_tr_b16 a[0:1], v24 offset:0\n"
    "ds_read_b64_tr_b16 a[4:5], v25 offset:0\n"
    "ds_read_b64_tr_b16 a[8:9], v24 offset:8\n"
    "ds_read_b64_tr_b16 a[12:13], v25 offset:8\n"
    "ds_read_b64_tr_b16 a[2:3], v24 offset:4096\n"
    "ds_read_b64_tr_b16 a[6:7], v25 offset:4096\n"
    "ds_read_b64_tr_b16 a[10:11], v24 offset:4104\n"
    "ds_read_b64_tr_b16 a[14:15], v25 offset:4104\n"
    "ds_read_b64_tr_b16 a[16:17], v24 offset:8192\n"
    "ds_read_b64_tr_b16 a[20:21], v25 offset:8192\n"
    "ds_read_b64_tr_b16 a[24:25], v24 offset:8200\n"
    "ds_read_b64_tr_b16 a[28:29], v25 offset:8200\n"
    "ds_read_b64_tr_b16 a[18:19], v24 offset:12288\n"
    "ds_read_b64_tr_b16 a[22:23], v25 offset:12288\n"
    "ds_read_b64_tr_b16 a[26:27], v24 offset:12296\n"
    "ds_read_b64_tr_b16 a[30:31], v25 offset:12296\n"
    "s_waitcnt vmcnt(24) lgkmcnt(0)\n"
    "s_barrier\n"
    "ds_read_b64_tr_b16 a[32:33], v24 offset:16384\n"
    "ds_read_b64_tr_b16 a[36:37], v25 offset:16384\n"
    "ds_read_b64_tr_b16 a[40:41], v24 offset:16392\n"
    "ds_read_b64_tr_b16 a[44:45], v25 offset:16392\n"
    "ds_read_b64_tr_b16 a[34:35], v24 offset:20480\n"
    "ds_read_b64_tr_b16 a[38:39], v25 offset:20480\n"
    "ds_read_b64_tr_b16 a[42:43], v24 offset:20488\n"
    "ds_read_b64_tr_b16 a[46:47], v25 offset:20488\n"
    "ds_read_b64_tr_b16 a[48:49], v24 offset:24576\n"
    "ds_read_b64_tr_b16 a[52:53], v25 offset:24576\n"
    "ds_read_b64_tr_b16 a[56:57], v24 offset:24584\n"
    "ds_read_b64_tr_b16 a[60:61], v25 offset:24584\n"
    "ds_read_b64_tr_b16 a[50:51], v24 offset:28672\n"
    "ds_read_b64_tr_b16 a[54:55], v25 offset:28672\n"
    "ds_read_b64_tr_b16 a[58:59], v24 offset:28680\n"
    "ds_read_b64_tr_b16 a[62:63], v25 offset:28680\n"
    "s_waitcnt vmcnt(20) lgkmcnt(0)\n"
    "s_barrier\n"
    "ds_read_b64_tr_b16 a[64:65], v24 offset:32768\n"
    "ds_read_b64_tr_b16 a[68:69], v25 offset:32768\n"
    "ds_read_b64_tr_b16 a[72:73], v24 offset:32776\n"
    "ds_read_b64_tr_b16 a[76:77], v25 offset:32776\n"
    "ds_read_b64_tr_b16 a[66:67], v24 offset:36864\n"
    "ds_read_b64_tr_b16 a[70:71], v25 offset:36864\n"
    "ds_read_b64_tr_b16 a[74:75], v24 offset:36872\n"
    "ds_read_b64_tr_b16 a[78:79], v25 offset:36872\n"
    "ds_read_b64_tr_b16 a[80:81], v24 offset:40960\n"
    "ds_read_b64_tr_b16 a[84:85], v25 offset:40960\n"
    "ds_read_b64_tr_b16 a[88:89], v24 offset:40968\n"
    "ds_read_b64_tr_b16 a[92:93], v25 offset:40968\n"
    "ds_read_b64_tr_b16 a[82:83], v24 offset:45056\n"
    "ds_read_b64_tr_b16 a[86:87], v25 offset:45056\n"
    "ds_read_b64_tr_b16 a[90:91], v24 offset:45064\n"
    "ds_read_b64_tr_b16 a[94:95], v25 offset:45064\n"
    "s_waitcnt vmcnt(16) lgkmcnt(0)\n"
    "s_barrier\n"
    "ds_read_b64_tr_b16 a[96:97], v24 offset:49152\n"
    "ds_read_b64_tr_b16 a[100:101], v25 offset:49152\n"
    "ds_read_b64_tr_b16 a[104:105], v24 offset:49160\n"
    "ds_read_b64_tr_b16 a[108:109], v25 offset:49160\n"
    "ds_read_b64_tr_b16 a[98:99], v24 offset:53248\n"
    "ds_read_b64_tr_b16 a[102:103], v25 offset:53248\n"
    "ds_read_b64_tr_b16 a[106:107], v24 offset:53256\n"
    "ds_read_b64_tr_b16 a[110:111], v25 offset:53256\n"
    "ds_read_b64_tr_b16 a[112:113], v24 offset:57344\n"
    "ds_read_b64_tr_b16 a[116:117], v25 offset:57344\n"
    "ds_read_b64_tr_b16 a[120:121], v24 offset:57352\n"
    "ds_read_b64_tr_b16 a[124:125], v25 offset:57352\n"
    "ds_read_b64_tr_b16 a[114:115], v24 offset:61440\n"
    "ds_read_b64_tr_b16 a[118:119], v25 offset:61440\n"
    "ds_read_b64_tr_b16 a[122:123], v24 offset:61448\n"
    "ds_read_b64_tr_b16 a[126:127], v25 offset:61448\n"
    "s_waitcnt vmcnt(12) lgkmcnt(0)\n"
    "s_barrier\n"
    "v_add_u32 v24, 65536, v24\n"
    "v_add_u32 v25, 65536, v25\n"
    "ds_read_b64_tr_b16 a[128:129], v24 offset:0\n"
    "ds_read_b64_tr_b16 a[132:133], v25 offset:0\n"
    "ds_read_b64_tr_b16 a[136:137], v24 offset:8\n"
    "ds_read_b64_tr_b16 a[140:141], v25 offset:8\n"
    "ds_read_b64_tr_b16 a[130:131], v24 offset:4096\n"
    "ds_read_b64_tr_b16 a[134:135], v25 offset:4096\n"
    "ds_read_b64_tr_b16 a[138:139], v24 offset:4104\n"
    "ds_read_b64_tr_b16 a[142:143], v25 offset:4104\n"
    "ds_read_b64_tr_b16 a[144:145], v24 offset:8192\n"
    "ds_read_b64_tr_b16 a[148:149], v25 offset:8192\n"
    "ds_read_b64_tr_b16 a[152:153], v24 offset:8200\n"
    "ds_read_b64_tr_b16 a[156:157], v25 offset:8200\n"
    "ds_read_b64_tr_b16 a[146:147], v24 offset:12288\n"
    "ds_read_b64_tr_b16 a[150:151], v25 offset:12288\n"
    "ds_read_b64_tr_b16 a[154:155], v24 offset:12296\n"
    "ds_read_b64_tr_b16 a[158:159], v25 offset:12296\n"
    "s_waitcnt vmcnt(8) lgkmcnt(0)\n"
    "s_barrier\n"
    "ds_read_b64_tr_b16 a[160:161], v24 offset:16384\n"
    "ds_read_b64_tr_b16 a[164:165], v25 offset:16384\n"
    "ds_read_b64_tr_b16 a[168:169], v24 offset:16392\n"
    "ds_read_b64_tr_b16 a[172:173], v25 offset:16392\n"
    "ds_read_b64_tr_b16 a[162:163], v24 offset:20480\n"
    "ds_read_b64_tr_b16 a[166:167], v25 offset:20480\n"
    "ds_read_b64_tr_b16 a[170:171], v24 offset:20488\n"
    "ds_read_b64_tr_b16 a[174:175], v25 offset:20488\n"
    "ds_read_b64_tr_b16 a[176:177], v24 offset:24576\n"
    "ds_read_b64_tr_b16 a[180:181], v25 offset:24576\n"
    "ds_read_b64_tr_b16 a[184:185], v24 offset:24584\n"
    "ds_read_b64_tr_b16 a[188:189], v25 offset:24584\n"
    "ds_read_b64_tr_b16 a[178:179], v24 offset:28672\n"
    "ds_read_b64_tr_b16 a[182:183], v25 offset:28672\n"
    "ds_read_b64_tr_b16 a[186:187], v24 offset:28680\n"
    "ds_read_b64_tr_b16 a[190:191], v25 offset:28680\n"
    "s_waitcnt vmcnt(4) lgkmcnt(0)\n"
    "s_barrier\n"
    "ds_read_b64_tr_b16 a[192:193], v24 offset:32768\n"
    "ds_read_b64_tr_b16 a[196:197], v25 offset:32768\n"
    "ds_read_b64_tr_b16 a[200:201], v24 offset:32776\n"
    "ds_read_b64_tr_b16 a[204:205], v25 offset:32776\n"
    "ds_read_b64_tr_b16 a[194:195], v24 offset:36864\n"
    "ds_read_b64_tr_b16 a[198:199], v25 offset:36864\n"
    "ds_read_b64_tr_b16 a[202:203], v24 offset:36872\n"
    "ds_read_b64_tr_b16 a[206:207], v25 offset:36872\n"
    "ds_read_b64_tr_b16 a[208:209], v24 offset:40960\n"
    "ds_read_b64_tr_b16 a[212:213], v25 offset:40960\n"
    "ds_read_b64_tr_b16 a[216:217], v24 offset:40968\n"
    "ds_read_b64_tr_b16 a[220:221], v25 offset:40968\n"
    "ds_read_b64_tr_b16 a[210:211], v24 offset:45056\n"
    "ds_read_b64_tr_b16 a[214:215], v25 offset:45056\n"
    "ds_read_b64_tr_b16 a[218:219], v24 offset:45064\n"
    "ds_read_b64_tr_b16 a[222:223], v25 offset:45064\n"
    "s_waitcnt vmcnt(0) lgkmcnt(0)\n"
    "s_barrier\n"
    "ds_read_b64_tr_b16 a[224:225], v24 offset:49152\n"
    "ds_read_b64_tr_b16 a[228:229], v25 offset:49152\n"
    "ds_read_b64_tr_b16 a[232:233], v24 offset:49160\n"
    "ds_read_b64_tr_b16 a[236:237], v25 offset:49160\n"
    "ds_read_b64_tr_b16 a[226:227], v24 offset:53248\n"
    "ds_read_b64_tr_b16 a[230:231], v25 offset:53248\n"
    "ds_read_b64_tr_b16 a[234:235], v24 offset:53256\n"
    "ds_read_b64_tr_b16 a[238:239], v25 offset:53256\n"
    "ds_read_b64_tr_b16 a[240:241], v24 offset:57344\n"
    "ds_read_b64_tr_b16 a[244:245], v25 offset:57344\n"
    "ds_read_b64_tr_b16 a[248:249], v24 offset:57352\n"
    "ds_read_b64_tr_b16 a[252:253], v25 offset:57352\n"
    "ds_read_b64_tr_b16 a[242:243], v24 offset:61440\n"
    "ds_read_b64_tr_b16 a[246:247], v25 offset:61440\n"
    "ds_read_b64_tr_b16 a[250:251], v24 offset:61448\n"
    "ds_read_b64_tr_b16 a[254:255], v25 offset:61448\n"
    "s_waitcnt lgkmcnt(0)\n"
    "s_barrier\n"
    "s_mov_b32 s41, -1\n"
    "v_readfirstlane_b32 s54, v20\n"
    "s_bitcmp1_b32 s54, 1\n"
    "s_cselect_b32 s41, s41, 0\n"
    "s_xor_b32 s54, s54, s24\n"
    "s_and_b32 s54, s54, 1\n"
    "s_lshl_b32 s54, s54, 13\n"
    "v_min_u32 v27, v27, v28\n"
    "v_cmp_gt_u32 vcc, 2, v27\n"
    "s_cmp_eq_u64 vcc, 0\n"
    "s_cselect_b32 s41, s41, 0\n"
    "s_cmp_lg_u32 s41, 0\n"
    "s_cbranch_scc1 OF9_%=\n"
    "v_readfirstlane_b32 s40, v21\n"
    "s_cmp_lg_u32 1, 0\n"
    "s_cselect_b32 s40, %2, s40\n"
    "s_mov_b32 s39, 0\n"
    "OBW_%=:\n"
    "buffer_load_dwordx2 v[20:21], off, s[20:23], s61 sc1\n"
    "s_add_u32 s39, s39, 1\n"
    "s_cmp_gt_u32 s39, 262144\n"
    "s_cbranch_scc1 OEND_%=\n"
    "s_waitcnt vmcnt(0)\n"
    "v_readfirstlane_b32 s54, v20\n"
    "v_cmp_eq_u32 vcc, s40, v21\n"
    "s_cbranch_vccnz OBW_%=\n"
    "s_bitcmp1_b32 s54, 1\n"
    "s_cbranch_scc0 OBW_%=\n"
    "s_xor_b32 s54, s54, s24\n"
    "s_and_b32 s54, s54, 1\n"
    "s_lshl_b32 s54, s54, 13\n"
    "v_lshrrev_b32 v4, 2, v0\n"
    "v_and_b32 v5, 3, v0\n"
    "v_lshl_add_u32 v4, v4, 3, v5\n"
    "v_lshlrev_b32 v4, 7, v4\n"
    "s_mov_b32 s39, 0\n"
    "OIW_%=:\n"
    "s_mov_b32 s40, 81793536\n"
    "buffer_load_dword v5, v4, s[20:23], s40 offen sc1\n"
    "s_mov_b32 s40, 81809920\n"
    "buffer_load_dword v6, v4, s[20:23], s40 offen sc1\n"
    "s_add_u32 s39, s39, 1\n"
    "s_cmp_gt_u32 s39, 262144\n"
    "s_cbranch_scc1 OEND_%=\n"
    "s_waitcnt vmcnt(0)\n"
    "v_min_u32 v5, v5, v6\n"
    "v_cmp_gt_u32 vcc, 2, v5\n"
    "s_cbranch_vccnz OIW_%=\n"
    "OF9_%=:\n"
    "s_lshr_b32 s52, s25, 2\n"
    "s_and_b32 s53, s25, 3\n"
    "s_lshl_b32 s31, s52, 16\n"
    "s_and_b32 s40, s26, 1\n"
    "s_lshl_b32 s38, s40, 12\n"
    "s_add_u32 s31, s31, s38\n"
    "s_add_u32 s31, s31, s54\n"
    "s_add_u32 s31, s31, 25165824\n"
    "s_xor_b32 s41, s54, 0x2000\n"
    "s_add_u32 s41, s41, 2097152\n"
    "s_lshr_b32 s32, s52, 1\n"
    "s_lshl_b32 s32, s32, 14\n"
    "s_and_b32 s40, s52, 1\n"
    "s_lshl_b32 s40, s40, 12\n"
    "s_add_u32 s32, s32, s40\n"
    "s_add_u32 s32, s32, s41\n"
    "s_add_u32 s32, s32, 1114112\n"
    "s_add_u32 s56, s41, 1179648\n"
    "s_lshl_b32 s40, s25, 7\n"
    "s_add_u32 s55, s56, s40\n"
    "s_cmp_eq_u32 s26, 1\n"
    "s_cselect_b32 s55, s55, 0x7f000000\n"
    "s_cmp_eq_u32 s26, 0\n"
    "s_cselect_b32 s56, s56, 0x7f000000\n"
    "s_add_u32 s37, s40, s54\n"
    "s_add_u32 s37, s37, 3293184\n"
    "s_lshr_b32 s33, s52, 2\n"
    "s_lshl_b32 s33, s33, 14\n"
    "s_and_b32 s40, s52, 3\n"
    "s_lshl_b32 s40, s40, 11\n"
    "s_add_u32 s33, s33, s40\n"
    "s_lshr_b32 s40, s26, 1\n"
    "s_lshl_b32 s40, s40, 10\n"
    "s_add_u32 s33, s33, s40\n"
    "s_and_b32 s40, s26, 1\n"
    "s_lshl_b32 s43, s40, 10\n"
    "v_add_u32 v249, s43, v230\n"
    "s_lshl_b32 s40, s40, 18\n"
    "s_add_u32 s33, s33, s40\n"
    "s_lshl_b32 s40, s53, 7\n"
    "s_add_u32 s33, s33, s40\n"
    "s_add_u32 s33, s33, s41\n"
    "s_lshr_b32 s34, s25, 1\n"
    "s_lshl_b32 s34, s34, 15\n"
    "s_and_b32 s40, s25, 1\n"
    "s_lshl_b32 s40, s40, 6\n"
    "s_add_u32 s34, s34, s40\n"
    "s_add_u32 s34, s34, s41\n"
    "s_lshl_b32 s60, s25, 10\n"
    "s_lshr_b32 s58, s25, 3\n"
    "s_lshl_b32 s58, s58, 14\n"
    "s_and_b32 s40, s60, 0x1c00\n"
    "s_add_u32 s58, s58, s40\n"
    "s_add_u32 s58, s58, s54\n"
    "s_mov_b32 s29, 1\n"
    "s_lshl_b32 s40, s24, 2\n"
    "s_add_u32 s40, s40, s26\n"
    "s_mov_b32 s61, s40\n"
    "s_cmp_eq_u32 s40, 1\n"
    "s_cselect_b32 s62, 56, 0\n"
    "v_mov_b64 v[176:177], -1\n"
    "v_mov_b64 v[178:179], -1\n"
    "s_mov_b32 s35, 0x7f000000\n"
    "s_mov_b32 s36, 0x7f000000\n"
    "s_mov_b32 s30, 1\n"
    "s_mov_b32 s43, 0\n"
    "s_mul_i32 s42, s27, 2176\n"
    "s_add_u32 s42, s42, 84934656\n"
    "s_cmp_eq_u32 s26, 0\n"
    "s_cselect_b32 s42, s42, 0x7f000000\n"
    "v_and_b32 v1, 15, v0\n"
    "v_lshrrev_b32 v2, 4, v0\n"
    "v_and_b32 v3, 3, v0\n"
    "v_bfe_u32 v4, v0, 2, 2\n"
    "v_lshlrev_b32 v5, 7, v3\n"
    "v_lshl_add_u32 v233, v4, 10, v5\n"
    "v_lshlrev_b32 v5, 4, v2\n"
    "v_and_b32 v6, 1, v0\n"
    "v_lshl_add_u32 v5, v6, 6, v5\n"
    "v_lshrrev_b32 v6, 1, v1\n"
    "v_lshl_add_u32 v234, v6, 15, v5\n"
    "v_and_b32 v5, 31, v0\n"
    "v_lshlrev_b32 v241, 7, v5\n"
    "v_lshlrev_b32 v5, 4, v3\n"
    "v_lshl_add_u32 v5, v4, 7, v5\n"
    "v_lshl_add_u32 v235, v2, 11, v5\n"
    "v_lshlrev_b32 v5, 4, v3\n"
    "v_lshl_add_u32 v5, v2, 6, v5\n"
    "v_lshl_add_u32 v236, v4, 8, v5\n"
    "v_mov_b32 v239, 0x3b000000\n"
    "v_mov_b32 v240, 0x3727c5ac\n"
    "v_and_b32 v10, 1, v0\n"
    "v_lshlrev_b32 v10, 4, v10\n"
    "v_mov_b32 v11, 0x3f80\n"
    "v_lshlrev_b32 v11, v10, v11\n"
    "v_bfe_u32 v13, v0, 1, 3\n"
    "v_cmp_eq_u32 vcc, 0, v13\n"
    "s_nop 1\n"
    "v_cndmask_b32 v208, 0, v11, vcc\n"
    "v_cmp_eq_u32 vcc, 1, v13\n"
    "s_nop 1\n"
    "v_cndmask_b32 v209, 0, v11, vcc\n"
    "v_cmp_eq_u32 vcc, 2, v13\n"
    "s_nop 1\n"
    "v_cndmask_b32 v210, 0, v11, vcc\n"
    "v_cmp_eq_u32 vcc, 3, v13\n"
    "s_nop 1\n"
    "v_cndmask_b32 v211, 0, v11, vcc\n"
    "v_cmp_eq_u32 vcc, 4, v13\n"
    "s_nop 1\n"
    "v_cndmask_b32 v212, 0, v11, vcc\n"
    "v_cmp_eq_u32 vcc, 5, v13\n"
    "s_nop 1\n"
    "v_cndmask_b32 v213, 0, v11, vcc\n"
    "v_cmp_eq_u32 vcc, 6, v13\n"
    "s_nop 1\n"
    "v_cndmask_b32 v214, 0, v11, vcc\n"
    "v_cmp_eq_u32 vcc, 7, v13\n"
    "s_nop 1\n"
    "v_cndmask_b32 v215, 0, v11, vcc\n"
    "v_mov_b32 v247, 65536\n"
    "v_mov_b32 v237, 1\n"
    "ds_write_b32 v247, v237\n"
    "s_waitcnt vmcnt(0) lgkmcnt(0)\n"
    "s_barrier\n"
    ".p2align 6\n"
    "OLP_%=:\n"
    "v_add_u32 v232, s43, v249\n"
    "v_mov_b32 v246, s29\n"
    "s_lshr_b32 s40, s31, 8\n"
    "s_and_b32 s40, s40, 0x2000\n"
    "s_xor_b32 s57, s31, s40\n"
    "s_lshr_b32 s40, s58, 8\n"
    "s_and_b32 s40, s40, 0x2000\n"
    "s_xor_b32 s59, s58, s40\n"
    "s_cmp_lt_u32 s26, 2\n"
    "s_cbranch_scc1 OX_%=\n"
    "s_mov_b32 s39, 0\n"
    "s_branch OF_%=b\n"
    "OF_%=:\n"
    "s_sleep 1\n"
    "s_add_u32 s39, s39, 1\n"
    "s_cmp_gt_u32 s39, 262144\n"
    "s_cbranch_scc1 OEND_%=\n"
    "OF_%=b:\n"
    "buffer_load_dword v237, v233, s[20:23], s32 offen sc1\n"
    "s_waitcnt vmcnt(0)\n"
    "v_cmp_gt_u32 vcc, s29, v237\n"
    "s_cbranch_vccnz OF_%=\n"
    "OFk_%=:\n"
    "ds_read_b32 v237, v247\n"
    "s_add_u32 s39, s39, 1\n"
    "s_cmp_gt_u32 s39, 4194304\n"
    "s_cbranch_scc1 OEND_%=\n"
    "s_waitcnt lgkmcnt(0)\n"
    "v_cmp_gt_u32 vcc, s29, v237\n"
    "s_cbranch_vccnz OFk_%=\n"
    "s_add_u32 m0, s43, s38\n"
    "s_nop 0\n"
    "buffer_load_dwordx4 v230, s[20:23], s57 offen offset:0 sc1 lds\n"
    "buffer_load_dwordx4 v230, s[20:23], s57 offen offset:1024 sc1 lds\n"
    "buffer_load_dwordx4 v230, s[20:23], s57 offen offset:2048 sc1 lds\n"
    "buffer_load_dwordx4 v230, s[20:23], s57 offen offset:3072 sc1 lds\n"
    "s_add_u32 m0, m0, 8192\n"
    "s_add_u32 s57, s57, 16384\n"
    "buffer_load_dwordx4 v230, s[20:23], s57 offen offset:0 sc1 lds\n"
    "buffer_load_dwordx4 v230, s[20:23], s57 offen offset:1024 sc1 lds\n"
    "buffer_load_dwordx4 v230, s[20:23], s57 offen offset:2048 sc1 lds\n"
    "buffer_load_dwordx4 v230, s[20:23], s57 offen offset:3072 sc1 lds\n"
    "s_add_u32 m0, m0, 8192\n"
    "s_add_u32 s57, s57, 16384\n"
    "buffer_load_dwordx4 v230, s[20:23], s57 offen offset:0 sc1 lds\n"
    "buffer_load_dwordx4 v230, s[20:23], s57 offen offset:1024 sc1 lds\n"
    "buffer_load_dwordx4 v230, s[20:23], s57 offen offset:2048 sc1 lds\n"
    "buffer_load_dwordx4 v230, s[20:23], s57 offen offset:3072 sc1 lds\n"
    "s_add_u32 m0, m0, 8192\n"
    "s_add_u32 s57, s57, 16384\n"
    "buffer_load_dwordx4 v230, s[20:23], s57 offen offset:0 sc1 lds\n"
    "buffer_load_dwordx4 v230, s[20:23], s57 offen offset:1024 sc1 lds\n"
    "buffer_load_dwordx4 v230, s[20:23], s57 offen offset:2048 sc1 lds\n"
    "buffer_load_dwordx4 v230, s[20:23], s57 offen offset:3072 sc1 lds\n"
    "s_add_u32 s31, s31, 524288\n"
    "s_cmp_ge_u32 s31, 58720256\n"
    "s_cselect_b32 s40, 33554432, 0\n"
    "s_sub_u32 s31, s31, s40\n"
    "s_xor_b32 s32, s32, 512\n"
    "s_waitcnt vmcnt(12)\n"
    "s_barrier\n"
    "ds_read_b128 v[0:3], v232 offset:0\n"
    "ds_read_b128 v[4:7], v232 offset:2048\n"
    "ds_read_b128 v[8:11], v232 offset:4096\n"
    "ds_read_b128 v[12:15], v232 offset:6144\n"
    "s_waitcnt lgkmcnt(3)\n"
    "v_mfma_f32_16x16x32_bf16 v[128:131], a[0:3], v[0:3], 0\n"
    "v_mfma_f32_16x16x32_bf16 v[132:135], a[4:7], v[0:3], 0\n"
    "v_mfma_f32_16x16x32_bf16 v[136:139], a[8:11], v[0:3], 0\n"
    "v_mfma_f32_16x16x32_bf16 v[140:143], a[12:15], v[0:3], 0\n"
    "s_waitcnt lgkmcnt(2)\n"
    "v_mfma_f32_16x16x32_bf16 v[128:131], a[16:19], v[4:7], v[128:131]\n"
    "v_mfma_f32_16x16x32_bf16 v[132:135], a[20:23], v[4:7], v[132:135]\n"
    "v_mfma_f32_16x16x32_bf16 v[136:139], a[24:27], v[4:7], v[136:139]\n"
    "v_mfma_f32_16x16x32_bf16 v[140:143], a[28:31], v[4:7], v[140:143]\n"
    "s_waitcnt vmcnt(8)\n"
    "s_barrier\n"
    "s_waitcnt lgkmcnt(1)\n"
    "v_mfma_f32_16x16x32_bf16 v[128:131], a[32:35], v[8:11], v[128:131]\n"
    "ds_read_b128 v[16:19], v232 offset:8192\n"
    "v_mfma_f32_16x16x32_bf16 v[132:135], a[36:39], v[8:11], v[132:135]\n"
    "ds_read_b128 v[20:23], v232 offset:10240\n"
    "v_mfma_f32_16x16x32_bf16 v[136:139], a[40:43], v[8:11], v[136:139]\n"
    "ds_read_b128 v[24:27], v232 offset:12288\n"
    "v_mfma_f32_16x16x32_bf16 v[140:143], a[44:47], v[8:11], v[140:143]\n"
    "ds_read_b128 v[28:31], v232 offset:14336\n"
    "s_waitcnt lgkmcnt(4)\n"
    "v_mfma_f32_16x16x32_bf16 v[128:131], a[48:51], v[12:15], v[128:131]\n"
    "v_mfma_f32_16x16x32_bf16 v[132:135], a[52:55], v[12:15], v[132:135]\n"
    "v_mfma_f32_16x16x32_bf16 v[136:139], a[56:59], v[12:15], v[136:139]\n"
    "v_mfma_f32_16x16x32_bf16 v[140:143], a[60:63], v[12:15], v[140:143]\n"
    "s_waitcnt lgkmcnt(3)\n"
    "v_mfma_f32_16x16x32_bf16 v[128:131], a[64:67], v[16:19], v[128:131]\n"
    "v_mfma_f32_16x16x32_bf16 v[132:135], a[68:71], v[16:19], v[132:135]\n"
    "v_mfma_f32_16x16x32_bf16 v[136:139], a[72:75], v[16:19], v[136:139]\n"
    "v_mfma_f32_16x16x32_bf16 v[140:143], a[76:79], v[16:19], v[140:143]\n"
    "s_waitcnt lgkmcnt(2)\n"
    "v_mfma_f32_16x16x32_bf16 v[128:131], a[80:83], v[20:23], v[128:131]\n"
    "v_mfma_f32_16x16x32_bf16 v[132:135], a[84:87], v[20:23], v[132:135]\n"
    "v_mfma_f32_16x16x32_bf16 v[136:139], a[88:91], v[20:23], v[136:139]\n"
    "v_mfma_f32_16x16x32_bf16 v[140:143], a[92:95], v[20:23], v[140:143]\n"
    "s_waitcnt vmcnt(4)\n"
    "s_barrier\n"
    "s_waitcnt lgkmcnt(1)\n"
    "v_mfma_f32_16x16x32_bf16 v[128:131], a[96:99], v[24:27], v[128:131]\n"
    "ds_read_b128 v[32:35], v232 offset:16384\n"
    "v_mfma_f32_16x16x32_bf16 v[132:135], a[100:103], v[24:27], v[132:135]\n"
    "ds_read_b128 v[36:39], v232 offset:18432\n"
    "v_mfma_f32_16x16x32_bf16 v[136:139], a[104:107], v[24:27], v[136:139]\n"
    "ds_read_b128 v[40:43], v232 offset:20480\n"
    "v_mfma_f32_16x16x32_bf16 v[140:143], a[108:111], v[24:27], v[140:143]\n"
    "ds_read_b128 v[44:47], v232 offset:22528\n"
    "s_waitcnt lgkmcnt(4)\n"
    "v_mfma_f32_16x16x32_bf16 v[128:131], a[112:115], v[28:31], v[128:131]\n"
    "v_mfma_f32_16x16x32_bf16 v[132:135], a[116:119], v[28:31], v[132:135]\n"
    "v_mfma_f32_16x16x32_bf16 v[136:139], a[120:123], v[28:31], v[136:139]\n"
    "v_mfma_f32_16x16x32_bf16 v[140:143], a[124:127], v[28:31], v[140:143]\n"
    "s_waitcnt lgkmcnt(3)\n"
    "v_mfma_f32_16x16x32_bf16 v[128:131], a[128:131], v[32:35], v[128:131]\n"
    "v_mfma_f32_16x16x32_bf16 v[132:135], a[132:135], v[32:35], v[132:135]\n"
    "v_mfma_f32_16x16x32_bf16 v[136:139], a[136:139], v[32:35], v[136:139]\n"
    "v_mfma_f32_16x16x32_bf16 v[140:143], a[140:143], v[32:35], v[140:143]\n"
    "s_waitcnt lgkmcnt(2)\n"
    "v_mfma_f32_16x16x32_bf16 v[128:131], a[144:147], v[36:39], v[128:131]\n"
    "v_mfma_f32_16x16x32_bf16 v[132:135], a[148:151], v[36:39], v[132:135]\n"
    "v_mfma_f32_16x16x32_bf16 v[136:139], a[152:155], v[36:39], v[136:139]\n"
    "v_mfma_f32_16x16x32_bf16 v[140:143], a[156:159], v[36:39], v[140:143]\n"
    "s_waitcnt vmcnt(0)\n"
    "s_barrier\n"
    "s_waitcnt lgkmcnt(1)\n"
    "v_mfma_f32_16x16x32_bf16 v[128:131], a[160:163], v[40:43], v[128:131]\n"
    "ds_read_b128 v[48:51], v232 offset:24576\n"
    "v_mfma_f32_16x16x32_bf16 v[132:135], a[164:167], v[40:43], v[132:135]\n"
    "ds_read_b128 v[52:55], v232 offset:26624\n"
    "v_mfma_f32_16x16x32_bf16 v[136:139], a[168:171], v[40:43], v[136:139]\n"
    "ds_read_b128 v[56:59], v232 offset:28672\n"
    "v_mfma_f32_16x16x32_bf16 v[140:143], a[172:175], v[40:43], v[140:143]\n"
    "ds_read_b128 v[60:63], v232 offset:30720\n"
    "s_waitcnt lgkmcnt(4)\n"
    "v_mfma_f32_16x16x32_bf16 v[128:131], a[176:179], v[44:47], v[128:131]\n"
    "v_mfma_f32_16x16x32_bf16 v[132:135], a[180:183], v[44:47], v[132:135]\n"
    "v_mfma_f32_16x16x32_bf16 v[136:139], a[184:187], v[44:47], v[136:139]\n"
    "v_mfma_f32_16x16x32_bf16 v[140:143], a[188:191], v[44:47], v[140:143]\n"
    "s_waitcnt lgkmcnt(3)\n"
    "v_mfma_f32_16x16x32_bf16 v[128:131], a[192:195], v[48:51], v[128:131]\n"
    "v_mfma_f32_16x16x32_bf16 v[132:135], a[196:199], v[48:51], v[132:135]\n"
    "v_mfma_f32_16x16x32_bf16 v[136:139], a[200:203], v[48:51], v[136:139]\n"
    "v_mfma_f32_16x16x32_bf16 v[140:143], a[204:207], v[48:51], v[140:143]\n"
    "s_waitcnt lgkmcnt(2)\n"
    "v_mfma_f32_16x16x32_bf16 v[128:131], a[208:211], v[52:55], v[128:131]\n"
    "v_mfma_f32_16x16x32_bf16 v[132:135], a[212:215], v[52:55], v[132:135]\n"
    "v_mfma_f32_16x16x32_bf16 v[136:139], a[216:219], v[52:55], v[136:139]\n"
    "v_mfma_f32_16x16x32_bf16 v[140:143], a[220:223], v[52:55], v[140:143]\n"
    "s_waitcnt lgkmcnt(1)\n"
    "v_mfma_f32_16x16x32_bf16 v[128:131], a[224:227], v[56:59], v[128:131]\n"
    "v_mfma_f32_16x16x32_bf16 v[132:135], a[228:231], v[56:59], v[132:135]\n"
    "v_mfma_f32_16x16x32_bf16 v[136:139], a[232:235], v[56:59], v[136:139]\n"
    "v_mfma_f32_16x16x32_bf16 v[140:143], a[236:239], v[56:59], v[140:143]\n"
    "s_waitcnt lgkmcnt(0)\n"
    "v_mfma_f32_16x16x32_bf16 v[128:131], a[240:243], v[60:63], v[128:131]\n"
    "v_mfma_f32_16x16x32_bf16 v[132:135], a[244:247], v[60:63], v[132:135]\n"
    "s_nop 0\n"
    "v_mfma_f32_16x16x32_bf16 v[136:139], a[248:251], v[60:63], v[136:139]\n"
    "s_nop 2\n"
    "v_mfma_f32_16x16x32_bf16 v[140:143], a[252:255], v[60:63], v[140:143]\n"
    "s_nop 0\n"
    "v_cvt_pk_bf16_f32 v128, v128, v129\n"
    "v_cvt_pk_bf16_f32 v129, v130, v131\n"
    "v_bfi_b32 v128, 1, s30, v128\n"
    "v_cvt_pk_bf16_f32 v130, v132, v133\n"
    "v_cvt_pk_bf16_f32 v131, v134, v135\n"
    "v_bfi_b32 v130, 1, s30, v130\n"
    "buffer_store_dwordx4 v[128:131], v234, s[20:23], s33 offen offset:0 sc1\n"
    "v_cvt_pk_bf16_f32 v136, v136, v137\n"
    "v_cvt_pk_bf16_f32 v137, v138, v139\n"
    "v_bfi_b32 v136, 1, s30, v136\n"
    "v_cvt_pk_bf16_f32 v138, v140, v141\n"
    "v_cvt_pk_bf16_f32 v139, v142, v143\n"
    "v_bfi_b32 v138, 1, s30, v138\n"
    "buffer_store_dwordx4 v[136:139], v234, s[20:23], s33 offen offset:512 sc1\n"
    "s_branch ONX_%=\n"
    "OX_%=:\n"
    "s_add_u32 s35, s59, 8388608\n"
    "s_add_u32 s36, s59, 12582912\n"
    "s_cmp_eq_u32 s26, 0\n"
    "s_cselect_b32 s35, s35, 0x7f000000\n"
    "s_cmp_eq_u32 s61, 0\n"
    "s_cselect_b32 s36, s36, 0x7f000000\n"
    "s_add_u32 s41, s58, 524288\n"
    "s_lshr_b32 s40, s41, 8\n"
    "s_and_b32 s40, s40, 0x2000\n"
    "s_xor_b32 s41, s41, s40\n"
    "s_xor_b32 s41, s41, 0x2000\n"
    "s_cmp_gt_u32 s29, s62\n"
    "s_cselect_b32 s41, 0x7e000000, s41\n"
    "s_add_u32 s70, s41, 4194304\n"
    "s_add_u32 s71, s41, 8388608\n"
    "s_barrier\n"
    "s_memrealtime s[64:65]\n"
    "ds_read_b128 v[0:3], v232 offset:0\n"
    "ds_read_b128 v[4:7], v232 offset:2048\n"
    "ds_read_b128 v[8:11], v232 offset:4096\n"
    "ds_read_b128 v[12:15], v232 offset:6144\n"
    "s_waitcnt lgkmcnt(3)\n"
    "v_mfma_f32_16x16x32_bf16 v[128:131], a[0:3], v[0:3], 0\n"
    "v_mfma_f32_16x16x32_bf16 v[132:135], a[4:7], v[0:3], 0\n"
    "v_mfma_f32_16x16x32_bf16 v[136:139], a[8:11], v[0:3], 0\n"
    "v_mfma_f32_16x16x32_bf16 v[140:143], a[12:15], v[0:3], 0\n"
    "s_waitcnt lgkmcnt(2)\n"
    "v_mfma_f32_16x16x32_bf16 v[128:131], a[16:19], v[4:7], v[128:131]\n"
    "v_mfma_f32_16x16x32_bf16 v[132:135], a[20:23], v[4:7], v[132:135]\n"
    "v_mfma_f32_16x16x32_bf16 v[136:139], a[24:27], v[4:7], v[136:139]\n"
    "v_mfma_f32_16x16x32_bf16 v[140:143], a[28:31], v[4:7], v[140:143]\n"
    "s_waitcnt lgkmcnt(0)\n"
    "s_lshl_b32 s40, s64, 8\n"
    "s_or_b32 s40, s40, s29\n"
    "v_mov_b32 v248, s40\n"
    "s_mov_b64 exec, 1\n"
    "buffer_store_dword v248, off, s[20:23], s55 sc1\n"
    "s_mov_b64 exec, -1\n"
    "s_barrier\n"
    "v_mfma_f32_16x16x32_bf16 v[128:131], a[32:35], v[8:11], v[128:131]\n"
    "ds_read_b128 v[16:19], v232 offset:8192\n"
    "v_mfma_f32_16x16x32_bf16 v[132:135], a[36:39], v[8:11], v[132:135]\n"
    "ds_read_b128 v[20:23], v232 offset:10240\n"
    "v_mfma_f32_16x16x32_bf16 v[136:139], a[40:43], v[8:11], v[136:139]\n"
    "ds_read_b128 v[24:27], v232 offset:12288\n"
    "v_mfma_f32_16x16x32_bf16 v[140:143], a[44:47], v[8:11], v[140:143]\n"
    "ds_read_b128 v[28:31], v232 offset:14336\n"
    "buffer_store_dwordx4 v[176:179], v230, s[20:23], s70 offen sc1\n"
    "buffer_store_dwordx4 v[176:179], v230, s[20:23], s71 offen sc1\n"
    "v_mfma_f32_16x16x32_bf16 v[128:131], a[48:51], v[12:15], v[128:131]\n"
    "v_mfma_f32_16x16x32_bf16 v[132:135], a[52:55], v[12:15], v[132:135]\n"
    "v_mfma_f32_16x16x32_bf16 v[136:139], a[56:59], v[12:15], v[136:139]\n"
    "v_mfma_f32_16x16x32_bf16 v[140:143], a[60:63], v[12:15], v[140:143]\n"
    "s_waitcnt lgkmcnt(3)\n"
    "v_mfma_f32_16x16x32_bf16 v[128:131], a[64:67], v[16:19], v[128:131]\n"
    "v_mfma_f32_16x16x32_bf16 v[132:135], a[68:71], v[16:19], v[132:135]\n"
    "v_mfma_f32_16x16x32_bf16 v[136:139], a[72:75], v[16:19], v[136:139]\n"
    "v_mfma_f32_16x16x32_bf16 v[140:143], a[76:79], v[16:19], v[140:143]\n"
    "s_waitcnt lgkmcnt(2)\n"
    "v_mfma_f32_16x16x32_bf16 v[128:131], a[80:83], v[20:23], v[128:131]\n"
    "v_mfma_f32_16x16x32_bf16 v[132:135], a[84:87], v[20:23], v[132:135]\n"
    "v_mfma_f32_16x16x32_bf16 v[136:139], a[88:91], v[20:23], v[136:139]\n"
    "v_mfma_f32_16x16x32_bf16 v[140:143], a[92:95], v[20:23], v[140:143]\n"
    "buffer_load_dwordx4 v[220:223], v236, s[20:23], s36 offen sc1\n"
    "buffer_load_dwordx4 v[216:219], v236, s[20:23], s35 offen sc1\n"
    "s_barrier\n"
    "s_waitcnt lgkmcnt(1)\n"
    "v_mfma_f32_16x16x32_bf16 v[128:131], a[96:99], v[24:27], v[128:131]\n"
    "ds_read_b128 v[32:35], v232 offset:16384\n"
    "v_mfma_f32_16x16x32_bf16 v[132:135], a[100:103], v[24:27], v[132:135]\n"
    "ds_read_b128 v[36:39], v232 offset:18432\n"
    "v_mfma_f32_16x16x32_bf16 v[136:139], a[104:107], v[24:27], v[136:139]\n"
    "ds_read_b128 v[40:43], v232 offset:20480\n"
    "v_mfma_f32_16x16x32_bf16 v[140:143], a[108:111], v[24:27], v[140:143]\n"
    "ds_read_b128 v[44:47], v232 offset:22528\n"
    "s_waitcnt lgkmcnt(4)\n"
    "v_mfma_f32_16x16x32_bf16 v[128:131], a[112:115], v[28:31], v[128:131]\n"
    "v_mfma_f32_16x16x32_bf16 v[132:135], a[116:119], v[28:31], v[132:135]\n"
    "v_mfma_f32_16x16x32_bf16 v[136:139], a[120:123], v[28:31], v[136:139]\n"
    "v_mfma_f32_16x16x32_bf16 v[140:143], a[124:127], v[28:31], v[140:143]\n"
    "s_waitcnt lgkmcnt(3)\n"
    "v_mfma_f32_16x16x32_bf16 v[128:131], a[128:131], v[32:35], v[128:131]\n"
    "v_mfma_f32_16x16x32_bf16 v[132:135], a[132:135], v[32:35], v[132:135]\n"
    "v_mfma_f32_16x16x32_bf16 v[136:139], a[136:139], v[32:35], v[136:139]\n"
    "v_mfma_f32_16x16x32_bf16 v[140:143], a[140:143], v[32:35], v[140:143]\n"
    "s_waitcnt lgkmcnt(2)\n"
    "v_mfma_f32_16x16x32_bf16 v[128:131], a[144:147], v[36:39], v[128:131]\n"
    "v_mfma_f32_16x16x32_bf16 v[132:135], a[148:151], v[36:39], v[132:135]\n"
    "v_mfma_f32_16x16x32_bf16 v[136:139], a[152:155], v[36:39], v[136:139]\n"
    "v_mfma_f32_16x16x32_bf16 v[140:143], a[156:159], v[36:39], v[140:143]\n"
    "s_barrier\n"
    "s_waitcnt lgkmcnt(1)\n"
    "v_mfma_f32_16x16x32_bf16 v[128:131], a[160:163], v[40:43], v[128:131]\n"
    "ds_read_b128 v[48:51], v232 offset:24576\n"
    "v_mfma_f32_16x16x32_bf16 v[132:135], a[164:167], v[40:43], v[132:135]\n"
    "ds_read_b128 v[52:55], v232 offset:26624\n"
    "v_mfma_f32_16x16x32_bf16 v[136:139], a[168:171], v[40:43], v[136:139]\n"
    "ds_read_b128 v[56:59], v232 offset:28672\n"
    "v_mfma_f32_16x16x32_bf16 v[140:143], a[172:175], v[40:43], v[140:143]\n"
    "ds_read_b128 v[60:63], v232 offset:30720\n"
    "s_waitcnt lgkmcnt(4)\n"
    "v_mfma_f32_16x16x32_bf16 v[128:131], a[176:179], v[44:47], v[128:131]\n"
    "v_mfma_f32_16x16x32_bf16 v[132:135], a[180:183], v[44:47], v[132:135]\n"
    "v_mfma_f32_16x16x32_bf16 v[136:139], a[184:187], v[44:47], v[136:139]\n"
    "v_mfma_f32_16x16x32_bf16 v[140:143], a[188:191], v[44:47], v[140:143]\n"
    "buffer_load_dword v237, v241, s[20:23], s56 offen sc1\n"
    "s_waitcnt lgkmcnt(3)\n"
    "v_mfma_f32_16x16x32_bf16 v[128:131], a[192:195], v[48:51], v[128:131]\n"
    "v_mfma_f32_16x16x32_bf16 v[132:135], a[196:199], v[48:51], v[132:135]\n"
    "v_mfma_f32_16x16x32_bf16 v[136:139], a[200:203], v[48:51], v[136:139]\n"
    "v_mfma_f32_16x16x32_bf16 v[140:143], a[204:207], v[48:51], v[140:143]\n"
    "s_waitcnt lgkmcnt(2)\n"
    "v_mfma_f32_16x16x32_bf16 v[128:131], a[208:211], v[52:55], v[128:131]\n"
    "v_mfma_f32_16x16x32_bf16 v[132:135], a[212:215], v[52:55], v[132:135]\n"
    "v_mfma_f32_16x16x32_bf16 v[136:139], a[216:219], v[52:55], v[136:139]\n"
    "v_mfma_f32_16x16x32_bf16 v[140:143], a[220:223], v[52:55], v[140:143]\n"
    "s_waitcnt lgkmcnt(1)\n"
    "v_mfma_f32_16x16x32_bf16 v[128:131], a[224:227], v[56:59], v[128:131]\n"
    "v_mfma_f32_16x16x32_bf16 v[132:135], a[228:231], v[56:59], v[132:135]\n"
    "v_mfma_f32_16x16x32_bf16 v[136:139], a[232:235], v[56:59], v[136:139]\n"
    "v_mfma_f32_16x16x32_bf16 v[140:143], a[236:239], v[56:59], v[140:143]\n"
    "s_waitcnt lgkmcnt(0)\n"
    "v_mfma_f32_16x16x32_bf16 v[128:131], a[240:243], v[60:63], v[128:131]\n"
    "v_mfma_f32_16x16x32_bf16 v[132:135], a[244:247], v[60:63], v[132:135]\n"
    "s_nop 0\n"
    "v_mfma_f32_16x16x32_bf16 v[136:139], a[248:251], v[60:63], v[136:139]\n"
    "s_nop 2\n"
    "v_mfma_f32_16x16x32_bf16 v[140:143], a[252:255], v[60:63], v[140:143]\n"
    "s_nop 0\n"
    "v_cvt_pk_bf16_f32 v128, v128, v129\n"
    "v_cvt_pk_bf16_f32 v129, v130, v131\n"
    "v_bfi_b32 v128, 1, s30, v128\n"
    "v_cvt_pk_bf16_f32 v130, v132, v133\n"
    "v_cvt_pk_bf16_f32 v131, v134, v135\n"
    "v_bfi_b32 v130, 1, s30, v130\n"
    "buffer_store_dwordx4 v[128:131], v234, s[20:23], s33 offen offset:0 sc1\n"
    "v_cvt_pk_bf16_f32 v136, v136, v137\n"
    "v_cvt_pk_bf16_f32 v137, v138, v139\n"
    "v_bfi_b32 v136, 1, s30, v136\n"
    "v_cvt_pk_bf16_f32 v138, v140, v141\n"
    "v_cvt_pk_bf16_f32 v139, v142, v143\n"
    "v_bfi_b32 v138, 1, s30, v138\n"
    "buffer_store_dwordx4 v[136:139], v234, s[20:23], s33 offen offset:512 sc1\n"
    "s_cmp_lg_u32 s26, 0\n"
    "s_cbranch_scc1 ONX_%=\n"
    "s_mov_b32 s39, 0\n"
    "s_waitcnt vmcnt(2)\n"
    "OH_%=:\n"
    "v_and_b32 v238, 0xff, v237\n"
    "v_sub_u32 v237, v237, v248\n"
    "v_cmp_gt_u32 vcc, s29, v238\n"
    "s_cbranch_vccnz OHR_%=\n"
    "v_cmp_eq_u32 vcc, s29, v238\n"
    "s_nop 1\n"
    "v_cndmask_b32 v237, 0, v237, vcc\n"
    "s_nop 1\n"
    "v_max_i32_dpp v237, v237, v237 row_ror:1 row_mask:0xf bank_mask:0xf\n"
    "s_nop 1\n"
    "v_max_i32_dpp v237, v237, v237 row_ror:2 row_mask:0xf bank_mask:0xf\n"
    "s_nop 1\n"
    "v_max_i32_dpp v237, v237, v237 row_ror:4 row_mask:0xf bank_mask:0xf\n"
    "s_nop 1\n"
    "v_max_i32_dpp v237, v237, v237 row_ror:8 row_mask:0xf bank_mask:0xf\n"
    "s_nop 0\n"
    "v_readlane_b32 s40, v237, 0\n"
    "v_readlane_b32 s41, v237, 16\n"
    "s_max_i32 s40, s40, s41\n"
    "s_ashr_i32 s40, s40, 8\n"
    "s_min_i32 s40, s40, 400\n"
    "s_cmp_eq_u32 s29, 64\n"
    "s_cselect_b32 s41, 14, 0\n"
    "s_sub_u32 s40, s40, s41\n"
    "s_add_u32 s40, s40, s28\n"
    "s_add_u32 s63, s40, s64\n"
    "s_memrealtime s[66:67]\n"
    "s_nop 3\n"
    "OU_%=:\n"
    "s_memrealtime s[68:69]\n"
    "s_waitcnt lgkmcnt(1)\n"
    "s_sub_u32 s40, s66, s63\n"
    "s_cmp_lt_i32 s40, 0\n"
    "s_cbranch_scc0 OU_%=x\n"
    "s_memrealtime s[66:67]\n"
    "s_waitcnt lgkmcnt(1)\n"
    "s_sub_u32 s40, s68, s63\n"
    "s_cmp_lt_i32 s40, 0\n"
    "s_cbranch_scc1 OU_%=\n"
    "OU_%=x:\n"
    "s_add_u32 s41, s34, 16384\n"
    "s_mov_b32 s39, 0\n"
    "OI_%=:\n"
    "buffer_load_dwordx4 v[176:179], v235, s[20:23], s34 offen offset:0 sc1\n"
    "buffer_load_dwordx4 v[180:183], v235, s[20:23], s34 offen offset:512 sc1\n"
    "buffer_load_dwordx4 v[184:187], v235, s[20:23], s34 offen offset:1024 sc1\n"
    "buffer_load_dwordx4 v[188:191], v235, s[20:23], s34 offen offset:1536 sc1\n"
    "buffer_load_dwordx4 v[192:195], v235, s[20:23], s41 offen offset:0 sc1\n"
    "buffer_load_dwordx4 v[196:199], v235, s[20:23], s41 offen offset:512 sc1\n"
    "buffer_load_dwordx4 v[200:203], v235, s[20:23], s41 offen offset:1024 sc1\n"
    "buffer_load_dwordx4 v[204:207], v235, s[20:23], s41 offen offset:1536 sc1\n"
    "v_add_u32 v248, 1, v246\n"
    "ds_write_b32 v247, v248\n"
    "s_add_u32 s39, s39, 1\n"
    "s_cmp_gt_u32 s39, 262144\n"
    "s_cbranch_scc1 OEND_%=\n"
    "s_waitcnt vmcnt(8)\n"
    "v_max_u32 v238, v216, v218\n"
    "v_cmp_eq_u32 vcc, -1, v238\n"
    "s_nop 1\n"
    "v_cndmask_b32 v238, 0, 1, vcc\n"
    "v_lshlrev_b32 v144, 16, v216\n"
    "v_and_b32 v145, 0xffff0000, v216\n"
    "v_lshlrev_b32 v146, 16, v217\n"
    "v_and_b32 v147, 0xffff0000, v217\n"
    "v_lshlrev_b32 v148, 16, v218\n"
    "v_and_b32 v149, 0xffff0000, v218\n"
    "v_lshlrev_b32 v150, 16, v219\n"
    "v_and_b32 v151, 0xffff0000, v219\n"
    "v_lshlrev_b32 v152, 16, v220\n"
    "v_and_b32 v153, 0xffff0000, v220\n"
    "v_lshlrev_b32 v154, 16, v221\n"
    "v_and_b32 v155, 0xffff0000, v221\n"
    "v_lshlrev_b32 v156, 16, v222\n"
    "v_and_b32 v157, 0xffff0000, v222\n"
    "v_lshlrev_b32 v158, 16, v223\n"
    "v_and_b32 v159, 0xffff0000, v223\n"
    "s_waitcnt vmcnt(7)\n"
    "v_mfma_f32_16x16x32_bf16 v[160:163], v[208:211], v[176:179], v[144:147]\n"
    "v_bitop3_b32 v237, v176, v178, s30 bitop3:0x7e\n"
    "v_and_or_b32 v238, v237, 1, v238\n"
    "s_waitcnt vmcnt(6)\n"
    "v_mfma_f32_16x16x32_bf16 v[164:167], v[208:211], v[180:183], v[148:151]\n"
    "v_bitop3_b32 v237, v180, v182, s30 bitop3:0x7e\n"
    "v_and_or_b32 v238, v237, 1, v238\n"
    "s_waitcnt vmcnt(5)\n"
    "v_mfma_f32_16x16x32_bf16 v[160:163], v[212:215], v[184:187], v[160:163]\n"
    "v_bitop3_b32 v237, v184, v186, s30 bitop3:0x7e\n"
    "v_and_or_b32 v238, v237, 1, v238\n"
    "s_waitcnt vmcnt(4)\n"
    "v_mfma_f32_16x16x32_bf16 v[164:167], v[212:215], v[188:191], v[164:167]\n"
    "v_bitop3_b32 v237, v188, v190, s30 bitop3:0x7e\n"
    "v_and_or_b32 v238, v237, 1, v238\n"
    "s_waitcnt vmcnt(3)\n"
    "v_mfma_f32_16x16x32_bf16 v[160:163], v[208:211], v[192:195], v[160:163]\n"
    "v_bitop3_b32 v237, v192, v194, s30 bitop3:0x7e\n"
    "v_and_or_b32 v238, v237, 1, v238\n"
    "s_waitcnt vmcnt(2)\n"
    "v_mfma_f32_16x16x32_bf16 v[164:167], v[208:211], v[196:199], v[164:167]\n"
    "v_bitop3_b32 v237, v196, v198, s30 bitop3:0x7e\n"
    "v_and_or_b32 v238, v237, 1, v238\n"
    "s_waitcnt vmcnt(1)\n"
    "v_mfma_f32_16x16x32_bf16 v[160:163], v[212:215], v[200:203], v[160:163]\n"
    "v_bitop3_b32 v237, v200, v202, s30 bitop3:0x7e\n"
    "v_and_or_b32 v238, v237, 1, v238\n"
    "s_waitcnt vmcnt(0)\n"
    "v_mfma_f32_16x16x32_bf16 v[164:167], v[212:215], v[204:207], v[164:167]\n"
    "v_bitop3_b32 v237, v204, v206, s30 bitop3:0x7e\n"
    "v_and_or_b32 v238, v237, 1, v238\n"
    "v_cmp_ne_u32 vcc, 0, v238\n"
    "s_cmp_eq_u32 s24, 0\n"
    "s_nop 4\n"
    "s_cbranch_vccnz OIR_%=\n"
    "s_cbranch_scc1 OL_%=\n"
    "v_cvt_pk_bf16_f32 v160, v160, v161\n"
    "v_cvt_pk_bf16_f32 v161, v162, v163\n"
    "v_cvt_pk_bf16_f32 v162, v164, v165\n"
    "v_cvt_pk_bf16_f32 v163, v166, v167\n"
    "buffer_store_dwordx4 v[160:163], v236, s[44:47], s60 offen sc1\n"
    "s_branch ONX_%=\n"
    "OL_%=:\n"
    "s_mov_b64 exec, 1\n"
    "buffer_store_dword v246, off, s[20:23], s37 sc1\n"
    "s_mov_b64 exec, -1\n"
    "v_readfirstlane_b32 s48, v160\n"
    "s_nop 1\n"
    "v_pk_add_f32 v[168:169], v[160:161], s[48:49] op_sel_hi:[1,0] neg_lo:[0,1] neg_hi:[0,1]\n"
    "v_pk_add_f32 v[170:171], v[162:163], s[48:49] op_sel_hi:[1,0] neg_lo:[0,1] neg_hi:[0,1]\n"
    "v_pk_add_f32 v[172:173], v[164:165], s[48:49] op_sel_hi:[1,0] neg_lo:[0,1] neg_hi:[0,1]\n"
    "v_pk_add_f32 v[174:175], v[166:167], s[48:49] op_sel_hi:[1,0] neg_lo:[0,1] neg_hi:[0,1]\n"
    "v_pk_add_f32 v[224:225], v[168:169], v[170:171]\n"
    "v_pk_mul_f32 v[226:227], v[168:169], v[168:169]\n"
    "v_pk_add_f32 v[224:225], v[224:225], v[172:173]\n"
    "v_pk_fma_f32 v[226:227], v[170:171], v[170:171], v[226:227]\n"
    "v_pk_add_f32 v[224:225], v[224:225], v[174:175]\n"
    "v_pk_fma_f32 v[226:227], v[172:173], v[172:173], v[226:227]\n"
    "v_pk_fma_f32 v[226:227], v[174:175], v[174:175], v[226:227]\n"
    "v_add_f32 v224, v224, v225\n"
    "v_add_f32 v226, v226, v227\n"
    "s_nop 0\n"
    "v_add_f32_dpp v224, v224, v224 row_ror:1 row_mask:0xf bank_mask:0xf\n"
    "v_add_f32_dpp v226, v226, v226 row_ror:1 row_mask:0xf bank_mask:0xf\n"
    "s_nop 0\n"
    "v_add_f32_dpp v224, v224, v224 row_ror:2 row_mask:0xf bank_mask:0xf\n"
    "v_add_f32_dpp v226, v226, v226 row_ror:2 row_mask:0xf bank_mask:0xf\n"
    "s_nop 0\n"
    "v_add_f32_dpp v224, v224, v224 row_ror:4 row_mask:0xf bank_mask:0xf\n"
    "v_add_f32_dpp v226, v226, v226 row_ror:4 row_mask:0xf bank_mask:0xf\n"
    "s_nop 0\n"
    "v_add_f32_dpp v224, v224, v224 row_ror:8 row_mask:0xf bank_mask:0xf\n"
    "v_add_f32_dpp v226, v226, v226 row_ror:8 row_mask:0xf bank_mask:0xf\n"
    "s_nop 0\n"
    "v_add_f32_dpp v224, v224, v224 row_bcast:15 row_mask:0xa bank_mask:0xf\n"
    "v_add_f32_dpp v226, v226, v226 row_bcast:15 row_mask:0xa bank_mask:0xf\n"
    "s_nop 0\n"
    "v_add_f32_dpp v224, v224, v224 row_bcast:31 row_mask:0xc bank_mask:0xf\n"
    "v_add_f32_dpp v226, v226, v226 row_bcast:31 row_mask:0xc bank_mask:0xf\n"
    "v_readlane_b32 s48, v224, 63\n"
    "v_readlane_b32 s50, v226, 63\n"
    "s_nop 0\n"
    "v_mul_f32 v242, s48, v239\n"
    "v_mul_f32 v244, s50, v239\n"
    "v_fma_f32 v244, -v242, v242, v244\n"
    "v_max_f32 v244, 0, v244\n"
    "v_add_f32 v244, v240, v244\n"
    "v_rsq_f32 v244, v244\n"
    "s_nop 0\n"
    "v_mul_f32 v242, v242, v244\n"
    "v_pk_fma_f32 v[144:145], v[168:169], v[244:245], v[242:243] op_sel_hi:[1,0,0] neg_lo:[0,0,1] neg_hi:[0,0,1]\n"
    "v_pk_fma_f32 v[146:147], v[170:171], v[244:245], v[242:243] op_sel_hi:[1,0,0] neg_lo:[0,0,1] neg_hi:[0,0,1]\n"
    "v_pk_fma_f32 v[148:149], v[172:173], v[244:245], v[242:243] op_sel_hi:[1,0,0] neg_lo:[0,0,1] neg_hi:[0,0,1]\n"
    "v_pk_fma_f32 v[150:151], v[174:175], v[244:245], v[242:243] op_sel_hi:[1,0,0] neg_lo:[0,0,1] neg_hi:[0,0,1]\n"
    "v_pk_fma_f32 v[160:161], v[144:145], v[152:153], v[160:161]\n"
    "v_pk_fma_f32 v[162:163], v[146:147], v[154:155], v[162:163]\n"
    "v_pk_fma_f32 v[164:165], v[148:149], v[156:157], v[164:165]\n"
    "v_pk_fma_f32 v[166:167], v[150:151], v[158:159], v[166:167]\n"
    "v_readfirstlane_b32 s48, v160\n"
    "s_nop 1\n"
    "v_pk_add_f32 v[168:169], v[160:161], s[48:49] op_sel_hi:[1,0] neg_lo:[0,1] neg_hi:[0,1]\n"
    "v_pk_add_f32 v[170:171], v[162:163], s[48:49] op_sel_hi:[1,0] neg_lo:[0,1] neg_hi:[0,1]\n"
    "v_pk_add_f32 v[172:173], v[164:165], s[48:49] op_sel_hi:[1,0] neg_lo:[0,1] neg_hi:[0,1]\n"
    "v_pk_add_f32 v[174:175], v[166:167], s[48:49] op_sel_hi:[1,0] neg_lo:[0,1] neg_hi:[0,1]\n"
    "v_pk_add_f32 v[224:225], v[168:169], v[170:171]\n"
    "v_pk_mul_f32 v[226:227], v[168:169], v[168:169]\n"
    "v_pk_add_f32 v[224:225], v[224:225], v[172:173]\n"
    "v_pk_fma_f32 v[226:227], v[170:171], v[170:171], v[226:227]\n"
    "v_pk_add_f32 v[224:225], v[224:225], v[174:175]\n"
    "v_pk_fma_f32 v[226:227], v[172:173], v[172:173], v[226:227]\n"
    "v_pk_fma_f32 v[226:227], v[174:175], v[174:175], v[226:227]\n"
    "v_add_f32 v224, v224, v225\n"
    "v_add_f32 v226, v226, v227\n"
    "s_nop 0\n"
    "v_add_f32_dpp v224, v224, v224 row_ror:1 row_mask:0xf bank_mask:0xf\n"
    "v_add_f32_dpp v226, v226, v226 row_ror:1 row_mask:0xf bank_mask:0xf\n"
    "s_nop 0\n"
    "v_add_f32_dpp v224, v224, v224 row_ror:2 row_mask:0xf bank_mask:0xf\n"
    "v_add_f32_dpp v226, v226, v226 row_ror:2 row_mask:0xf bank_mask:0xf\n"
    "s_nop 0\n"
    "v_add_f32_dpp v224, v224, v224 row_ror:4 row_mask:0xf bank_mask:0xf\n"
    "v_add_f32_dpp v226, v226, v226 row_ror:4 row_mask:0xf bank_mask:0xf\n"
    "s_nop 0\n"
    "v_add_f32_dpp v224, v224, v224 row_ror:8 row_mask:0xf bank_mask:0xf\n"
    "v_add_f32_dpp v226, v226, v226 row_ror:8 row_mask:0xf bank_mask:0xf\n"
    "s_nop 0\n"
    "v_add_f32_dpp v224, v224, v224 row_bcast:15 row_mask:0xa bank_mask:0xf\n"
    "v_add_f32_dpp v226, v226, v226 row_bcast:15 row_mask:0xa bank_mask:0xf\n"
    "s_nop 0\n"
    "v_add_f32_dpp v224, v224, v224 row_bcast:31 row_mask:0xc bank_mask:0xf\n"
    "v_add_f32_dpp v226, v226, v226 row_bcast:31 row_mask:0xc bank_mask:0xf\n"
    "v_readlane_b32 s48, v224, 63\n"
    "v_readlane_b32 s50, v226, 63\n"
    "s_nop 0\n"
    "v_mul_f32 v242, s48, v239\n"
    "v_mul_f32 v244, s50, v239\n"
    "v_fma_f32 v244, -v242, v242, v244\n"
    "v_max_f32 v244, 0, v244\n"
    "v_add_f32 v244, v240, v244\n"
    "v_rsq_f32 v244, v244\n"
    "s_nop 0\n"
    "v_mul_f32 v242, v242, v244\n"
    "v_pk_fma_f32 v[144:145], v[168:169], v[244:245], v[242:243] op_sel_hi:[1,0,0] neg_lo:[0,0,1] neg_hi:[0,0,1]\n"
    "v_pk_fma_f32 v[146:147], v[170:171], v[244:245], v[242:243] op_sel_hi:[1,0,0] neg_lo:[0,0,1] neg_hi:[0,0,1]\n"
    "v_pk_fma_f32 v[148:149], v[172:173], v[244:245], v[242:243] op_sel_hi:[1,0,0] neg_lo:[0,0,1] neg_hi:[0,0,1]\n"
    "v_pk_fma_f32 v[150:151], v[174:175], v[244:245], v[242:243] op_sel_hi:[1,0,0] neg_lo:[0,0,1] neg_hi:[0,0,1]\n"
    "s_xor_b32 s41, s59, 0x2000\n"
    "s_add_u32 s40, s41, 4194304\n"
    "s_add_u32 s41, s41, 8388608\n"
    "v_cvt_pk_bf16_f32 v144, v144, v145\n"
    "v_cvt_pk_bf16_f32 v145, v146, v147\n"
    "v_min_u32 v144, -2, v144\n"
    "v_cvt_pk_bf16_f32 v146, v148, v149\n"
    "v_cvt_pk_bf16_f32 v147, v150, v151\n"
    "v_min_u32 v146, -2, v146\n"
    "buffer_store_dwordx4 v[144:147], v236, s[20:23], s40 offen sc1\n"
    "v_cvt_pk_bf16_f32 v160, v160, v161\n"
    "v_cvt_pk_bf16_f32 v161, v162, v163\n"
    "v_min_u32 v160, -2, v160\n"
    "v_cvt_pk_bf16_f32 v162, v164, v165\n"
    "v_cvt_pk_bf16_f32 v163, v166, v167\n"
    "v_min_u32 v162, -2, v162\n"
    "buffer_store_dwordx4 v[160:163], v236, s[20:23], s41 offen sc1\n"
    "ONX_%=:\n"
    "s_xor_b32 s33, s33, 524288\n"
    "s_xor_b32 s34, s34, 524288\n"
    "s_xor_b32 s43, s43, 32768\n"
    "s_add_u32 s60, s60, 32768\n"
    "s_add_u32 s58, s58, 65536\n"
    "s_lshr_b32 s30, s29, 1\n"
    "s_andn2_b32 s30, 1, s30\n"
    "s_add_u32 s29, s29, 1\n"
    "s_cmp_le_u32 s29, 64\n"
    "s_cbranch_scc1 OLP_%=\n"
    "s_branch OEND_%=\n"
    "OHR_%=:\n"
    "buffer_load_dword v237, v241, s[20:23], s56 offen sc1\n"
    "s_add_u32 s39, s39, 1\n"
    "s_cmp_gt_u32 s39, 262144\n"
    "s_cbranch_scc1 OEND_%=\n"
    "s_waitcnt vmcnt(0)\n"
    "s_branch OH_%=\n"
    "OIR_%=:\n"
    "buffer_load_dwordx4 v[216:219], v236, s[20:23], s35 offen sc1\n"
    "s_branch OI_%=\n"
    "OEND_%=:\n"
    "s_endpgm\n"
    "ROLE_R_%=:\n"
    "s_load_dwordx16 s[4:19], %0, 0\n"
    "s_bfe_u32 s25, %1, 0x80008\n"
    "s_bfe_u32 s29, %1, 0x20001\n"
    "s_bfe_u32 s64, %1, 0x80010\n"
    "v_mbcnt_lo_u32_b32 v0, -1, 0\n"
    "v_mbcnt_hi_u32_b32 v0, -1, v0\n"
    "v_lshrrev_b32 v2, 4, v0\n"
    "v_and_b32 v1, 15, v0\n"
    "v_lshlrev_b32 v3, 4, v1\n"
    "v_lshl_add_u32 v24, v2, 10, v3\n"
    "v_xor_b32 v25, 128, v24\n"
    "s_lshl_b32 s61, s25, 17\n"
    "s_lshl_b32 s46, s29, 8\n"
    "s_add_u32 s61, s61, s46\n"
    "s_lshl_b32 s60, s25, 8\n"
    "s_lshl_b32 s46, s29, 17\n"
    "s_add_u32 s60, s60, s46\n"
    "v_lshrrev_b32 v4, 4, v0\n"
    "v_and_b32 v5, 15, v0\n"
    "v_lshlrev_b32 v6, 1, v4\n"
    "v_xor_b32 v5, v5, v6\n"
    "v_lshlrev_b32 v5, 4, v5\n"
    "v_lshl_add_u32 v20, v4, 13, v5\n"
    "v_xor_b32 v21, 128, v20\n"
    "v_bfe_u32 v5, v0, 2, 2\n"
    "v_lshl_add_u32 v6, v4, 3, v5\n"
    "v_and_b32 v4, 1, v4\n"
    "v_lshl_add_u32 v4, v4, 2, v5\n"
    "v_lshlrev_b32 v4, 5, v4\n"
    "v_and_b32 v5, 3, v0\n"
    "v_lshlrev_b32 v5, 3, v5\n"
    "v_mov_b32 v7, s29\n"
    "v_lshl_add_u32 v5, v7, 6, v5\n"
    "v_xor_b32 v4, v4, v5\n"
    "v_lshl_add_u32 v22, v6, 8, v4\n"
    "v_xor_b32 v23, 32, v22\n"
    "s_waitcnt lgkmcnt(0)\n"
    "s_mov_b32 s20, s12\n"
    "s_and_b32 s21, s13, 0xffff\n"
    "s_mov_b32 s22, 85983232\n"
    "s_mov_b32 s23, 0x20000\n"
    "s_and_b32 s63, s64, 7\n"
    "s_lshl_b32 s63, s63, 7\n"
    "s_add_u32 s63, s63, 81788928\n"
    "buffer_load_dwordx2 v[66:67], off, s[20:23], s63 sc1\n"
    "s_cmp_eq_u32 s29, 1\n"
    "s_cbranch_scc1 RW1_%=\n"
    "s_cmp_eq_u32 s29, 2\n"
    "s_cbranch_scc1 RW2_%=\n"
    "s_cmp_eq_u32 s29, 3\n"
    "s_cbranch_scc1 RW3_%=\n"
    "RW0_%=:\n"
    "s_mov_b64 s[76:77], s[10:11]\n"
    "s_and_b32 s77, s77, 0xffff\n"
    "s_mov_b32 s78, 4194304\n"
    "s_mov_b32 s79, 0x20000\n"
    "s_mov_b32 m0, 0\n"
    "s_add_u32 s80, s61, 0\n"
    "buffer_load_dwordx4 v24, s[76:79], s80 offen offset:0  lds\n"
    "s_add_u32 s80, s61, 3072\n"
    "buffer_load_dwordx4 v24, s[76:79], s80 offen offset:1024  lds\n"
    "s_add_u32 s80, s61, 14336\n"
    "buffer_load_dwordx4 v25, s[76:79], s80 offen offset:2048  lds\n"
    "s_add_u32 s80, s61, 17408\n"
    "buffer_load_dwordx4 v25, s[76:79], s80 offen offset:3072  lds\n"
    "s_mov_b32 m0, 16384\n"
    "s_add_u32 s80, s61, 8192\n"
    "buffer_load_dwordx4 v24, s[76:79], s80 offen offset:0  lds\n"
    "s_add_u32 s80, s61, 11264\n"
    "buffer_load_dwordx4 v24, s[76:79], s80 offen offset:1024  lds\n"
    "s_add_u32 s80, s61, 22528\n"
    "buffer_load_dwordx4 v25, s[76:79], s80 offen offset:2048  lds\n"
    "s_add_u32 s80, s61, 25600\n"
    "buffer_load_dwordx4 v25, s[76:79], s80 offen offset:3072  lds\n"
    "s_mov_b32 m0, 32768\n"
    "s_add_u32 s80, s61, 32768\n"
    "buffer_load_dwordx4 v24, s[76:79], s80 offen offset:0  lds\n"
    "s_add_u32 s80, s61, 35840\n"
    "buffer_load_dwordx4 v24, s[76:79], s80 offen offset:1024  lds\n"
    "s_add_u32 s80, s61, 47104\n"
    "buffer_load_dwordx4 v25, s[76:79], s80 offen offset:2048  lds\n"
    "s_add_u32 s80, s61, 50176\n"
    "buffer_load_dwordx4 v25, s[76:79], s80 offen offset:3072  lds\n"
    "s_mov_b32 m0, 49152\n"
    "s_add_u32 s80, s61, 40960\n"
    "buffer_load_dwordx4 v24, s[76:79], s80 offen offset:0  lds\n"
    "s_add_u32 s80, s61, 44032\n"
    "buffer_load_dwordx4 v24, s[76:79], s80 offen offset:1024  lds\n"
    "s_add_u32 s80, s61, 55296\n"
    "buffer_load_dwordx4 v25, s[76:79], s80 offen offset:2048  lds\n"
    "s_add_u32 s80, s61, 58368\n"
    "buffer_load_dwordx4 v25, s[76:79], s80 offen offset:3072  lds\n"
    "s_mov_b32 m0, 65536\n"
    "s_add_u32 s80, s61, 65536\n"
    "buffer_load_dwordx4 v24, s[76:79], s80 offen offset:0  lds\n"
    "s_add_u32 s80, s61, 68608\n"
    "buffer_load_dwordx4 v24, s[76:79], s80 offen offset:1024  lds\n"
    "s_add_u32 s80, s61, 79872\n"
    "buffer_load_dwordx4 v25, s[76:79], s80 offen offset:2048  lds\n"
    "s_add_u32 s80, s61, 82944\n"
    "buffer_load_dwordx4 v25, s[76:79], s80 offen offset:3072  lds\n"
    "s_mov_b32 m0, 81920\n"
    "s_add_u32 s80, s61, 73728\n"
    "buffer_load_dwordx4 v24, s[76:79], s80 offen offset:0  lds\n"
    "s_add_u32 s80, s61, 76800\n"
    "buffer_load_dwordx4 v24, s[76:79], s80 offen offset:1024  lds\n"
    "s_add_u32 s80, s61, 88064\n"
    "buffer_load_dwordx4 v25, s[76:79], s80 offen offset:2048  lds\n"
    "s_add_u32 s80, s61, 91136\n"
    "buffer_load_dwordx4 v25, s[76:79], s80 offen offset:3072  lds\n"
    "s_mov_b32 m0, 98304\n"
    "s_add_u32 s80, s61, 98304\n"
    "buffer_load_dwordx4 v24, s[76:79], s80 offen offset:0  lds\n"
    "s_add_u32 s80, s61, 101376\n"
    "buffer_load_dwordx4 v24, s[76:79], s80 offen offset:1024  lds\n"
    "s_add_u32 s80, s61, 112640\n"
    "buffer_load_dwordx4 v25, s[76:79], s80 offen offset:2048  lds\n"
    "s_add_u32 s80, s61, 115712\n"
    "buffer_load_dwordx4 v25, s[76:79], s80 offen offset:3072  lds\n"
    "s_mov_b32 m0, 114688\n"
    "s_add_u32 s80, s61, 106496\n"
    "buffer_load_dwordx4 v24, s[76:79], s80 offen offset:0  lds\n"
    "s_add_u32 s80, s61, 109568\n"
    "buffer_load_dwordx4 v24, s[76:79], s80 offen offset:1024  lds\n"
    "s_add_u32 s80, s61, 120832\n"
    "buffer_load_dwordx4 v25, s[76:79], s80 offen offset:2048  lds\n"
    "s_add_u32 s80, s61, 123904\n"
    "buffer_load_dwordx4 v25, s[76:79], s80 offen offset:3072  lds\n"
    "s_bfe_u32 s24, %1, 0x10000\n"
    "s_and_b32 s26, s16, 0xffff\n"
    "s_and_b32 s27, s17, 0xffff\n"
    "s_mov_b32 s48, 0x3e0293ee\n"
    "s_mov_b32 s96, 0x00ff00ff\n"
    "s_mov_b32 s97, s96\n"
    "s_not_b64 s[98:99], s[96:97]\n"
    "v_lshlrev_b32 v107, 4, v0\n"
    "v_mov_b64 v[64:65], 0\n"
    "v_lshlrev_b32 v5, 4, v0\n"
    "v_add_u32 v117, 135168, v5\n"
    "v_and_b32 v3, 7, v0\n"
    "v_bfe_u32 v4, v0, 3, 1\n"
    "v_mov_b32 v5, 0x4444\n"
    "v_mov_b32 v6, 0xeeee\n"
    "v_cmp_eq_u32 vcc, 1, v4\n"
    "s_nop 1\n"
    "v_cndmask_b32 v106, v5, v6, vcc\n"
    "v_add_u32 v108, 0x10000, v107\n"
    "v_add_u32 v109, 131072, v107\n"
    "v_and_b32 v5, 31, v0\n"
    "v_lshlrev_b32 v5, 4, v5\n"
    "v_add_u32 v110, 135680, v5\n"
    "v_lshl_add_u32 v5, v2, 3, v3\n"
    "v_lshlrev_b32 v5, 4, v5\n"
    "v_add_u32 v112, 137216, v5\n"
    "v_lshl_add_u32 v111, v4, 3, v112\n"
    "v_lshrrev_b32 v5, 1, v2\n"
    "v_lshl_add_u32 v6, v5, 1, v4\n"
    "v_lshlrev_b32 v6, 14, v6\n"
    "v_and_b32 v7, 1, v2\n"
    "v_lshl_add_u32 v8, v7, 3, v3\n"
    "v_lshl_add_u32 v113, v8, 4, v6\n"
    "v_mul_u32_u24 v8, 2176, v7\n"
    "v_lshl_add_u32 v8, v5, 10, v8\n"
    "v_lshrrev_b32 v9, 1, v1\n"
    "v_lshl_add_u32 v26, v9, 3, v8\n"
    "v_xor_b32 v27, 128, v26\n"
    "v_xor_b32 v28, 2176, v26\n"
    "v_xor_b32 v29, 2048, v26\n"
    "v_and_b32 v10, 1, v0\n"
    "v_mul_u32_u24 v8, 0x020202, v10\n"
    "v_lshl_add_u32 v8, v10, 25, v8\n"
    "v_add_u32 v68, 0x05040100, v8\n"
    "v_and_b32 v8, 3, v0\n"
    "v_lshl_add_u32 v8, v5, 2, v8\n"
    "v_bfe_u32 v9, v0, 2, 1\n"
    "v_lshl_add_u32 v8, v9, 3, v8\n"
    "v_lshlrev_b32 v8, 4, v8\n"
    "v_lshl_add_u32 v8, v7, 3, v8\n"
    "v_lshl_add_u32 v114, v4, 20, v8\n"
    "v_lshlrev_b32 v10, 4, v10\n"
    "v_mov_b32 v11, 0x3f80\n"
    "v_lshlrev_b32 v11, v10, v11\n"
    "v_cmp_eq_u32 vcc, v9, v7\n"
    "s_nop 1\n"
    "v_cndmask_b32 v12, 0, v11, vcc\n"
    "v_bfe_u32 v13, v0, 1, 1\n"
    "v_lshl_add_u32 v13, v4, 1, v13\n"
    "v_cmp_eq_u32 vcc, 0, v13\n"
    "s_nop 1\n"
    "v_cndmask_b32 v120, 0, v12, vcc\n"
    "v_cmp_eq_u32 vcc, 1, v13\n"
    "s_nop 1\n"
    "v_cndmask_b32 v121, 0, v12, vcc\n"
    "v_cmp_eq_u32 vcc, 2, v13\n"
    "s_nop 1\n"
    "v_cndmask_b32 v122, 0, v12, vcc\n"
    "v_cmp_eq_u32 vcc, 3, v13\n"
    "s_nop 1\n"
    "v_cndmask_b32 v123, 0, v12, vcc\n"
    "v_bfe_u32 v13, v0, 1, 2\n"
    "v_cmp_eq_u32 vcc, 0, v13\n"
    "s_nop 1\n"
    "v_cndmask_b32 v16, 0, v11, vcc\n"
    "v_cmp_eq_u32 vcc, 1, v13\n"
    "s_nop 1\n"
    "v_cndmask_b32 v17, 0, v11, vcc\n"
    "v_cmp_eq_u32 vcc, 2, v13\n"
    "s_nop 1\n"
    "v_cndmask_b32 v18, 0, v11, vcc\n"
    "v_cmp_eq_u32 vcc, 3, v13\n"
    "s_nop 1\n"
    "v_cndmask_b32 v19, 0, v11, vcc\n"
    "v_mov_b32 v124, 0x3f803f80\n"
    "v_mov_b32 v125, 0x3f803f80\n"
    "v_mov_b32 v126, 0x3f003f00\n"
    "v_mov_b32 v127, 0\n"
    "v_mov_b64 v[118:119], 0\n"
    "s_waitcnt vmcnt(28) lgkmcnt(0)\n"
    "s_barrier\n"
    "ds_read_b64 v[70:71], v26 offset:0\n"
    "ds_read_b64 v[72:73], v26 offset:256\n"
    "ds_read_b64 v[74:75], v26 offset:512\n"
    "ds_read_b64 v[76:77], v26 offset:768\n"
    "ds_read_b64 v[78:79], v28 offset:64\n"
    "ds_read_b64 v[80:81], v28 offset:320\n"
    "ds_read_b64 v[82:83], v28 offset:576\n"
    "ds_read_b64 v[84:85], v28 offset:832\n"
    "ds_read_b64 v[86:87], v27 offset:0\n"
    "ds_read_b64 v[88:89], v27 offset:256\n"
    "ds_read_b64 v[90:91], v27 offset:512\n"
    "ds_read_b64 v[92:93], v27 offset:768\n"
    "ds_read_b64 v[94:95], v29 offset:64\n"
    "ds_read_b64 v[96:97], v29 offset:320\n"
    "ds_read_b64 v[98:99], v29 offset:576\n"
    "ds_read_b64 v[100:101], v29 offset:832\n"
    "s_waitcnt lgkmcnt(14)\n"
    "v_perm_b32 v128, v72, v70, v68\n"
    "v_perm_b32 v160, v73, v71, v68\n"
    "s_waitcnt lgkmcnt(12)\n"
    "v_perm_b32 v130, v76, v74, v68\n"
    "v_perm_b32 v162, v77, v75, v68\n"
    "s_waitcnt lgkmcnt(10)\n"
    "v_perm_b32 v129, v80, v78, v68\n"
    "v_perm_b32 v161, v81, v79, v68\n"
    "s_waitcnt lgkmcnt(8)\n"
    "v_perm_b32 v131, v84, v82, v68\n"
    "v_perm_b32 v163, v85, v83, v68\n"
    "s_waitcnt lgkmcnt(6)\n"
    "v_perm_b32 v192, v88, v86, v68\n"
    "v_perm_b32 v224, v89, v87, v68\n"
    "s_waitcnt lgkmcnt(4)\n"
    "v_perm_b32 v194, v92, v90, v68\n"
    "v_perm_b32 v226, v93, v91, v68\n"
    "s_waitcnt lgkmcnt(2)\n"
    "v_perm_b32 v193, v96, v94, v68\n"
    "v_perm_b32 v225, v97, v95, v68\n"
    "s_waitcnt lgkmcnt(0)\n"
    "v_perm_b32 v195, v100, v98, v68\n"
    "v_perm_b32 v227, v101, v99, v68\n"
    "s_waitcnt vmcnt(24) lgkmcnt(0)\n"
    "s_barrier\n"
    "s_mov_b64 s[76:77], s[4:5]\n"
    "s_and_b32 s77, s77, 0xffff\n"
    "s_mov_b32 s78, 4194304\n"
    "s_mov_b32 s79, 0x20000\n"
    "s_mov_b32 m0, 0\n"
    "s_add_u32 s80, s60, 0\n"
    "buffer_load_dwordx4 v20, s[76:79], s80 offen offset:0  lds\n"
    "s_add_u32 s80, s60, 31744\n"
    "buffer_load_dwordx4 v20, s[76:79], s80 offen offset:1024  lds\n"
    "s_add_u32 s80, s60, 63488\n"
    "buffer_load_dwordx4 v21, s[76:79], s80 offen offset:2048  lds\n"
    "s_add_u32 s80, s60, 95232\n"
    "buffer_load_dwordx4 v21, s[76:79], s80 offen offset:3072  lds\n"
    "ds_read_b64 v[70:71], v26 offset:16384\n"
    "ds_read_b64 v[72:73], v26 offset:16640\n"
    "ds_read_b64 v[74:75], v26 offset:16896\n"
    "ds_read_b64 v[76:77], v26 offset:17152\n"
    "ds_read_b64 v[78:79], v28 offset:16448\n"
    "ds_read_b64 v[80:81], v28 offset:16704\n"
    "ds_read_b64 v[82:83], v28 offset:16960\n"
    "ds_read_b64 v[84:85], v28 offset:17216\n"
    "ds_read_b64 v[86:87], v27 offset:16384\n"
    "ds_read_b64 v[88:89], v27 offset:16640\n"
    "ds_read_b64 v[90:91], v27 offset:16896\n"
    "ds_read_b64 v[92:93], v27 offset:17152\n"
    "ds_read_b64 v[94:95], v29 offset:16448\n"
    "ds_read_b64 v[96:97], v29 offset:16704\n"
    "ds_read_b64 v[98:99], v29 offset:16960\n"
    "ds_read_b64 v[100:101], v29 offset:17216\n"
    "s_waitcnt lgkmcnt(14)\n"
    "v_perm_b32 v132, v72, v70, v68\n"
    "v_perm_b32 v164, v73, v71, v68\n"
    "s_waitcnt lgkmcnt(12)\n"
    "v_perm_b32 v134, v76, v74, v68\n"
    "v_perm_b32 v166, v77, v75, v68\n"
    "s_waitcnt lgkmcnt(10)\n"
    "v_perm_b32 v133, v80, v78, v68\n"
    "v_perm_b32 v165, v81, v79, v68\n"
    "s_waitcnt lgkmcnt(8)\n"
    "v_perm_b32 v135, v84, v82, v68\n"
    "v_perm_b32 v167, v85, v83, v68\n"
    "s_waitcnt lgkmcnt(6)\n"
    "v_perm_b32 v196, v88, v86, v68\n"
    "v_perm_b32 v228, v89, v87, v68\n"
    "s_waitcnt lgkmcnt(4)\n"
    "v_perm_b32 v198, v92, v90, v68\n"
    "v_perm_b32 v230, v93, v91, v68\n"
    "s_waitcnt lgkmcnt(2)\n"
    "v_perm_b32 v197, v96, v94, v68\n"
    "v_perm_b32 v229, v97, v95, v68\n"
    "s_waitcnt lgkmcnt(0)\n"
    "v_perm_b32 v199, v100, v98, v68\n"
    "v_perm_b32 v231, v101, v99, v68\n"
    "s_waitcnt vmcnt(24) lgkmcnt(0)\n"
    "s_barrier\n"
    "s_mov_b32 m0, 16384\n"
    "s_add_u32 s80, s60, 524288\n"
    "buffer_load_dwordx4 v20, s[76:79], s80 offen offset:0  lds\n"
    "s_add_u32 s80, s60, 556032\n"
    "buffer_load_dwordx4 v20, s[76:79], s80 offen offset:1024  lds\n"
    "s_add_u32 s80, s60, 587776\n"
    "buffer_load_dwordx4 v21, s[76:79], s80 offen offset:2048  lds\n"
    "s_add_u32 s80, s60, 619520\n"
    "buffer_load_dwordx4 v21, s[76:79], s80 offen offset:3072  lds\n"
    "ds_read_b64 v[70:71], v26 offset:32768\n"
    "ds_read_b64 v[72:73], v26 offset:33024\n"
    "ds_read_b64 v[74:75], v26 offset:33280\n"
    "ds_read_b64 v[76:77], v26 offset:33536\n"
    "ds_read_b64 v[78:79], v26 offset:32832\n"
    "ds_read_b64 v[80:81], v26 offset:33088\n"
    "ds_read_b64 v[82:83], v26 offset:33344\n"
    "ds_read_b64 v[84:85], v26 offset:33600\n"
    "ds_read_b64 v[86:87], v27 offset:32768\n"
    "ds_read_b64 v[88:89], v27 offset:33024\n"
    "ds_read_b64 v[90:91], v27 offset:33280\n"
    "ds_read_b64 v[92:93], v27 offset:33536\n"
    "ds_read_b64 v[94:95], v27 offset:32832\n"
    "ds_read_b64 v[96:97], v27 offset:33088\n"
    "ds_read_b64 v[98:99], v27 offset:33344\n"
    "ds_read_b64 v[100:101], v27 offset:33600\n"
    "s_waitcnt lgkmcnt(14)\n"
    "v_perm_b32 v136, v72, v70, v68\n"
    "v_perm_b32 v168, v73, v71, v68\n"
    "s_waitcnt lgkmcnt(12)\n"
    "v_perm_b32 v138, v76, v74, v68\n"
    "v_perm_b32 v170, v77, v75, v68\n"
    "s_waitcnt lgkmcnt(10)\n"
    "v_perm_b32 v137, v80, v78, v68\n"
    "v_perm_b32 v169, v81, v79, v68\n"
    "s_waitcnt lgkmcnt(8)\n"
    "v_perm_b32 v139, v84, v82, v68\n"
    "v_perm_b32 v171, v85, v83, v68\n"
    "s_waitcnt lgkmcnt(6)\n"
    "v_perm_b32 v200, v88, v86, v68\n"
    "v_perm_b32 v232, v89, v87, v68\n"
    "s_waitcnt lgkmcnt(4)\n"
    "v_perm_b32 v202, v92, v90, v68\n"
    "v_perm_b32 v234, v93, v91, v68\n"
    "s_waitcnt lgkmcnt(2)\n"
    "v_perm_b32 v201, v96, v94, v68\n"
    "v_perm_b32 v233, v97, v95, v68\n"
    "s_waitcnt lgkmcnt(0)\n"
    "v_perm_b32 v203, v100, v98, v68\n"
    "v_perm_b32 v235, v101, v99, v68\n"
    "s_waitcnt vmcnt(24) lgkmcnt(0)\n"
    "s_barrier\n"
    "s_mov_b32 m0, 32768\n"
    "s_add_u32 s80, s60, 1048576\n"
    "buffer_load_dwordx4 v20, s[76:79], s80 offen offset:0  lds\n"
    "s_add_u32 s80, s60, 1080320\n"
    "buffer_load_dwordx4 v20, s[76:79], s80 offen offset:1024  lds\n"
    "s_add_u32 s80, s60, 1112064\n"
    "buffer_load_dwordx4 v21, s[76:79], s80 offen offset:2048  lds\n"
    "s_add_u32 s80, s60, 1143808\n"
    "buffer_load_dwordx4 v21, s[76:79], s80 offen offset:3072  lds\n"
    "ds_read_b64 v[70:71], v26 offset:49152\n"
    "ds_read_b64 v[72:73], v26 offset:49408\n"
    "ds_read_b64 v[74:75], v26 offset:49664\n"
    "ds_read_b64 v[76:77], v26 offset:49920\n"
    "ds_read_b64 v[78:79], v26 offset:49216\n"
    "ds_read_b64 v[80:81], v26 offset:49472\n"
    "ds_read_b64 v[82:83], v26 offset:49728\n"
    "ds_read_b64 v[84:85], v26 offset:49984\n"
    "ds_read_b64 v[86:87], v27 offset:49152\n"
    "ds_read_b64 v[88:89], v27 offset:49408\n"
    "ds_read_b64 v[90:91], v27 offset:49664\n"
    "ds_read_b64 v[92:93], v27 offset:49920\n"
    "ds_read_b64 v[94:95], v27 offset:49216\n"
    "ds_read_b64 v[96:97], v27 offset:49472\n"
    "ds_read_b64 v[98:99], v27 offset:49728\n"
    "ds_read_b64 v[100:101], v27 offset:49984\n"
    "s_waitcnt lgkmcnt(14)\n"
    "v_perm_b32 v140, v72, v70, v68\n"
    "v_perm_b32 v172, v73, v71, v68\n"
    "s_waitcnt lgkmcnt(12)\n"
    "v_perm_b32 v142, v76, v74, v68\n"
    "v_perm_b32 v174, v77, v75, v68\n"
    "s_waitcnt lgkmcnt(10)\n"
    "v_perm_b32 v141, v80, v78, v68\n"
    "v_perm_b32 v173, v81, v79, v68\n"
    "s_waitcnt lgkmcnt(8)\n"
    "v_perm_b32 v143, v84, v82, v68\n"
    "v_perm_b32 v175, v85, v83, v68\n"
    "s_waitcnt lgkmcnt(6)\n"
    "v_perm_b32 v204, v88, v86, v68\n"
    "v_perm_b32 v236, v89, v87, v68\n"
    "s_waitcnt lgkmcnt(4)\n"
    "v_perm_b32 v206, v92, v90, v68\n"
    "v_perm_b32 v238, v93, v91, v68\n"
    "s_waitcnt lgkmcnt(2)\n"
    "v_perm_b32 v205, v96, v94, v68\n"
    "v_perm_b32 v237, v97, v95, v68\n"
    "s_waitcnt lgkmcnt(0)\n"
    "v_perm_b32 v207, v100, v98, v68\n"
    "v_perm_b32 v239, v101, v99, v68\n"
    "s_waitcnt vmcnt(24) lgkmcnt(0)\n"
    "s_barrier\n"
    "s_mov_b32 m0, 49152\n"
    "s_add_u32 s80, s60, 1572864\n"
    "buffer_load_dwordx4 v20, s[76:79], s80 offen offset:0  lds\n"
    "s_add_u32 s80, s60, 1604608\n"
    "buffer_load_dwordx4 v20, s[76:79], s80 offen offset:1024  lds\n"
    "s_add_u32 s80, s60, 1636352\n"
    "buffer_load_dwordx4 v21, s[76:79], s80 offen offset:2048  lds\n"
    "s_add_u32 s80, s60, 1668096\n"
    "buffer_load_dwordx4 v21, s[76:79], s80 offen offset:3072  lds\n"
    "v_add_u32 v22, 65536, v22\n"
    "v_add_u32 v23, 65536, v23\n"
    "v_add_u32 v26, 65536, v26\n"
    "v_add_u32 v27, 65536, v27\n"
    "v_add_u32 v28, 65536, v28\n"
    "v_add_u32 v29, 65536, v29\n"
    "ds_read_b64 v[70:71], v26 offset:0\n"
    "ds_read_b64 v[72:73], v26 offset:256\n"
    "ds_read_b64 v[74:75], v26 offset:512\n"
    "ds_read_b64 v[76:77], v26 offset:768\n"
    "ds_read_b64 v[78:79], v26 offset:64\n"
    "ds_read_b64 v[80:81], v26 offset:320\n"
    "ds_read_b64 v[82:83], v26 offset:576\n"
    "ds_read_b64 v[84:85], v26 offset:832\n"
    "ds_read_b64 v[86:87], v27 offset:0\n"
    "ds_read_b64 v[88:89], v27 offset:256\n"
    "ds_read_b64 v[90:91], v27 offset:512\n"
    "ds_read_b64 v[92:93], v27 offset:768\n"
    "ds_read_b64 v[94:95], v27 offset:64\n"
    "ds_read_b64 v[96:97], v27 offset:320\n"
    "ds_read_b64 v[98:99], v27 offset:576\n"
    "ds_read_b64 v[100:101], v27 offset:832\n"
    "s_waitcnt lgkmcnt(14)\n"
    "v_perm_b32 v144, v72, v70, v68\n"
    "v_perm_b32 v176, v73, v71, v68\n"
    "s_waitcnt lgkmcnt(12)\n"
    "v_perm_b32 v146, v76, v74, v68\n"
    "v_perm_b32 v178, v77, v75, v68\n"
    "s_waitcnt lgkmcnt(10)\n"
    "v_perm_b32 v145, v80, v78, v68\n"
    "v_perm_b32 v177, v81, v79, v68\n"
    "s_waitcnt lgkmcnt(8)\n"
    "v_perm_b32 v147, v84, v82, v68\n"
    "v_perm_b32 v179, v85, v83, v68\n"
    "s_waitcnt lgkmcnt(6)\n"
    "v_perm_b32 v208, v88, v86, v68\n"
    "v_perm_b32 v240, v89, v87, v68\n"
    "s_waitcnt lgkmcnt(4)\n"
    "v_perm_b32 v210, v92, v90, v68\n"
    "v_perm_b32 v242, v93, v91, v68\n"
    "s_waitcnt lgkmcnt(2)\n"
    "v_perm_b32 v209, v96, v94, v68\n"
    "v_perm_b32 v241, v97, v95, v68\n"
    "s_waitcnt lgkmcnt(0)\n"
    "v_perm_b32 v211, v100, v98, v68\n"
    "v_perm_b32 v243, v101, v99, v68\n"
    "s_waitcnt vmcnt(24) lgkmcnt(0)\n"
    "s_barrier\n"
    "s_mov_b32 m0, 65536\n"
    "s_add_u32 s80, s60, 2097152\n"
    "buffer_load_dwordx4 v20, s[76:79], s80 offen offset:0  lds\n"
    "s_add_u32 s80, s60, 2128896\n"
    "buffer_load_dwordx4 v20, s[76:79], s80 offen offset:1024  lds\n"
    "s_add_u32 s80, s60, 2160640\n"
    "buffer_load_dwordx4 v21, s[76:79], s80 offen offset:2048  lds\n"
    "s_add_u32 s80, s60, 2192384\n"
    "buffer_load_dwordx4 v21, s[76:79], s80 offen offset:3072  lds\n"
    "ds_read_b64 v[70:71], v26 offset:16384\n"
    "ds_read_b64 v[72:73], v26 offset:16640\n"
    "ds_read_b64 v[74:75], v26 offset:16896\n"
    "ds_read_b64 v[76:77], v26 offset:17152\n"
    "ds_read_b64 v[78:79], v26 offset:16448\n"
    "ds_read_b64 v[80:81], v26 offset:16704\n"
    "ds_read_b64 v[82:83], v26 offset:16960\n"
    "ds_read_b64 v[84:85], v26 offset:17216\n"
    "ds_read_b64 v[86:87], v27 offset:16384\n"
    "ds_read_b64 v[88:89], v27 offset:16640\n"
    "ds_read_b64 v[90:91], v27 offset:16896\n"
    "ds_read_b64 v[92:93], v27 offset:17152\n"
    "ds_read_b64 v[94:95], v27 offset:16448\n"
    "ds_read_b64 v[96:97], v27 offset:16704\n"
    "ds_read_b64 v[98:99], v27 offset:16960\n"
    "ds_read_b64 v[100:101], v27 offset:17216\n"
    "s_waitcnt lgkmcnt(14)\n"
    "v_perm_b32 v148, v72, v70, v68\n"
    "v_perm_b32 v180, v73, v71, v68\n"
    "s_waitcnt lgkmcnt(12)\n"
    "v_perm_b32 v150, v76, v74, v68\n"
    "v_perm_b32 v182, v77, v75, v68\n"
    "s_waitcnt lgkmcnt(10)\n"
    "v_perm_b32 v149, v80, v78, v68\n"
    "v_perm_b32 v181, v81, v79, v68\n"
    "s_waitcnt lgkmcnt(8)\n"
    "v_perm_b32 v151, v84, v82, v68\n"
    "v_perm_b32 v183, v85, v83, v68\n"
    "s_waitcnt lgkmcnt(6)\n"
    "v_perm_b32 v212, v88, v86, v68\n"
    "v_perm_b32 v244, v89, v87, v68\n"
    "s_waitcnt lgkmcnt(4)\n"
    "v_perm_b32 v214, v92, v90, v68\n"
    "v_perm_b32 v246, v93, v91, v68\n"
    "s_waitcnt lgkmcnt(2)\n"
    "v_perm_b32 v213, v96, v94, v68\n"
    "v_perm_b32 v245, v97, v95, v68\n"
    "s_waitcnt lgkmcnt(0)\n"
    "v_perm_b32 v215, v100, v98, v68\n"
    "v_perm_b32 v247, v101, v99, v68\n"
    "s_waitcnt vmcnt(24) lgkmcnt(0)\n"
    "s_barrier\n"
    "s_mov_b32 m0, 81920\n"
    "s_add_u32 s80, s60, 2621440\n"
    "buffer_load_dwordx4 v20, s[76:79], s80 offen offset:0  lds\n"
    "s_add_u32 s80, s60, 2653184\n"
    "buffer_load_dwordx4 v20, s[76:79], s80 offen offset:1024  lds\n"
    "s_add_u32 s80, s60, 2684928\n"
    "buffer_load_dwordx4 v21, s[76:79], s80 offen offset:2048  lds\n"
    "s_add_u32 s80, s60, 2716672\n"
    "buffer_load_dwordx4 v21, s[76:79], s80 offen offset:3072  lds\n"
    "ds_read_b64 v[70:71], v26 offset:32768\n"
    "ds_read_b64 v[72:73], v26 offset:33024\n"
    "ds_read_b64 v[74:75], v26 offset:33280\n"
    "ds_read_b64 v[76:77], v26 offset:33536\n"
    "ds_read_b64 v[78:79], v26 offset:32832\n"
    "ds_read_b64 v[80:81], v26 offset:33088\n"
    "ds_read_b64 v[82:83], v26 offset:33344\n"
    "ds_read_b64 v[84:85], v26 offset:33600\n"
    "ds_read_b64 v[86:87], v27 offset:32768\n"
    "ds_read_b64 v[88:89], v27 offset:33024\n"
    "ds_read_b64 v[90:91], v27 offset:33280\n"
    "ds_read_b64 v[92:93], v27 offset:33536\n"
    "ds_read_b64 v[94:95], v27 offset:32832\n"
    "ds_read_b64 v[96:97], v27 offset:33088\n"
    "ds_read_b64 v[98:99], v27 offset:33344\n"
    "ds_read_b64 v[100:101], v27 offset:33600\n"
    "s_waitcnt lgkmcnt(14)\n"
    "v_perm_b32 v152, v72, v70, v68\n"
    "v_perm_b32 v184, v73, v71, v68\n"
    "s_waitcnt lgkmcnt(12)\n"
    "v_perm_b32 v154, v76, v74, v68\n"
    "v_perm_b32 v186, v77, v75, v68\n"
    "s_waitcnt lgkmcnt(10)\n"
    "v_perm_b32 v153, v80, v78, v68\n"
    "v_perm_b32 v185, v81, v79, v68\n"
    "s_waitcnt lgkmcnt(8)\n"
    "v_perm_b32 v155, v84, v82, v68\n"
    "v_perm_b32 v187, v85, v83, v68\n"
    "s_waitcnt lgkmcnt(6)\n"
    "v_perm_b32 v216, v88, v86, v68\n"
    "v_perm_b32 v248, v89, v87, v68\n"
    "s_waitcnt lgkmcnt(4)\n"
    "v_perm_b32 v218, v92, v90, v68\n"
    "v_perm_b32 v250, v93, v91, v68\n"
    "s_waitcnt lgkmcnt(2)\n"
    "v_perm_b32 v217, v96, v94, v68\n"
    "v_perm_b32 v249, v97, v95, v68\n"
    "s_waitcnt lgkmcnt(0)\n"
    "v_perm_b32 v219, v100, v98, v68\n"
    "v_perm_b32 v251, v101, v99, v68\n"
    "s_waitcnt vmcnt(24) lgkmcnt(0)\n"
    "s_barrier\n"
    "s_mov_b32 m0, 98304\n"
    "s_add_u32 s80, s60, 3145728\n"
    "buffer_load_dwordx4 v20, s[76:79], s80 offen offset:0  lds\n"
    "s_add_u32 s80, s60, 3177472\n"
    "buffer_load_dwordx4 v20, s[76:79], s80 offen offset:1024  lds\n"
    "s_add_u32 s80, s60, 3209216\n"
    "buffer_load_dwordx4 v21, s[76:79], s80 offen offset:2048  lds\n"
    "s_add_u32 s80, s60, 3240960\n"
    "buffer_load_dwordx4 v21, s[76:79], s80 offen offset:3072  lds\n"
    "ds_read_b64 v[70:71], v26 offset:49152\n"
    "ds_read_b64 v[72:73], v26 offset:49408\n"
    "ds_read_b64 v[74:75], v26 offset:49664\n"
    "ds_read_b64 v[76:77], v26 offset:49920\n"
    "ds_read_b64 v[78:79], v26 offset:49216\n"
    "ds_read_b64 v[80:81], v26 offset:49472\n"
    "ds_read_b64 v[82:83], v26 offset:49728\n"
    "ds_read_b64 v[84:85], v26 offset:49984\n"
    "ds_read_b64 v[86:87], v27 offset:49152\n"
    "ds_read_b64 v[88:89], v27 offset:49408\n"
    "ds_read_b64 v[90:91], v27 offset:49664\n"
    "ds_read_b64 v[92:93], v27 offset:49920\n"
    "ds_read_b64 v[94:95], v27 offset:49216\n"
    "ds_read_b64 v[96:97], v27 offset:49472\n"
    "ds_read_b64 v[98:99], v27 offset:49728\n"
    "ds_read_b64 v[100:101], v27 offset:49984\n"
    "s_waitcnt lgkmcnt(14)\n"
    "v_perm_b32 v156, v72, v70, v68\n"
    "v_perm_b32 v188, v73, v71, v68\n"
    "s_waitcnt lgkmcnt(12)\n"
    "v_perm_b32 v158, v76, v74, v68\n"
    "v_perm_b32 v190, v77, v75, v68\n"
    "s_waitcnt lgkmcnt(10)\n"
    "v_perm_b32 v157, v80, v78, v68\n"
    "v_perm_b32 v189, v81, v79, v68\n"
    "s_waitcnt lgkmcnt(8)\n"
    "v_perm_b32 v159, v84, v82, v68\n"
    "v_perm_b32 v191, v85, v83, v68\n"
    "s_waitcnt lgkmcnt(6)\n"
    "v_perm_b32 v220, v88, v86, v68\n"
    "v_perm_b32 v252, v89, v87, v68\n"
    "s_waitcnt lgkmcnt(4)\n"
    "v_perm_b32 v222, v92, v90, v68\n"
    "v_perm_b32 v254, v93, v91, v68\n"
    "s_waitcnt lgkmcnt(2)\n"
    "v_perm_b32 v221, v96, v94, v68\n"
    "v_perm_b32 v253, v97, v95, v68\n"
    "s_waitcnt lgkmcnt(0)\n"
    "v_perm_b32 v223, v100, v98, v68\n"
    "v_perm_b32 v255, v101, v99, v68\n"
    "s_waitcnt vmcnt(24) lgkmcnt(0)\n"
    "s_barrier\n"
    "s_mov_b32 m0, 114688\n"
    "s_add_u32 s80, s60, 3670016\n"
    "buffer_load_dwordx4 v20, s[76:79], s80 offen offset:0  lds\n"
    "s_add_u32 s80, s60, 3701760\n"
    "buffer_load_dwordx4 v20, s[76:79], s80 offen offset:1024  lds\n"
    "s_add_u32 s80, s60, 3733504\n"
    "buffer_load_dwordx4 v21, s[76:79], s80 offen offset:2048  lds\n"
    "s_add_u32 s80, s60, 3765248\n"
    "buffer_load_dwordx4 v21, s[76:79], s80 offen offset:3072  lds\n"
    "buffer_load_dwordx2 v[4:5], off, s[20:23], s63 sc1\n"
    "v_add_u32 v22, 4294901760, v22\n"
    "v_add_u32 v23, 4294901760, v23\n"
    "v_add_u32 v26, 4294901760, v26\n"
    "v_add_u32 v27, 4294901760, v27\n"
    "v_add_u32 v28, 4294901760, v28\n"
    "v_add_u32 v29, 4294901760, v29\n"
    "ds_read_b64_tr_b16 a[0:1], v22 offset:0\n"
    "ds_read_b64_tr_b16 a[64:65], v23 offset:0\n"
    "ds_read_b64_tr_b16 a[2:3], v22 offset:1024\n"
    "ds_read_b64_tr_b16 a[66:67], v23 offset:1024\n"
    "ds_read_b64_tr_b16 a[4:5], v22 offset:8192\n"
    "ds_read_b64_tr_b16 a[68:69], v23 offset:8192\n"
    "ds_read_b64_tr_b16 a[6:7], v22 offset:9216\n"
    "ds_read_b64_tr_b16 a[70:71], v23 offset:9216\n"
    "s_waitcnt vmcnt(25) lgkmcnt(0)\n"
    "s_barrier\n"
    "s_mov_b64 s[76:77], s[8:9]\n"
    "s_and_b32 s77, s77, 0xffff\n"
    "s_mov_b32 s78, 4194304\n"
    "s_mov_b32 s79, 0x20000\n"
    "s_mov_b32 m0, 0\n"
    "s_add_u32 s80, s60, 0\n"
    "buffer_load_dwordx4 v20, s[76:79], s80 offen offset:0  lds\n"
    "s_add_u32 s80, s60, 31744\n"
    "buffer_load_dwordx4 v20, s[76:79], s80 offen offset:1024  lds\n"
    "s_add_u32 s80, s60, 63488\n"
    "buffer_load_dwordx4 v21, s[76:79], s80 offen offset:2048  lds\n"
    "s_add_u32 s80, s60, 95232\n"
    "buffer_load_dwordx4 v21, s[76:79], s80 offen offset:3072  lds\n"
    "ds_read_b64_tr_b16 a[8:9], v22 offset:16384\n"
    "ds_read_b64_tr_b16 a[72:73], v23 offset:16384\n"
    "ds_read_b64_tr_b16 a[10:11], v22 offset:17408\n"
    "ds_read_b64_tr_b16 a[74:75], v23 offset:17408\n"
    "ds_read_b64_tr_b16 a[12:13], v22 offset:24576\n"
    "ds_read_b64_tr_b16 a[76:77], v23 offset:24576\n"
    "ds_read_b64_tr_b16 a[14:15], v22 offset:25600\n"
    "ds_read_b64_tr_b16 a[78:79], v23 offset:25600\n"
    "s_waitcnt vmcnt(25) lgkmcnt(0)\n"
    "s_barrier\n"
    "s_mov_b32 m0, 16384\n"
    "s_add_u32 s80, s60, 524288\n"
    "buffer_load_dwordx4 v20, s[76:79], s80 offen offset:0  lds\n"
    "s_add_u32 s80, s60, 556032\n"
    "buffer_load_dwordx4 v20, s[76:79], s80 offen offset:1024  lds\n"
    "s_add_u32 s80, s60, 587776\n"
    "buffer_load_dwordx4 v21, s[76:79], s80 offen offset:2048  lds\n"
    "s_add_u32 s80, s60, 619520\n"
    "buffer_load_dwordx4 v21, s[76:79], s80 offen offset:3072  lds\n"
    "ds_read_b64_tr_b16 a[16:17], v22 offset:32768\n"
    "ds_read_b64_tr_b16 a[80:81], v23 offset:32768\n"
    "ds_read_b64_tr_b16 a[18:19], v22 offset:33792\n"
    "ds_read_b64_tr_b16 a[82:83], v23 offset:33792\n"
    "ds_read_b64_tr_b16 a[20:21], v22 offset:40960\n"
    "ds_read_b64_tr_b16 a[84:85], v23 offset:40960\n"
    "ds_read_b64_tr_b16 a[22:23], v22 offset:41984\n"
    "ds_read_b64_tr_b16 a[86:87], v23 offset:41984\n"
    "s_waitcnt vmcnt(25) lgkmcnt(0)\n"
    "s_barrier\n"
    "s_mov_b32 m0, 32768\n"
    "s_add_u32 s80, s60, 1048576\n"
    "buffer_load_dwordx4 v20, s[76:79], s80 offen offset:0  lds\n"
    "s_add_u32 s80, s60, 1080320\n"
    "buffer_load_dwordx4 v20, s[76:79], s80 offen offset:1024  lds\n"
    "s_add_u32 s80, s60, 1112064\n"
    "buffer_load_dwordx4 v21, s[76:79], s80 offen offset:2048  lds\n"
    "s_add_u32 s80, s60, 1143808\n"
    "buffer_load_dwordx4 v21, s[76:79], s80 offen offset:3072  lds\n"
    "ds_read_b64_tr_b16 a[24:25], v22 offset:49152\n"
    "ds_read_b64_tr_b16 a[88:89], v23 offset:49152\n"
    "ds_read_b64_tr_b16 a[26:27], v22 offset:50176\n"
    "ds_read_b64_tr_b16 a[90:91], v23 offset:50176\n"
    "ds_read_b64_tr_b16 a[28:29], v22 offset:57344\n"
    "ds_read_b64_tr_b16 a[92:93], v23 offset:57344\n"
    "ds_read_b64_tr_b16 a[30:31], v22 offset:58368\n"
    "ds_read_b64_tr_b16 a[94:95], v23 offset:58368\n"
    "s_waitcnt vmcnt(25) lgkmcnt(0)\n"
    "s_barrier\n"
    "s_mov_b32 m0, 49152\n"
    "s_add_u32 s80, s60, 1572864\n"
    "buffer_load_dwordx4 v20, s[76:79], s80 offen offset:0  lds\n"
    "s_add_u32 s80, s60, 1604608\n"
    "buffer_load_dwordx4 v20, s[76:79], s80 offen offset:1024  lds\n"
    "s_add_u32 s80, s60, 1636352\n"
    "buffer_load_dwordx4 v21, s[76:79], s80 offen offset:2048  lds\n"
    "s_add_u32 s80, s60, 1668096\n"
    "buffer_load_dwordx4 v21, s[76:79], s80 offen offset:3072  lds\n"
    "s_getpc_b64 s[94:95]\n"
    "RCA12_0_%=:\n"
    "v_lshlrev_b32 v10, 7, v0\n"
    "v_min_u32 v10, .Lcode_end-RCA12_0_%=-4, v10\n"
    "global_load_dword v10, v10, s[94:95]\n"
    "v_add_u32 v22, 65536, v22\n"
    "v_add_u32 v23, 65536, v23\n"
    "v_add_u32 v26, 65536, v26\n"
    "v_add_u32 v27, 65536, v27\n"
    "v_add_u32 v28, 65536, v28\n"
    "v_add_u32 v29, 65536, v29\n"
    "ds_read_b64_tr_b16 a[32:33], v22 offset:0\n"
    "ds_read_b64_tr_b16 a[96:97], v23 offset:0\n"
    "ds_read_b64_tr_b16 a[34:35], v22 offset:1024\n"
    "ds_read_b64_tr_b16 a[98:99], v23 offset:1024\n"
    "ds_read_b64_tr_b16 a[36:37], v22 offset:8192\n"
    "ds_read_b64_tr_b16 a[100:101], v23 offset:8192\n"
    "ds_read_b64_tr_b16 a[38:39], v22 offset:9216\n"
    "ds_read_b64_tr_b16 a[102:103], v23 offset:9216\n"
    "s_waitcnt vmcnt(26) lgkmcnt(0)\n"
    "s_barrier\n"
    "s_mov_b32 m0, 65536\n"
    "s_add_u32 s80, s60, 2097152\n"
    "buffer_load_dwordx4 v20, s[76:79], s80 offen offset:0  lds\n"
    "s_add_u32 s80, s60, 2128896\n"
    "buffer_load_dwordx4 v20, s[76:79], s80 offen offset:1024  lds\n"
    "s_add_u32 s80, s60, 2160640\n"
    "buffer_load_dwordx4 v21, s[76:79], s80 offen offset:2048  lds\n"
    "s_add_u32 s80, s60, 2192384\n"
    "buffer_load_dwordx4 v21, s[76:79], s80 offen offset:3072  lds\n"
    "ds_read_b64_tr_b16 a[40:41], v22 offset:16384\n"
    "ds_read_b64_tr_b16 a[104:105], v23 offset:16384\n"
    "ds_read_b64_tr_b16 a[42:43], v22 offset:17408\n"
    "ds_read_b64_tr_b16 a[106:107], v23 offset:17408\n"
    "ds_read_b64_tr_b16 a[44:45], v22 offset:24576\n"
    "ds_read_b64_tr_b16 a[108:109], v23 offset:24576\n"
    "ds_read_b64_tr_b16 a[46:47], v22 offset:25600\n"
    "ds_read_b64_tr_b16 a[110:111], v23 offset:25600\n"
    "s_waitcnt vmcnt(26) lgkmcnt(0)\n"
    "s_barrier\n"
    "s_mov_b32 m0, 81920\n"
    "s_add_u32 s80, s60, 2621440\n"
    "buffer_load_dwordx4 v20, s[76:79], s80 offen offset:0  lds\n"
    "s_add_u32 s80, s60, 2653184\n"
    "buffer_load_dwordx4 v20, s[76:79], s80 offen offset:1024  lds\n"
    "s_add_u32 s80, s60, 2684928\n"
    "buffer_load_dwordx4 v21, s[76:79], s80 offen offset:2048  lds\n"
    "s_add_u32 s80, s60, 2716672\n"
    "buffer_load_dwordx4 v21, s[76:79], s80 offen offset:3072  lds\n"
    "ds_read_b64_tr_b16 a[48:49], v22 offset:32768\n"
    "ds_read_b64_tr_b16 a[112:113], v23 offset:32768\n"
    "ds_read_b64_tr_b16 a[50:51], v22 offset:33792\n"
    "ds_read_b64_tr_b16 a[114:115], v23 offset:33792\n"
    "ds_read_b64_tr_b16 a[52:53], v22 offset:40960\n"
    "ds_read_b64_tr_b16 a[116:117], v23 offset:40960\n"
    "ds_read_b64_tr_b16 a[54:55], v22 offset:41984\n"
    "ds_read_b64_tr_b16 a[118:119], v23 offset:41984\n"
    "s_waitcnt vmcnt(26) lgkmcnt(0)\n"
    "s_barrier\n"
    "s_mov_b32 m0, 98304\n"
    "s_add_u32 s80, s60, 3145728\n"
    "buffer_load_dwordx4 v20, s[76:79], s80 offen offset:0  lds\n"
    "s_add_u32 s80, s60, 3177472\n"
    "buffer_load_dwordx4 v20, s[76:79], s80 offen offset:1024  lds\n"
    "s_add_u32 s80, s60, 3209216\n"
    "buffer_load_dwordx4 v21, s[76:79], s80 offen offset:2048  lds\n"
    "s_add_u32 s80, s60, 3240960\n"
    "buffer_load_dwordx4 v21, s[76:79], s80 offen offset:3072  lds\n"
    "ds_read_b64_tr_b16 a[56:57], v22 offset:49152\n"
    "ds_read_b64_tr_b16 a[120:121], v23 offset:49152\n"
    "ds_read_b64_tr_b16 a[58:59], v22 offset:50176\n"
    "ds_read_b64_tr_b16 a[122:123], v23 offset:50176\n"
    "ds_read_b64_tr_b16 a[60:61], v22 offset:57344\n"
    "ds_read_b64_tr_b16 a[124:125], v23 offset:57344\n"
    "ds_read_b64_tr_b16 a[62:63], v22 offset:58368\n"
    "ds_read_b64_tr_b16 a[126:127], v23 offset:58368\n"
    "s_waitcnt vmcnt(25) lgkmcnt(0)\n"
    "s_barrier\n"
    "s_mov_b32 m0, 114688\n"
    "s_add_u32 s80, s60, 3670016\n"
    "buffer_load_dwordx4 v20, s[76:79], s80 offen offset:0  lds\n"
    "s_add_u32 s80, s60, 3701760\n"
    "buffer_load_dwordx4 v20, s[76:79], s80 offen offset:1024  lds\n"
    "s_add_u32 s80, s60, 3733504\n"
    "buffer_load_dwordx4 v21, s[76:79], s80 offen offset:2048  lds\n"
    "s_add_u32 s80, s60, 3765248\n"
    "buffer_load_dwordx4 v21, s[76:79], s80 offen offset:3072  lds\n"
    "v_readfirstlane_b32 s46, v67\n"
    "v_readfirstlane_b32 s68, v5\n"
    "s_cmp_lg_u32 s24, 0\n"
    "s_cselect_b32 s46, %2, s46\n"
    "s_cmp_lg_u32 s46, s68\n"
    "s_cselect_b32 s68, -1, 0\n"
    "s_lshl_b32 s46, s64, 7\n"
    "s_add_u32 s46, s46, 81793024\n"
    "s_cmp_lg_u32 s24, 0\n"
    "s_cselect_b32 s46, 0x7f000000, s46\n"
    "s_cmp_eq_u32 s68, 0\n"
    "s_cselect_b32 s46, 0x7f000000, s46\n"
    "v_mov_b32 v9, 1\n"
    "s_mov_b64 exec, 1\n"
    "buffer_store_dword v9, off, s[20:23], s46 sc1\n"
    "s_mov_b64 exec, -1\n"
    "buffer_load_dwordx2 v[4:5], off, s[20:23], s63 sc1\n"
    "v_lshrrev_b32 v6, 2, v0\n"
    "v_and_b32 v7, 3, v0\n"
    "v_lshl_add_u32 v6, v6, 3, v7\n"
    "v_lshlrev_b32 v6, 7, v6\n"
    "s_mov_b32 s46, 81793536\n"
    "buffer_load_dword v7, v6, s[20:23], s46 offen sc1\n"
    "s_mov_b32 s46, 81809920\n"
    "buffer_load_dword v8, v6, s[20:23], s46 offen sc1\n"
    "v_add_u32 v22, 4294901760, v22\n"
    "v_add_u32 v23, 4294901760, v23\n"
    "v_add_u32 v26, 4294901760, v26\n"
    "v_add_u32 v27, 4294901760, v27\n"
    "v_add_u32 v28, 4294901760, v28\n"
    "v_add_u32 v29, 4294901760, v29\n"
    "ds_read_b64_tr_b16 v[32:33], v22 offset:0\n"
    "ds_read_b64_tr_b16 v[40:41], v23 offset:0\n"
    "ds_read_b64_tr_b16 v[34:35], v22 offset:1024\n"
    "ds_read_b64_tr_b16 v[42:43], v23 offset:1024\n"
    "ds_read_b64_tr_b16 v[36:37], v22 offset:8192\n"
    "ds_read_b64_tr_b16 v[44:45], v23 offset:8192\n"
    "ds_read_b64_tr_b16 v[38:39], v22 offset:9216\n"
    "ds_read_b64_tr_b16 v[46:47], v23 offset:9216\n"
    "s_waitcnt vmcnt(29) lgkmcnt(0)\n"
    "s_barrier\n"
    "s_mov_b64 s[76:77], s[6:7]\n"
    "s_and_b32 s77, s77, 0xffff\n"
    "s_mov_b32 s78, 4194304\n"
    "s_mov_b32 s79, 0x20000\n"
    "s_mov_b32 m0, 0\n"
    "s_add_u32 s80, s60, 0\n"
    "buffer_load_dwordx4 v20, s[76:79], s80 offen offset:0  lds\n"
    "s_add_u32 s80, s60, 31744\n"
    "buffer_load_dwordx4 v20, s[76:79], s80 offen offset:1024  lds\n"
    "s_add_u32 s80, s60, 63488\n"
    "buffer_load_dwordx4 v21, s[76:79], s80 offen offset:2048  lds\n"
    "s_add_u32 s80, s60, 95232\n"
    "buffer_load_dwordx4 v21, s[76:79], s80 offen offset:3072  lds\n"
    "ds_read_b64_tr_b16 v[48:49], v22 offset:16384\n"
    "ds_read_b64_tr_b16 v[56:57], v23 offset:16384\n"
    "ds_read_b64_tr_b16 v[50:51], v22 offset:17408\n"
    "ds_read_b64_tr_b16 v[58:59], v23 offset:17408\n"
    "ds_read_b64_tr_b16 v[52:53], v22 offset:24576\n"
    "ds_read_b64_tr_b16 v[60:61], v23 offset:24576\n"
    "ds_read_b64_tr_b16 v[54:55], v22 offset:25600\n"
    "ds_read_b64_tr_b16 v[62:63], v23 offset:25600\n"
    "s_waitcnt vmcnt(29) lgkmcnt(0)\n"
    "s_barrier\n"
    "s_mov_b32 m0, 16384\n"
    "s_add_u32 s80, s60, 524288\n"
    "buffer_load_dwordx4 v20, s[76:79], s80 offen offset:0  lds\n"
    "s_add_u32 s80, s60, 556032\n"
    "buffer_load_dwordx4 v20, s[76:79], s80 offen offset:1024  lds\n"
    "s_add_u32 s80, s60, 587776\n"
    "buffer_load_dwordx4 v21, s[76:79], s80 offen offset:2048  lds\n"
    "s_add_u32 s80, s60, 619520\n"
    "buffer_load_dwordx4 v21, s[76:79], s80 offen offset:3072  lds\n"
    "ds_read_b64_tr_b16 a[160:161], v22 offset:32768\n"
    "ds_read_b64_tr_b16 a[208:209], v23 offset:32768\n"
    "ds_read_b64_tr_b16 a[162:163], v22 offset:33792\n"
    "ds_read_b64_tr_b16 a[210:211], v23 offset:33792\n"
    "ds_read_b64_tr_b16 a[164:165], v22 offset:40960\n"
    "ds_read_b64_tr_b16 a[212:213], v23 offset:40960\n"
    "ds_read_b64_tr_b16 a[166:167], v22 offset:41984\n"
    "ds_read_b64_tr_b16 a[214:215], v23 offset:41984\n"
    "s_waitcnt vmcnt(29) lgkmcnt(0)\n"
    "s_barrier\n"
    "s_mov_b32 m0, 32768\n"
    "s_add_u32 s80, s60, 1048576\n"
    "buffer_load_dwordx4 v20, s[76:79], s80 offen offset:0  lds\n"
    "s_add_u32 s80, s60, 1080320\n"
    "buffer_load_dwordx4 v20, s[76:79], s80 offen offset:1024  lds\n"
    "s_add_u32 s80, s60, 1112064\n"
    "buffer_load_dwordx4 v21, s[76:79], s80 offen offset:2048  lds\n"
    "s_add_u32 s80, s60, 1143808\n"
    "buffer_load_dwordx4 v21, s[76:79], s80 offen offset:3072  lds\n"
    "ds_read_b64_tr_b16 a[168:169], v22 offset:49152\n"
    "ds_read_b64_tr_b16 a[216:217], v23 offset:49152\n"
    "ds_read_b64_tr_b16 a[170:171], v22 offset:50176\n"
    "ds_read_b64_tr_b16 a[218:219], v23 offset:50176\n"
    "ds_read_b64_tr_b16 a[172:173], v22 offset:57344\n"
    "ds_read_b64_tr_b16 a[220:221], v23 offset:57344\n"
    "ds_read_b64_tr_b16 a[174:175], v22 offset:58368\n"
    "ds_read_b64_tr_b16 a[222:223], v23 offset:58368\n"
    "s_waitcnt vmcnt(28) lgkmcnt(0)\n"
    "s_barrier\n"
    "s_mov_b32 m0, 49152\n"
    "s_add_u32 s80, s60, 1572864\n"
    "buffer_load_dwordx4 v20, s[76:79], s80 offen offset:0  lds\n"
    "s_add_u32 s80, s60, 1604608\n"
    "buffer_load_dwordx4 v20, s[76:79], s80 offen offset:1024  lds\n"
    "s_add_u32 s80, s60, 1636352\n"
    "buffer_load_dwordx4 v21, s[76:79], s80 offen offset:2048  lds\n"
    "s_add_u32 s80, s60, 1668096\n"
    "buffer_load_dwordx4 v21, s[76:79], s80 offen offset:3072  lds\n"
    "v_add_u32 v22, 65536, v22\n"
    "v_add_u32 v23, 65536, v23\n"
    "v_add_u32 v26, 65536, v26\n"
    "v_add_u32 v27, 65536, v27\n"
    "v_add_u32 v28, 65536, v28\n"
    "v_add_u32 v29, 65536, v29\n"
    "ds_read_b64_tr_b16 a[176:177], v22 offset:0\n"
    "ds_read_b64_tr_b16 a[224:225], v23 offset:0\n"
    "ds_read_b64_tr_b16 a[178:179], v22 offset:1024\n"
    "ds_read_b64_tr_b16 a[226:227], v23 offset:1024\n"
    "ds_read_b64_tr_b16 a[180:181], v22 offset:8192\n"
    "ds_read_b64_tr_b16 a[228:229], v23 offset:8192\n"
    "ds_read_b64_tr_b16 a[182:183], v22 offset:9216\n"
    "ds_read_b64_tr_b16 a[230:231], v23 offset:9216\n"
    "s_waitcnt vmcnt(28) lgkmcnt(0)\n"
    "s_barrier\n"
    "s_mov_b32 m0, 65536\n"
    "s_add_u32 s80, s60, 2097152\n"
    "buffer_load_dwordx4 v20, s[76:79], s80 offen offset:0  lds\n"
    "s_add_u32 s80, s60, 2128896\n"
    "buffer_load_dwordx4 v20, s[76:79], s80 offen offset:1024  lds\n"
    "s_add_u32 s80, s60, 2160640\n"
    "buffer_load_dwordx4 v21, s[76:79], s80 offen offset:2048  lds\n"
    "s_add_u32 s80, s60, 2192384\n"
    "buffer_load_dwordx4 v21, s[76:79], s80 offen offset:3072  lds\n"
    "ds_read_b64_tr_b16 a[184:185], v22 offset:16384\n"
    "ds_read_b64_tr_b16 a[232:233], v23 offset:16384\n"
    "ds_read_b64_tr_b16 a[186:187], v22 offset:17408\n"
    "ds_read_b64_tr_b16 a[234:235], v23 offset:17408\n"
    "ds_read_b64_tr_b16 a[188:189], v22 offset:24576\n"
    "ds_read_b64_tr_b16 a[236:237], v23 offset:24576\n"
    "ds_read_b64_tr_b16 a[190:191], v22 offset:25600\n"
    "ds_read_b64_tr_b16 a[238:239], v23 offset:25600\n"
    "s_waitcnt vmcnt(28) lgkmcnt(0)\n"
    "s_barrier\n"
    "s_mov_b32 m0, 81920\n"
    "s_add_u32 s80, s60, 2621440\n"
    "buffer_load_dwordx4 v20, s[76:79], s80 offen offset:0  lds\n"
    "s_add_u32 s80, s60, 2653184\n"
    "buffer_load_dwordx4 v20, s[76:79], s80 offen offset:1024  lds\n"
    "s_add_u32 s80, s60, 2684928\n"
    "buffer_load_dwordx4 v21, s[76:79], s80 offen offset:2048  lds\n"
    "s_add_u32 s80, s60, 2716672\n"
    "buffer_load_dwordx4 v21, s[76:79], s80 offen offset:3072  lds\n"
    "ds_read_b64_tr_b16 a[192:193], v22 offset:32768\n"
    "ds_read_b64_tr_b16 a[240:241], v23 offset:32768\n"
    "ds_read_b64_tr_b16 a[194:195], v22 offset:33792\n"
    "ds_read_b64_tr_b16 a[242:243], v23 offset:33792\n"
    "ds_read_b64_tr_b16 a[196:197], v22 offset:40960\n"
    "ds_read_b64_tr_b16 a[244:245], v23 offset:40960\n"
    "ds_read_b64_tr_b16 a[198:199], v22 offset:41984\n"
    "ds_read_b64_tr_b16 a[246:247], v23 offset:41984\n"
    "s_waitcnt vmcnt(28) lgkmcnt(0)\n"
    "s_barrier\n"
    "s_mov_b32 m0, 98304\n"
    "s_add_u32 s80, s60, 3145728\n"
    "buffer_load_dwordx4 v20, s[76:79], s80 offen offset:0  lds\n"
    "s_add_u32 s80, s60, 3177472\n"
    "buffer_load_dwordx4 v20, s[76:79], s80 offen offset:1024  lds\n"
    "s_add_u32 s80, s60, 3209216\n"
    "buffer_load_dwordx4 v21, s[76:79], s80 offen offset:2048  lds\n"
    "s_add_u32 s80, s60, 3240960\n"
    "buffer_load_dwordx4 v21, s[76:79], s80 offen offset:3072  lds\n"
    "ds_read_b64_tr_b16 a[200:201], v22 offset:49152\n"
    "ds_read_b64_tr_b16 a[248:249], v23 offset:49152\n"
    "ds_read_b64_tr_b16 a[202:203], v22 offset:50176\n"
    "ds_read_b64_tr_b16 a[250:251], v23 offset:50176\n"
    "ds_read_b64_tr_b16 a[204:205], v22 offset:57344\n"
    "ds_read_b64_tr_b16 a[252:253], v23 offset:57344\n"
    "ds_read_b64_tr_b16 a[206:207], v22 offset:58368\n"
    "ds_read_b64_tr_b16 a[254:255], v23 offset:58368\n"
    "s_waitcnt vmcnt(24) lgkmcnt(0)\n"
    "s_barrier\n"
    "s_mov_b32 m0, 114688\n"
    "s_add_u32 s80, s60, 3670016\n"
    "buffer_load_dwordx4 v20, s[76:79], s80 offen offset:0  lds\n"
    "s_add_u32 s80, s60, 3701760\n"
    "buffer_load_dwordx4 v20, s[76:79], s80 offen offset:1024  lds\n"
    "s_add_u32 s80, s60, 3733504\n"
    "buffer_load_dwordx4 v21, s[76:79], s80 offen offset:2048  lds\n"
    "s_add_u32 s80, s60, 3765248\n"
    "buffer_load_dwordx4 v21, s[76:79], s80 offen offset:3072  lds\n"
    "v_readfirstlane_b32 s62, v4\n"
    "s_bitcmp1_b32 s62, 1\n"
    "s_cselect_b32 s68, s68, 0\n"
    "s_xor_b32 s62, s62, s24\n"
    "s_and_b32 s62, s62, 1\n"
    "s_lshl_b32 s62, s62, 13\n"
    "v_min_u32 v7, v7, v8\n"
    "v_cmp_gt_u32 vcc, 1, v7\n"
    "s_cmp_eq_u64 vcc, 0\n"
    "s_cselect_b32 s68, s68, 0\n"
    "s_add_u32 s30, s62, 0\n"
    "s_add_u32 s31, s30, 4096\n"
    "s_lshl_b32 s47, s25, 8\n"
    "s_add_u32 s36, s62, s47\n"
    "s_mov_b32 s50, 0\n"
    "s_mov_b32 s28, s30\n"
    "s_add_u32 s32, s62, s47\n"
    "s_add_u32 s32, s32, 67108864\n"
    "s_add_u32 s33, s32, 65536\n"
    "s_lshl_b32 s47, s25, 14\n"
    "s_add_u32 s34, s62, s47\n"
    "s_add_u32 s34, s34, 67108864\n"
    "s_add_u32 s35, s34, 4096\n"
    "s_add_u32 s44, s34, 4194304\n"
    "s_lshl_b32 s39, s25, 15\n"
    "s_add_u32 s39, s39, s62\n"
    "s_add_u32 s39, s39, 16777216\n"
    "s_xor_b32 s69, s62, 0x2000\n"
    "s_lshr_b32 s41, s25, 3\n"
    "s_lshl_b32 s41, s41, 14\n"
    "s_and_b32 s47, s25, 7\n"
    "s_lshl_b32 s47, s47, 10\n"
    "s_add_u32 s41, s41, s47\n"
    "s_add_u32 s41, s41, s69\n"
    "s_add_u32 s41, s41, 3145728\n"
    "s_add_u32 s69, s69, 3309568\n"
    "s_mov_b32 s37, 0x7f000000\n"
    "s_mov_b32 s42, 0\n"
    "s_cmp_eq_u32 s25, 0\n"
    "s_cselect_b32 s37, s69, s37\n"
    "s_add_u32 s69, s69, 16384\n"
    "s_cmp_eq_u32 s25, 11\n"
    "s_cselect_b32 s37, s69, s37\n"
    "s_add_u32 s69, s69, 16384\n"
    "s_cmp_eq_u32 s25, 22\n"
    "s_cselect_b32 s37, s69, s37\n"
    "s_add_u32 s69, s69, 16384\n"
    "v_add_u32 v22, 4294901760, v22\n"
    "v_add_u32 v23, 4294901760, v23\n"
    "v_add_u32 v26, 4294901760, v26\n"
    "v_add_u32 v27, 4294901760, v27\n"
    "v_add_u32 v28, 4294901760, v28\n"
    "v_add_u32 v29, 4294901760, v29\n"
    "ds_read_b64_tr_b16 v[70:71], v22 offset:0\n"
    "ds_read_b64_tr_b16 v[78:79], v23 offset:0\n"
    "ds_read_b64_tr_b16 v[72:73], v22 offset:1024\n"
    "ds_read_b64_tr_b16 v[80:81], v23 offset:1024\n"
    "ds_read_b64_tr_b16 v[74:75], v22 offset:8192\n"
    "ds_read_b64_tr_b16 v[82:83], v23 offset:8192\n"
    "ds_read_b64_tr_b16 v[76:77], v22 offset:9216\n"
    "ds_read_b64_tr_b16 v[84:85], v23 offset:9216\n"
    "s_waitcnt vmcnt(24) lgkmcnt(0)\n"
    "s_barrier\n"
    "s_getpc_b64 s[94:95]\n"
    "RCA25_0_%=:\n"
    "v_lshlrev_b32 v11, 7, v0\n"
    "v_min_u32 v11, .Lcode_end-RCA25_0_%=-4, v11\n"
    "global_load_dword v11, v11, s[94:95]\n"
    "ds_read_b64_tr_b16 v[86:87], v22 offset:16384\n"
    "ds_read_b64_tr_b16 v[94:95], v23 offset:16384\n"
    "ds_read_b64_tr_b16 v[88:89], v22 offset:17408\n"
    "ds_read_b64_tr_b16 v[96:97], v23 offset:17408\n"
    "ds_read_b64_tr_b16 v[90:91], v22 offset:24576\n"
    "ds_read_b64_tr_b16 v[98:99], v23 offset:24576\n"
    "ds_read_b64_tr_b16 v[92:93], v22 offset:25600\n"
    "ds_read_b64_tr_b16 v[100:101], v23 offset:25600\n"
    "ds_write_b128 v107, v[70:73] offset:0\n"
    "ds_write_b128 v107, v[74:77] offset:1024\n"
    "ds_write_b128 v107, v[78:81] offset:2048\n"
    "ds_write_b128 v107, v[82:85] offset:3072\n"
    "s_waitcnt vmcnt(21) lgkmcnt(0)\n"
    "s_barrier\n"
    "ds_read_b64_tr_b16 v[70:71], v22 offset:32768\n"
    "ds_read_b64_tr_b16 v[78:79], v23 offset:32768\n"
    "ds_read_b64_tr_b16 v[72:73], v22 offset:33792\n"
    "ds_read_b64_tr_b16 v[80:81], v23 offset:33792\n"
    "ds_read_b64_tr_b16 v[74:75], v22 offset:40960\n"
    "ds_read_b64_tr_b16 v[82:83], v23 offset:40960\n"
    "ds_read_b64_tr_b16 v[76:77], v22 offset:41984\n"
    "ds_read_b64_tr_b16 v[84:85], v23 offset:41984\n"
    "ds_write_b128 v107, v[86:89] offset:16384\n"
    "ds_write_b128 v107, v[90:93] offset:17408\n"
    "ds_write_b128 v107, v[94:97] offset:18432\n"
    "ds_write_b128 v107, v[98:101] offset:19456\n"
    "s_waitcnt vmcnt(17) lgkmcnt(0)\n"
    "s_barrier\n"
    "ds_read_b64_tr_b16 v[86:87], v22 offset:49152\n"
    "ds_read_b64_tr_b16 v[94:95], v23 offset:49152\n"
    "ds_read_b64_tr_b16 v[88:89], v22 offset:50176\n"
    "ds_read_b64_tr_b16 v[96:97], v23 offset:50176\n"
    "ds_read_b64_tr_b16 v[90:91], v22 offset:57344\n"
    "ds_read_b64_tr_b16 v[98:99], v23 offset:57344\n"
    "ds_read_b64_tr_b16 v[92:93], v22 offset:58368\n"
    "ds_read_b64_tr_b16 v[100:101], v23 offset:58368\n"
    "ds_write_b128 v107, v[70:73] offset:32768\n"
    "ds_write_b128 v107, v[74:77] offset:33792\n"
    "ds_write_b128 v107, v[78:81] offset:34816\n"
    "ds_write_b128 v107, v[82:85] offset:35840\n"
    "s_waitcnt vmcnt(13) lgkmcnt(0)\n"
    "s_barrier\n"
    "v_add_u32 v22, 65536, v22\n"
    "v_add_u32 v23, 65536, v23\n"
    "v_add_u32 v26, 65536, v26\n"
    "v_add_u32 v27, 65536, v27\n"
    "v_add_u32 v28, 65536, v28\n"
    "v_add_u32 v29, 65536, v29\n"
    "ds_read_b64_tr_b16 v[70:71], v22 offset:0\n"
    "ds_read_b64_tr_b16 v[78:79], v23 offset:0\n"
    "ds_read_b64_tr_b16 v[72:73], v22 offset:1024\n"
    "ds_read_b64_tr_b16 v[80:81], v23 offset:1024\n"
    "ds_read_b64_tr_b16 v[74:75], v22 offset:8192\n"
    "ds_read_b64_tr_b16 v[82:83], v23 offset:8192\n"
    "ds_read_b64_tr_b16 v[76:77], v22 offset:9216\n"
    "ds_read_b64_tr_b16 v[84:85], v23 offset:9216\n"
    "ds_write_b128 v107, v[86:89] offset:49152\n"
    "ds_write_b128 v107, v[90:93] offset:50176\n"
    "ds_write_b128 v107, v[94:97] offset:51200\n"
    "ds_write_b128 v107, v[98:101] offset:52224\n"
    "s_waitcnt vmcnt(9) lgkmcnt(0)\n"
    "s_barrier\n"
    "v_lshrrev_b32 v6, 2, v0\n"
    "v_and_b32 v7, 3, v0\n"
    "v_lshl_add_u32 v6, v6, 3, v7\n"
    "v_lshlrev_b32 v6, 7, v6\n"
    "s_mov_b32 s46, 81793536\n"
    "buffer_load_dword v7, v6, s[20:23], s46 offen sc1\n"
    "s_mov_b32 s46, 81809920\n"
    "buffer_load_dword v8, v6, s[20:23], s46 offen sc1\n"
    "buffer_load_dword v115, off, s[20:23], s41 sc1\n"
    "ds_read_b64_tr_b16 v[86:87], v22 offset:16384\n"
    "ds_read_b64_tr_b16 v[94:95], v23 offset:16384\n"
    "ds_read_b64_tr_b16 v[88:89], v22 offset:17408\n"
    "ds_read_b64_tr_b16 v[96:97], v23 offset:17408\n"
    "ds_read_b64_tr_b16 v[90:91], v22 offset:24576\n"
    "ds_read_b64_tr_b16 v[98:99], v23 offset:24576\n"
    "ds_read_b64_tr_b16 v[92:93], v22 offset:25600\n"
    "ds_read_b64_tr_b16 v[100:101], v23 offset:25600\n"
    "ds_write_b128 v108, v[70:73] offset:0\n"
    "ds_write_b128 v108, v[74:77] offset:1024\n"
    "ds_write_b128 v108, v[78:81] offset:2048\n"
    "ds_write_b128 v108, v[82:85] offset:3072\n"
    "s_waitcnt vmcnt(8) lgkmcnt(0)\n"
    "s_barrier\n"
    "ds_read_b64_tr_b16 a[128:129], v22 offset:32768\n"
    "ds_read_b64_tr_b16 a[144:145], v23 offset:32768\n"
    "ds_read_b64_tr_b16 a[130:131], v22 offset:33792\n"
    "ds_read_b64_tr_b16 a[146:147], v23 offset:33792\n"
    "ds_read_b64_tr_b16 a[132:133], v22 offset:40960\n"
    "ds_read_b64_tr_b16 a[148:149], v23 offset:40960\n"
    "ds_read_b64_tr_b16 a[134:135], v22 offset:41984\n"
    "ds_read_b64_tr_b16 a[150:151], v23 offset:41984\n"
    "ds_write_b128 v108, v[86:89] offset:16384\n"
    "ds_write_b128 v108, v[90:93] offset:17408\n"
    "ds_write_b128 v108, v[94:97] offset:18432\n"
    "ds_write_b128 v108, v[98:101] offset:19456\n"
    "s_waitcnt vmcnt(4) lgkmcnt(0)\n"
    "s_barrier\n"
    "ds_read_b64_tr_b16 a[136:137], v22 offset:49152\n"
    "ds_read_b64_tr_b16 a[152:153], v23 offset:49152\n"
    "ds_read_b64_tr_b16 a[138:139], v22 offset:50176\n"
    "ds_read_b64_tr_b16 a[154:155], v23 offset:50176\n"
    "ds_read_b64_tr_b16 a[140:141], v22 offset:57344\n"
    "ds_read_b64_tr_b16 a[156:157], v23 offset:57344\n"
    "ds_read_b64_tr_b16 a[142:143], v22 offset:58368\n"
    "ds_read_b64_tr_b16 a[158:159], v23 offset:58368\n"
    "ds_write_b128 v108, v[32:35] offset:32768\n"
    "ds_write_b128 v108, v[36:39] offset:33792\n"
    "ds_write_b128 v108, v[40:43] offset:34816\n"
    "ds_write_b128 v108, v[44:47] offset:35840\n"
    "s_waitcnt lgkmcnt(0)\n"
    "s_barrier\n"
    "ds_write_b128 v108, v[48:51] offset:49152\n"
    "ds_write_b128 v108, v[52:55] offset:50176\n"
    "ds_write_b128 v108, v[56:59] offset:51200\n"
    "ds_write_b128 v108, v[60:63] offset:52224\n"
    "ds_write_b128 v109, v[16:19] offset:8192\n"
    "s_waitcnt vmcnt(1)\n"
    "v_min_u32 v7, v7, v8\n"
    "v_cmp_gt_u32 vcc, 2, v7\n"
    "s_cmp_eq_u64 vcc, 0\n"
    "s_cselect_b32 s68, s68, 0\n"
    "s_waitcnt vmcnt(0)\n"
    "s_cmp_lg_u32 s68, 0\n"
    "s_cbranch_scc1 RF00_%=\n"
    "v_readfirstlane_b32 s46, v67\n"
    "s_cmp_lg_u32 s24, 0\n"
    "s_cselect_b32 s46, %2, s46\n"
    "s_mov_b32 s45, 0\n"
    "RBW0_%=:\n"
    "buffer_load_dwordx2 v[66:67], off, s[20:23], s63 sc1\n"
    "s_add_u32 s45, s45, 1\n"
    "s_cmp_gt_u32 s45, 262144\n"
    "s_cbranch_scc1 REND_%=\n"
    "s_waitcnt vmcnt(0)\n"
    "v_readfirstlane_b32 s62, v66\n"
    "v_cmp_eq_u32 vcc, s46, v67\n"
    "s_cbranch_vccnz RBW0_%=\n"
    "s_bitcmp1_b32 s62, 1\n"
    "s_cbranch_scc0 RBW0_%=\n"
    "s_xor_b32 s62, s62, s24\n"
    "s_and_b32 s62, s62, 1\n"
    "s_lshl_b32 s62, s62, 13\n"
    "s_lshl_b32 s46, s64, 7\n"
    "s_add_u32 s46, s46, 81793024\n"
    "s_cmp_lg_u32 s24, 0\n"
    "s_cselect_b32 s46, 0x7f000000, s46\n"
    "v_mov_b32 v66, 1\n"
    "s_mov_b64 exec, 1\n"
    "buffer_store_dword v66, off, s[20:23], s46 sc1\n"
    "s_mov_b64 exec, -1\n"
    "v_lshrrev_b32 v4, 2, v0\n"
    "v_and_b32 v5, 3, v0\n"
    "v_lshl_add_u32 v4, v4, 3, v5\n"
    "v_lshlrev_b32 v4, 7, v4\n"
    "s_mov_b32 s45, 0\n"
    "RIW0_%=:\n"
    "s_mov_b32 s46, 81793536\n"
    "buffer_load_dword v5, v4, s[20:23], s46 offen sc1\n"
    "s_mov_b32 s46, 81809920\n"
    "buffer_load_dword v6, v4, s[20:23], s46 offen sc1\n"
    "s_add_u32 s45, s45, 1\n"
    "s_cmp_gt_u32 s45, 262144\n"
    "s_cbranch_scc1 REND_%=\n"
    "s_waitcnt vmcnt(0)\n"
    "v_min_u32 v5, v5, v6\n"
    "v_cmp_gt_u32 vcc, 2, v5\n"
    "s_cbranch_vccnz RIW0_%=\n"
    "s_add_u32 s30, s62, 0\n"
    "s_add_u32 s31, s30, 4096\n"
    "s_lshl_b32 s47, s25, 8\n"
    "s_add_u32 s36, s62, s47\n"
    "s_mov_b32 s50, 0\n"
    "s_mov_b32 s28, s30\n"
    "s_add_u32 s32, s62, s47\n"
    "s_add_u32 s32, s32, 67108864\n"
    "s_add_u32 s33, s32, 65536\n"
    "s_lshl_b32 s47, s25, 14\n"
    "s_add_u32 s34, s62, s47\n"
    "s_add_u32 s34, s34, 67108864\n"
    "s_add_u32 s35, s34, 4096\n"
    "s_add_u32 s44, s34, 4194304\n"
    "s_lshl_b32 s39, s25, 15\n"
    "s_add_u32 s39, s39, s62\n"
    "s_add_u32 s39, s39, 16777216\n"
    "s_xor_b32 s69, s62, 0x2000\n"
    "s_lshr_b32 s41, s25, 3\n"
    "s_lshl_b32 s41, s41, 14\n"
    "s_and_b32 s47, s25, 7\n"
    "s_lshl_b32 s47, s47, 10\n"
    "s_add_u32 s41, s41, s47\n"
    "s_add_u32 s41, s41, s69\n"
    "s_add_u32 s41, s41, 3145728\n"
    "s_add_u32 s69, s69, 3309568\n"
    "s_mov_b32 s37, 0x7f000000\n"
    "s_mov_b32 s42, 0\n"
    "s_cmp_eq_u32 s25, 0\n"
    "s_cselect_b32 s37, s69, s37\n"
    "s_add_u32 s69, s69, 16384\n"
    "s_cmp_eq_u32 s25, 11\n"
    "s_cselect_b32 s37, s69, s37\n"
    "s_add_u32 s69, s69, 16384\n"
    "s_cmp_eq_u32 s25, 22\n"
    "s_cselect_b32 s37, s69, s37\n"
    "s_add_u32 s69, s69, 16384\n"
    "v_mov_b32 v115, 0\n"
    "RF00_%=:\n"
    "v_mov_b32 v116, 1\n"
    "s_mov_b64 exec, 1\n"
    "buffer_store_dword v116, off, s[20:23], s37 sc1\n"
    "s_mov_b64 exec, -1\n"
    "s_getpc_b64 s[82:83]\n"
    "RPC0_%=:\n"
    "s_add_u32 s80, s82, RF10_%=-RPC0_%=\n"
    "s_addc_u32 s83, s83, 0\n"
    "s_add_u32 s81, s80, RF20_%=-RF10_%=\n"
    "s_addc_u32 s79, s83, 0\n"
    "v_cmp_gt_u32 vcc, 1, v115\n"
    "s_cbranch_vccz RF30_%=\n"
    "s_mov_b32 s45, 0\n"
    "RPK0_%=:\n"
    "buffer_load_dword v115, off, s[20:23], s41 sc1\n"
    "s_add_u32 s45, s45, 1\n"
    "s_cmp_gt_u32 s45, 0x40000\n"
    "s_cbranch_scc1 REND_%=\n"
    "s_waitcnt vmcnt(0)\n"
    "v_cmp_gt_u32 vcc, 1, v115\n"
    "s_cbranch_vccnz RPK0_%=\n"
    "RF30_%=:\n"
    "buffer_load_dwordx4 v[90:93], v107, s[20:23], s39 offen offset:0 sc1\n"
    "buffer_load_dwordx4 v[94:97], v107, s[20:23], s39 offen offset:1024 sc1\n"
    "buffer_load_dwordx4 v[98:101], v107, s[20:23], s39 offen offset:2048 sc1\n"
    "buffer_load_dwordx4 v[102:105], v107, s[20:23], s39 offen offset:3072 sc1\n"
    "s_mov_b32 s45, 0\n"
    "s_memrealtime s[86:87]\n"
    "s_waitcnt lgkmcnt(0)\n"
    "s_mov_b32 s84, s86\n"
    "s_mov_b32 s63, s86\n"
    "s_cmp_eq_u32 s24, 0\n"
    "s_cbranch_scc1 RF40_%=\n"
    "s_xor_b32 s46, s69, 0x2000\n"
    "s_sub_u32 s46, s46, 48768\n"
    "buffer_load_dword v116, off, s[20:23], s46 sc1\n"
    "s_lshr_b32 s47, s16, 16\n"
    "s_sub_u32 s47, s47, 232\n"
    "s_waitcnt vmcnt(0)\n"
    "v_readfirstlane_b32 s46, v116\n"
    "s_cmp_eq_u32 s46, 0\n"
    "s_cbranch_scc1 RF40_%=\n"
    "s_add_u32 s46, s46, s47\n"
    "s_sub_u32 s47, s46, s86\n"
    "s_cmp_gt_i32 s47, 0\n"
    "s_cselect_b32 s86, s46, s86\n"
    "RF40_%=:\n"
    "v_or_b32 v116, s86, 1\n"
    "s_lshl_b32 s46, s25, 7\n"
    "s_add_u32 s46, s46, s69\n"
    "s_mov_b64 exec, 1\n"
    "buffer_store_dword v116, off, s[20:23], s46 sc1\n"
    "s_mov_b64 exec, -1\n"
    "s_mov_b32 s68, s34\n"
    "s_mov_b32 s34, 0x7f000000\n"
    "s_mov_b32 s35, s34\n"
    "s_lshr_b32 s66, s17, 16\n"
    "s_mov_b32 s38, s39\n"
    "s_mov_b32 s85, s84\n"
    "ds_read_b128 v[32:35], v107 offset:0\n"
    "ds_read_b128 v[36:39], v107 offset:1024\n"
    "ds_read_b128 v[40:43], v107 offset:2048\n"
    "ds_read_b128 v[44:47], v107 offset:3072\n"
    "ds_read_b128 v[48:51], v107 offset:16384\n"
    "ds_read_b128 v[52:55], v107 offset:17408\n"
    "ds_read_b128 v[56:59], v107 offset:18432\n"
    "ds_read_b128 v[60:63], v107 offset:19456\n"
    "v_mov_b64 v[66:67], 0\n"
    "v_mov_b64 v[68:69], 0\n"
    "v_mov_b64 v[70:71], 0\n"
    "v_mov_b64 v[72:73], 0\n"
    "v_mov_b64 v[74:75], 0\n"
    "v_mov_b64 v[76:77], 0\n"
    "v_mov_b64 v[78:79], 0\n"
    "v_mov_b64 v[80:81], 0\n"
    "v_mov_b64 v[82:83], 0\n"
    "v_mov_b64 v[84:85], 0\n"
    "v_mov_b64 v[86:87], 0\n"
    "v_mov_b64 v[88:89], 0\n"
    "s_waitcnt lgkmcnt(0)\n"
    ".p2align 6\n"
    "RLP0_%=:\n"
    "buffer_load_dwordx4 v[0:3], v107, s[20:23], s30 offen offset:0 sc1\n"
    "s_memrealtime s[90:91]\n"
    "s_add_u32 s42, s42, 1\n"
    "s_add_u32 s43, s42, 1\n"
    "s_and_b32 s46, s42, 1\n"
    "s_lshl_b32 s46, s46, 9\n"
    "s_add_u32 s40, s41, s46\n"
    "s_and_b32 s46, s42, 7\n"
    "s_lshl_b32 s46, s46, 20\n"
    "s_add_u32 s38, s39, s46\n"
    "s_nop 4\n"
    "buffer_load_dwordx4 v[4:7], v107, s[20:23], s30 offen offset:1024 sc1\n"
    "s_lshr_b32 s46, s38, 8\n"
    "s_and_b32 s46, s46, 0x2000\n"
    "s_xor_b32 s38, s38, s46\n"
    "s_cmp_eq_u32 s42, 63\n"
    "s_cselect_b32 s43, 0, s43\n"
    "s_nop 8\n"
    "buffer_load_dwordx4 v[8:11], v107, s[20:23], s30 offen offset:2048 sc1\n"
    "s_nop 13\n"
    "buffer_load_dwordx4 v[12:15], v107, s[20:23], s30 offen offset:3072 sc1\n"
    "s_nop 13\n"
    "buffer_load_dwordx4 v[16:19], v107, s[20:23], s31 offen offset:0 sc1\n"
    "s_waitcnt lgkmcnt(0)\n"
    "s_nop 12\n"
    "buffer_load_dwordx4 v[20:23], v107, s[20:23], s31 offen offset:1024 sc1\n"
    "s_nop 13\n"
    "buffer_load_dwordx4 v[24:27], v107, s[20:23], s31 offen offset:2048 sc1\n"
    "s_nop 13\n"
    "buffer_load_dwordx4 v[28:31], v107, s[20:23], s31 offen offset:3072 sc1\n"
    "RRB0_%=:\n"
    "s_waitcnt vmcnt(7)\n"
    "v_smfmac_f32_16x16x64_bf16 v[66:69], v[0:3], a[0:7], v106\n"
    "v_smfmac_f32_16x16x64_bf16 v[70:73], v[0:3], v[32:39], v106\n"
    "ds_read_b128 v[32:35], v107 offset:32768\n"
    "v_smfmac_f32_16x16x64_bf16 v[74:77], v[0:3], a[64:71], v106\n"
    "ds_read_b128 v[36:39], v107 offset:33792\n"
    "v_smfmac_f32_16x16x64_bf16 v[78:81], v[0:3], v[40:47], v106\n"
    "ds_read_b128 v[40:43], v107 offset:34816\n"
    "s_waitcnt vmcnt(6)\n"
    "v_smfmac_f32_16x16x64_bf16 v[66:69], v[4:7], a[8:15], v106\n"
    "ds_read_b128 v[44:47], v107 offset:35840\n"
    "v_smfmac_f32_16x16x64_bf16 v[70:73], v[4:7], v[48:55], v106\n"
    "ds_read_b128 v[48:51], v107 offset:49152\n"
    "v_max_u32 v116, v0, v2\n"
    "v_smfmac_f32_16x16x64_bf16 v[74:77], v[4:7], a[72:79], v106\n"
    "ds_read_b128 v[52:55], v107 offset:50176\n"
    "v_smfmac_f32_16x16x64_bf16 v[78:81], v[4:7], v[56:63], v106\n"
    "ds_read_b128 v[56:59], v107 offset:51200\n"
    "s_waitcnt vmcnt(5)\n"
    "v_smfmac_f32_16x16x64_bf16 v[66:69], v[8:11], a[16:23], v106\n"
    "ds_read_b128 v[60:63], v107 offset:52224\n"
    "s_waitcnt lgkmcnt(6)\n"
    "v_smfmac_f32_16x16x64_bf16 v[70:73], v[8:11], v[32:39], v106\n"
    "ds_read_b128 v[32:35], v108 offset:0\n"
    "v_max3_u32 v116, v116, v4, v6\n"
    "v_smfmac_f32_16x16x64_bf16 v[74:77], v[8:11], a[80:87], v106\n"
    "ds_read_b128 v[36:39], v108 offset:1024\n"
    "s_waitcnt lgkmcnt(6)\n"
    "v_smfmac_f32_16x16x64_bf16 v[78:81], v[8:11], v[40:47], v106\n"
    "ds_read_b128 v[40:43], v108 offset:2048\n"
    "s_waitcnt vmcnt(4)\n"
    "v_smfmac_f32_16x16x64_bf16 v[66:69], v[12:15], a[24:31], v106\n"
    "ds_read_b128 v[44:47], v108 offset:3072\n"
    "s_waitcnt lgkmcnt(6)\n"
    "v_smfmac_f32_16x16x64_bf16 v[70:73], v[12:15], v[48:55], v106\n"
    "ds_read_b128 v[48:51], v108 offset:16384\n"
    "v_max3_u32 v116, v116, v8, v10\n"
    "v_smfmac_f32_16x16x64_bf16 v[74:77], v[12:15], a[88:95], v106\n"
    "ds_read_b128 v[52:55], v108 offset:17408\n"
    "s_waitcnt lgkmcnt(6)\n"
    "v_smfmac_f32_16x16x64_bf16 v[78:81], v[12:15], v[56:63], v106\n"
    "ds_read_b128 v[56:59], v108 offset:18432\n"
    "s_waitcnt vmcnt(3)\n"
    "v_smfmac_f32_16x16x64_bf16 v[66:69], v[16:19], a[32:39], v106\n"
    "ds_read_b128 v[60:63], v108 offset:19456\n"
    "s_waitcnt lgkmcnt(6)\n"
    "v_smfmac_f32_16x16x64_bf16 v[70:73], v[16:19], v[32:39], v106\n"
    "ds_read_b128 v[32:35], v108 offset:32768\n"
    "v_max3_u32 v116, v116, v12, v14\n"
    "v_smfmac_f32_16x16x64_bf16 v[74:77], v[16:19], a[96:103], v106\n"
    "ds_read_b128 v[36:39], v108 offset:33792\n"
    "s_waitcnt lgkmcnt(6)\n"
    "v_smfmac_f32_16x16x64_bf16 v[78:81], v[16:19], v[40:47], v106\n"
    "ds_read_b128 v[40:43], v108 offset:34816\n"
    "s_waitcnt vmcnt(2)\n"
    "v_smfmac_f32_16x16x64_bf16 v[66:69], v[20:23], a[40:47], v106\n"
    "ds_read_b128 v[44:47], v108 offset:35840\n"
    "s_waitcnt lgkmcnt(6)\n"
    "v_smfmac_f32_16x16x64_bf16 v[70:73], v[20:23], v[48:55], v106\n"
    "ds_read_b128 v[48:51], v108 offset:49152\n"
    "v_max3_u32 v116, v116, v16, v18\n"
    "v_smfmac_f32_16x16x64_bf16 v[74:77], v[20:23], a[104:111], v106\n"
    "ds_read_b128 v[52:55], v108 offset:50176\n"
    "s_waitcnt lgkmcnt(6)\n"
    "v_smfmac_f32_16x16x64_bf16 v[78:81], v[20:23], v[56:63], v106\n"
    "ds_read_b128 v[56:59], v108 offset:51200\n"
    "s_waitcnt vmcnt(1)\n"
    "v_smfmac_f32_16x16x64_bf16 v[66:69], v[24:27], a[48:55], v106\n"
    "ds_read_b128 v[60:63], v108 offset:52224\n"
    "v_smfmac_f32_16x16x64_bf16 v[70:73], v[24:27], a[128:135], v106\n"
    "v_max3_u32 v116, v116, v20, v22\n"
    "v_smfmac_f32_16x16x64_bf16 v[74:77], v[24:27], a[112:119], v106\n"
    "v_max3_u32 v116, v116, v24, v26\n"
    "v_smfmac_f32_16x16x64_bf16 v[78:81], v[24:27], a[144:151], v106\n"
    "s_waitcnt vmcnt(0)\n"
    "v_smfmac_f32_16x16x64_bf16 v[66:69], v[28:31], a[56:63], v106\n"
    "v_max3_u32 v116, v116, v28, v30\n"
    "v_cmp_eq_u32 vcc, -1, v116\n"
    "v_smfmac_f32_16x16x64_bf16 v[70:73], v[28:31], a[136:143], v106\n"
    "s_cbranch_vccnz RRT0_%=\n"
    "v_smfmac_f32_16x16x64_bf16 v[74:77], v[28:31], a[120:127], v106\n"
    "s_nop 1\n"
    "s_nop 0\n"
    "v_cvt_pk_bf16_f32 v69, v68, v69\n"
    "v_smfmac_f32_16x16x64_bf16 v[78:81], v[28:31], a[152:159], v106\n"
    "s_nop 0\n"
    "v_cvt_pk_bf16_f32 v68, v66, v67\n"
    "v_cvt_pk_bf16_f32 v70, v70, v71\n"
    "v_cvt_pk_bf16_f32 v71, v72, v73\n"
    "v_cvt_pk_bf16_f32 v77, v76, v77\n"
    "v_cvt_pk_bf16_f32 v76, v74, v75\n"
    "v_mfma_f32_16x16x32_bf16 v[68:71], v[68:71], v[120:123], 0\n"
    "s_nop 0\n"
    "v_cvt_pk_bf16_f32 v78, v78, v79\n"
    "v_cvt_pk_bf16_f32 v79, v80, v81\n"
    "s_nop 1\n"
    "v_mfma_f32_16x16x32_bf16 v[76:79], v[76:79], v[120:123], 0\n"
    "s_waitcnt lgkmcnt(6)\n"
    "v_smfmac_f32_16x16x64_bf16 v[82:85], v[0:3], v[32:39], v106\n"
    "v_cvt_pk_bf16_f32 v72, v68, v69\n"
    "v_cvt_pk_bf16_f32 v73, v70, v71\n"
    "s_waitcnt lgkmcnt(4)\n"
    "v_smfmac_f32_16x16x64_bf16 v[86:89], v[0:3], v[40:47], v106\n"
    "s_nop 1\n"
    "v_cvt_pk_bf16_f32 v74, v76, v77\n"
    "v_cvt_pk_bf16_f32 v75, v78, v79\n"
    "s_nop 1\n"
    "v_mfma_f32_16x16x32_bf16 v[66:69], v[90:93], v[72:75], 0\n"
    "v_mfma_f32_16x16x32_bf16 v[78:81], v[94:97], v[72:75], 0\n"
    "s_nop 0\n"
    "v_mfma_f32_16x16x32_bf16 v[72:75], v[72:75], v[72:75], 0\n"
    "s_nop 1\n"
    "s_waitcnt lgkmcnt(2)\n"
    "v_smfmac_f32_16x16x64_bf16 v[82:85], v[4:7], v[48:55], v106\n"
    "s_nop 1\n"
    "v_mov_b32_dpp v66, v78 row_ror:8 row_mask:0xf bank_mask:0xc\n"
    "v_mov_b32_dpp v67, v79 row_ror:8 row_mask:0xf bank_mask:0xc\n"
    "v_mov_b32_dpp v68, v80 row_ror:8 row_mask:0xf bank_mask:0xc\n"
    "v_mov_b32_dpp v69, v81 row_ror:8 row_mask:0xf bank_mask:0xc\n"
    "ds_write_b128 v109, v[66:69] offset:0\n"
    "v_mov_b32_dpp v72, v74 row_ror:8 row_mask:0xf bank_mask:0xc\n"
    "v_mov_b32_dpp v73, v75 row_ror:8 row_mask:0xf bank_mask:0xc\n"
    "ds_write_b64 v117, v[72:73] offset:0\n"
    "s_waitcnt lgkmcnt(2)\n"
    "v_smfmac_f32_16x16x64_bf16 v[86:89], v[4:7], v[56:63], v106\n"
    "v_smfmac_f32_16x16x64_bf16 v[82:85], v[8:11], a[160:167], v106\n"
    "v_smfmac_f32_16x16x64_bf16 v[86:89], v[8:11], a[208:215], v106\n"
    "v_smfmac_f32_16x16x64_bf16 v[82:85], v[12:15], a[168:175], v106\n"
    "s_waitcnt lgkmcnt(0)\n"
    "s_barrier\n"
    "ds_read_b128 v[32:35], v109 offset:1024\n"
    "v_smfmac_f32_16x16x64_bf16 v[86:89], v[12:15], a[216:223], v106\n"
    "ds_read_b128 v[36:39], v109 offset:2048\n"
    "v_smfmac_f32_16x16x64_bf16 v[82:85], v[16:19], a[176:183], v106\n"
    "ds_read_b128 v[40:43], v109 offset:3072\n"
    "v_smfmac_f32_16x16x64_bf16 v[86:89], v[16:19], a[224:231], v106\n"
    "ds_read_b128 v[44:47], v110 offset:0\n"
    "v_smfmac_f32_16x16x64_bf16 v[82:85], v[20:23], a[184:191], v106\n"
    "ds_read_b128 v[48:51], v110 offset:1024\n"
    "v_smfmac_f32_16x16x64_bf16 v[86:89], v[20:23], a[232:239], v106\n"
    "s_waitcnt lgkmcnt(2)\n"
    "v_pk_add_f32 v[66:67], v[66:67], v[32:33]\n"
    "v_pk_add_f32 v[68:69], v[68:69], v[34:35]\n"
    "v_pk_add_f32 v[66:67], v[66:67], v[36:37]\n"
    "v_pk_add_f32 v[68:69], v[68:69], v[38:39]\n"
    "v_pk_add_f32 v[66:67], v[66:67], v[40:41]\n"
    "v_pk_add_f32 v[68:69], v[68:69], v[42:43]\n"
    "s_waitcnt lgkmcnt(0)\n"
    "v_pk_add_f32 v[70:71], v[44:45], v[46:47]\n"
    "v_pk_add_f32 v[70:71], v[70:71], v[48:49]\n"
    "v_pk_add_f32 v[70:71], v[70:71], v[50:51]\n"
    "v_max3_f32 v72, v66, v67, v68\n"
    "v_max3_f32 v72, v72, v69, v70\n"
    "v_max_f32 v72, v72, v71\n"
    "v_smfmac_f32_16x16x64_bf16 v[82:85], v[24:27], a[192:199], v106\n"
    "s_nop 0\n"
    "v_max_f32_dpp v73, v72, v72 row_ror:8 row_mask:0xf bank_mask:0xf\n"
    "v_max_f32_dpp v72, v72, v72 row_ror:8 row_mask:0xf bank_mask:0xf\n"
    "v_smfmac_f32_16x16x64_bf16 v[86:89], v[24:27], a[240:247], v106\n"
    "s_nop 0\n"
    "v_permlane16_swap_b32 v72, v73\n"
    "v_max_f32 v74, v72, v73\n"
    "v_max_f32 v72, v72, v73\n"
    "v_smfmac_f32_16x16x64_bf16 v[82:85], v[28:31], a[200:207], v106\n"
    "s_nop 0\n"
    "v_permlane32_swap_b32 v72, v74\n"
    "v_max_f32 v72, v72, v74\n"
    "v_mul_f32 v72, s48, v72\n"
    "v_fma_f32 v66, v66, s48, -v72\n"
    "v_fma_f32 v67, v67, s48, -v72\n"
    "v_fma_f32 v68, v68, s48, -v72\n"
    "v_fma_f32 v69, v69, s48, -v72\n"
    "v_exp_f32 v66, v66\n"
    "v_exp_f32 v67, v67\n"
    "v_exp_f32 v68, v68\n"
    "v_cvt_pk_bf16_f32 v62, v66, v67\n"
    "v_exp_f32 v69, v69\n"
    "v_smfmac_f32_16x16x64_bf16 v[86:89], v[28:31], a[248:255], v106\n"
    "v_mov_b32_dpp v60, v62 row_ror:8 row_mask:0xf bank_mask:0xf\n"
    "v_cvt_pk_bf16_f32 v63, v68, v69\n"
    "v_fma_f32 v70, v70, s48, -v72\n"
    "v_fma_f32 v71, v71, s48, -v72\n"
    "v_mov_b32_dpp v61, v63 row_ror:8 row_mask:0xf bank_mask:0xf\n"
    "v_exp_f32 v70, v70\n"
    "v_exp_f32 v71, v71\n"
    "v_mfma_f32_16x16x32_bf16 v[36:39], v[98:101], v[60:63], 0\n"
    "v_cvt_pk_bf16_f32 v64, v70, v71\n"
    "v_cvt_pk_bf16_f32 v82, v82, v83\n"
    "v_mfma_f32_16x16x32_bf16 v[40:43], v[102:105], v[60:63], 0\n"
    "v_cvt_pk_bf16_f32 v83, v84, v85\n"
    "v_cvt_pk_bf16_f32 v84, v88, v89\n"
    "v_mfma_f32_16x16x32_bf16 v[32:35], v[124:127], v[62:65], 0\n"
    "v_mov_b32_dpp v65, v64 row_ror:8 row_mask:0xf bank_mask:0xf\n"
    "v_cvt_pk_bf16_f32 v85, v86, v87\n"
    "s_nop 0\n"
    "v_mfma_f32_16x16x16_bf16 v[36:39], v[82:83], v[64:65], v[36:39]\n"
    "s_nop 0\n"
    "s_nop 1\n"
    "v_mfma_f32_16x16x16_bf16 v[40:43], v[84:85], v[64:65], v[40:43]\n"
    "s_nop 0\n"
    "v_add_f32_dpp v32, v32, v32 row_ror:8 row_mask:0xf bank_mask:0xf\n"
    "v_rcp_f32 v32, v32\n"
    "s_nop 0\n"
    "v_pk_mul_f32 v[36:37], v[36:37], v[32:33] op_sel_hi:[1,0]\n"
    "v_pk_mul_f32 v[38:39], v[38:39], v[32:33] op_sel_hi:[1,0]\n"
    "s_mov_b64 exec, s[98:99]\n"
    "s_nop 0\n"
    "v_pk_mul_f32 v[36:37], v[40:41], v[32:33] op_sel_hi:[1,0]\n"
    "v_pk_mul_f32 v[38:39], v[42:43], v[32:33] op_sel_hi:[1,0]\n"
    "s_mov_b64 exec, -1\n"
    "v_cvt_pk_bf16_f32 v0, v36, v37\n"
    "v_cvt_pk_bf16_f32 v1, v38, v39\n"
    "ds_write_b64 v111, v[0:1] offset:0\n"
    "v_mov_b32_dpp v2, v0 row_ror:8 row_mask:0xf bank_mask:0xf\n"
    "v_mov_b32_dpp v3, v1 row_ror:8 row_mask:0xf bank_mask:0xf\n"
    "v_mov_b64 v[16:17], 0\n"
    "v_mov_b64 v[18:19], 0\n"
    "v_mov_b64 v[20:21], 0\n"
    "v_mov_b64 v[22:23], 0\n"
    "v_mov_b64 v[24:25], 0\n"
    "v_mov_b64 v[26:27], 0\n"
    "v_mov_b64 v[28:29], 0\n"
    "v_mov_b64 v[30:31], 0\n"
    "v_smfmac_f32_16x16x64_bf16 v[16:19], v[0:3], v[128:135], v106\n"
    "s_waitcnt lgkmcnt(0)\n"
    "s_barrier\n"
    "ds_read_b128 v[4:7], v112 offset:512\n"
    "ds_read_b128 v[8:11], v112 offset:1024\n"
    "ds_read_b128 v[12:15], v112 offset:1536\n"
    "ds_read_b32 v118, v109 offset:9228\n"
    "v_smfmac_f32_16x16x64_bf16 v[20:23], v[0:3], v[160:167], v106\n"
    "v_smfmac_f32_16x16x64_bf16 v[24:27], v[0:3], v[192:199], v106\n"
    "v_smfmac_f32_16x16x64_bf16 v[28:31], v[0:3], v[224:231], v106\n"
    "s_waitcnt lgkmcnt(3)\n"
    "v_smfmac_f32_16x16x64_bf16 v[16:19], v[4:7], v[136:143], v106\n"
    "v_smfmac_f32_16x16x64_bf16 v[20:23], v[4:7], v[168:175], v106\n"
    "s_waitcnt lgkmcnt(2)\n"
    "v_smfmac_f32_16x16x64_bf16 v[16:19], v[8:11], v[144:151], v106\n"
    "v_smfmac_f32_16x16x64_bf16 v[20:23], v[8:11], v[176:183], v106\n"
    "s_waitcnt lgkmcnt(1)\n"
    "v_smfmac_f32_16x16x64_bf16 v[16:19], v[12:15], v[152:159], v106\n"
    "s_nop 0\n"
    "v_smfmac_f32_16x16x64_bf16 v[20:23], v[12:15], v[184:191], v106\n"
    "s_nop 2\n"
    "v_smfmac_f32_16x16x64_bf16 v[24:27], v[4:7], v[200:207], v106\n"
    "s_nop 1\n"
    "v_cvt_pk_bf16_f32 v19, v18, v19\n"
    "v_smfmac_f32_16x16x64_bf16 v[24:27], v[8:11], v[208:215], v106\n"
    "v_cvt_pk_bf16_f32 v18, v16, v17\n"
    "v_smfmac_f32_16x16x64_bf16 v[24:27], v[12:15], v[216:223], v106\n"
    "v_or_b32 v18, 1, v18\n"
    "v_smfmac_f32_16x16x64_bf16 v[28:31], v[4:7], v[232:239], v106\n"
    "v_cvt_pk_bf16_f32 v20, v20, v21\n"
    "v_smfmac_f32_16x16x64_bf16 v[28:31], v[8:11], v[240:247], v106\n"
    "v_cvt_pk_bf16_f32 v21, v22, v23\n"
    "v_or_b32 v20, 1, v20\n"
    "buffer_store_dwordx4 v[18:21], v113, s[20:23], s32 offen sc1\n"
    "v_smfmac_f32_16x16x64_bf16 v[28:31], v[12:15], v[248:255], v106\n"
    "s_nop 1\n"
    "v_cvt_pk_bf16_f32 v27, v26, v27\n"
    "v_cvt_pk_bf16_f32 v26, v24, v25\n"
    "v_or_b32 v26, 1, v26\n"
    "s_nop 2\n"
    "v_cvt_pk_bf16_f32 v28, v28, v29\n"
    "v_cvt_pk_bf16_f32 v29, v30, v31\n"
    "v_or_b32 v28, 1, v28\n"
    "buffer_store_dwordx4 v[26:29], v113, s[20:23], s33 offen sc1\n"
    "s_memtime s[56:57]\n"
    "s_memrealtime s[86:87]\n"
    "s_mov_b32 s45, 0\n"
    "RT00_%=:\n"
    "s_add_u32 s28, s28, 16384\n"
    "RKS0_%=:\n"
    "s_sub_u32 s46, s90, s85\n"
    "s_add_u32 s46, s46, s50\n"
    "s_cmp_gt_u32 s46, 20\n"
    "s_cselect_b32 s65, 0x10000, 0\n"
    "s_sub_u32 s65, s65, s84\n"
    "s_add_u32 s29, s84, s26\n"
    "s_waitcnt lgkmcnt(0)\n"
    "v_readfirstlane_b32 s26, v118\n"
    "s_add_u32 s84, s29, s50\n"
    "s_and_b32 s27, s17, 0xffff\n"
    "s_sub_u32 s44, s26, 189\n"
    "s_max_i32 s44, s44, 0\n"
    "s_lshr_b32 s44, s44, 2\n"
    "s_add_u32 s27, s27, s44\n"
    "s_add_u32 s86, s86, 6\n"
    "s_mov_b32 s50, 0\n"
    "s_cmp_eq_u32 s42, 1\n"
    "s_cbranch_scc1 RI10_%=\n"
    "s_sub_u32 s84, s84, 56\n"
    "RIB0_%=:\n"
    "s_add_u32 s84, s84, 56\n"
    "s_sub_u32 s46, s84, s86\n"
    "s_cmp_lt_i32 s46, 0\n"
    "s_cbranch_scc1 RIB0_%=\n"
    "s_andn2_b32 s47, 3, s84\n"
    "s_andn2_b32 s85, s84, 3\n"
    "s_lshl2_add_u32 s82, s47, s80\n"
    "s_memrealtime s[86:87]\n"
    "s_nop 3\n"
    "s_memrealtime s[92:93]\n"
    "s_nop 3\n"
    "RD10_%=:\n"
    "s_memrealtime s[94:95]\n"
    "s_waitcnt lgkmcnt(2)\n"
    "s_sub_u32 s46, s86, s85\n"
    "s_cmp_lt_i32 s46, 0\n"
    "s_cbranch_scc0 RD10_%=x\n"
    "s_memrealtime s[86:87]\n"
    "s_waitcnt lgkmcnt(2)\n"
    "s_sub_u32 s46, s92, s85\n"
    "s_cmp_lt_i32 s46, 0\n"
    "s_cbranch_scc0 RD10_%=x\n"
    "s_memrealtime s[92:93]\n"
    "s_waitcnt lgkmcnt(2)\n"
    "s_sub_u32 s46, s94, s85\n"
    "s_cmp_lt_i32 s46, 0\n"
    "s_cbranch_scc1 RD10_%=\n"
    "RD10_%=x:\n"
    "s_setpc_b64 s[82:83]\n"
    ".p2align 4\n"
    "RF10_%=:\n"
    "s_nop 4\n"
    "s_nop 4\n"
    "s_nop 4\n"
    "RI00_%=:\n"
    "s_memtime s[58:59]\n"
    "buffer_load_dwordx4 v[0:3], v107, s[20:23], s34 offen offset:0 sc1\n"
    "buffer_load_dwordx4 v[4:7], v107, s[20:23], s34 offen offset:1024 sc1\n"
    "buffer_load_dwordx4 v[8:11], v107, s[20:23], s34 offen offset:2048 sc1\n"
    "buffer_load_dwordx4 v[12:15], v107, s[20:23], s34 offen offset:3072 sc1\n"
    "buffer_load_dwordx4 v[16:19], v107, s[20:23], s35 offen offset:0 sc1\n"
    "buffer_load_dwordx4 v[20:23], v107, s[20:23], s35 offen offset:1024 sc1\n"
    "buffer_load_dwordx4 v[24:27], v107, s[20:23], s35 offen offset:2048 sc1\n"
    "buffer_load_dwordx4 v[28:31], v107, s[20:23], s35 offen offset:3072 sc1\n"
    "buffer_load_dword v115, off, s[20:23], s40 sc1\n"
    "s_cmp_lg_u32 s84, s29\n"
    "s_cselect_b32 s30, 1048576, 0\n"
    "s_add_u32 s30, s30, s28\n"
    "s_add_u32 s31, s30, 4096\n"
    "s_add_u32 s46, s84, s26\n"
    "s_add_u32 s46, s46, s44\n"
    "v_or_b32 v118, s46, 1\n"
    "v_mov_b32 v119, s30\n"
    "ds_write_b64 v109, v[118:119] offset:9216\n"
    "s_add_u32 s85, s84, s27\n"
    "s_add_u32 s45, s45, 1\n"
    "s_cmp_gt_u32 s45, 0x40000\n"
    "s_cbranch_scc1 REND_%=\n"
    "s_cmp_gt_u32 s45, 1\n"
    "s_cbranch_scc1 RIC0_%=\n"
    "s_add_u32 s65, s65, s84\n"
    "s_mul_i32 s65, s65, 21\n"
    "s_add_u32 s65, s65, s66\n"
    "s_waitcnt lgkmcnt(0)\n"
    "s_add_u32 s65, s65, s56\n"
    "s_sub_u32 s65, s65, s58\n"
    "v_mov_b32 v74, s65\n"
    "ds_write_b32 v109, v74 offset:9224\n"
    "s_add_u32 s36, s36, 16384\n"
    "s_bitcmp1_b32 s32, 19\n"
    "s_cselect_b32 s46, 4194304, 0\n"
    "s_bitset1_b32 s46, 19\n"
    "s_xor_b32 s32, s32, s46\n"
    "s_add_u32 s33, s32, 65536\n"
    "ds_read_b128 v[32:35], v107 offset:0\n"
    "ds_read_b128 v[36:39], v107 offset:1024\n"
    "ds_read_b128 v[40:43], v107 offset:2048\n"
    "ds_read_b128 v[44:47], v107 offset:3072\n"
    "ds_read_b128 v[48:51], v107 offset:16384\n"
    "ds_read_b128 v[52:55], v107 offset:17408\n"
    "ds_read_b128 v[56:59], v107 offset:18432\n"
    "ds_read_b128 v[60:63], v107 offset:19456\n"
    "ds_read_b128 v[66:69], v109 offset:8192\n"
    "v_mov_b64 v[76:77], 0\n"
    "v_mov_b64 v[78:79], 0\n"
    "v_mov_b64 v[80:81], 0\n"
    "v_mov_b64 v[82:83], 0\n"
    "v_mov_b64 v[84:85], 0\n"
    "v_mov_b64 v[86:87], 0\n"
    "v_mov_b64 v[88:89], 0\n"
    "RIC0_%=:\n"
    "s_andn2_b32 s47, 3, s85\n"
    "s_andn2_b32 s85, s85, 3\n"
    "s_lshl2_add_u32 s78, s47, s81\n"
    "v_mov_b32 v74, 1\n"
    "s_waitcnt lgkmcnt(0)\n"
    "s_waitcnt vmcnt(8)\n"
    "v_mfma_f32_16x16x32_bf16 v[70:73], v[0:3], v[66:69], 0\n"
    "v_bitop3_b32 v74, v74, v0, v2 bitop3:0x80\n"
    "s_waitcnt vmcnt(7)\n"
    "v_mfma_f32_16x16x32_bf16 v[70:73], v[4:7], v[66:69], v[70:73]\n"
    "v_bitop3_b32 v74, v74, v4, v6 bitop3:0x80\n"
    "s_waitcnt vmcnt(6)\n"
    "v_mfma_f32_16x16x32_bf16 v[70:73], v[8:11], v[66:69], v[70:73]\n"
    "v_bitop3_b32 v74, v74, v8, v10 bitop3:0x80\n"
    "s_waitcnt vmcnt(5)\n"
    "v_mfma_f32_16x16x32_bf16 v[70:73], v[12:15], v[66:69], v[70:73]\n"
    "v_bitop3_b32 v74, v74, v12, v14 bitop3:0x80\n"
    "s_waitcnt vmcnt(4)\n"
    "v_mfma_f32_16x16x32_bf16 v[70:73], v[16:19], v[66:69], v[70:73]\n"
    "v_bitop3_b32 v74, v74, v16, v18 bitop3:0x80\n"
    "s_waitcnt vmcnt(3)\n"
    "v_mfma_f32_16x16x32_bf16 v[70:73], v[20:23], v[66:69], v[70:73]\n"
    "v_bitop3_b32 v74, v74, v20, v22 bitop3:0x80\n"
    "s_waitcnt vmcnt(2)\n"
    "v_mfma_f32_16x16x32_bf16 v[70:73], v[24:27], v[66:69], v[70:73]\n"
    "v_bitop3_b32 v74, v74, v24, v26 bitop3:0x80\n"
    "s_waitcnt vmcnt(1)\n"
    "v_mfma_f32_16x16x32_bf16 v[70:73], v[28:31], v[66:69], v[70:73]\n"
    "v_bitop3_b32 v74, v74, v28, v30 bitop3:0x80\n"
    "v_cmp_eq_u32 vcc, 0, v74\n"
    "s_cbranch_vccnz RI20_%=\n"
    "s_nop 4\n"
    "v_cvt_pk_bf16_f32 v118, v70, v71\n"
    "v_cvt_pk_bf16_f32 v119, v72, v73\n"
    "v_min_u32 v118, -2, v118\n"
    "buffer_store_dwordx2 v[118:119], v114, s[20:23], s36 offen sc1\n"
    "s_barrier\n"
    "s_memrealtime s[86:87]\n"
    "v_mov_b64 v[66:67], 0\n"
    "v_mov_b64 v[68:69], 0\n"
    "v_mov_b64 v[70:71], 0\n"
    "v_mov_b64 v[72:73], 0\n"
    "v_mov_b64 v[74:75], 0\n"
    "s_memrealtime s[92:93]\n"
    "s_bitcmp1_b32 s34, 19\n"
    "s_cselect_b32 s46, 4194304, 0\n"
    "s_bitset1_b32 s46, 19\n"
    "s_xor_b32 s34, s34, s46\n"
    "s_add_u32 s35, s34, 4096\n"
    "s_mov_b32 s45, 0\n"
    "s_cmp_lg_u32 s38, -1\n"
    "s_cbranch_scc1 RKW0_%=\n"
    "RKB0_%=:\n"
    "RD20_%=:\n"
    "s_memrealtime s[94:95]\n"
    "s_waitcnt lgkmcnt(2)\n"
    "s_sub_u32 s46, s86, s85\n"
    "s_cmp_lt_i32 s46, 0\n"
    "s_cbranch_scc0 RD20_%=x\n"
    "s_memrealtime s[86:87]\n"
    "s_waitcnt lgkmcnt(2)\n"
    "s_sub_u32 s46, s92, s85\n"
    "s_cmp_lt_i32 s46, 0\n"
    "s_cbranch_scc0 RD20_%=x\n"
    "s_memrealtime s[92:93]\n"
    "s_waitcnt lgkmcnt(2)\n"
    "s_sub_u32 s46, s94, s85\n"
    "s_cmp_lt_i32 s46, 0\n"
    "s_cbranch_scc1 RD20_%=\n"
    "RD20_%=x:\n"
    "s_setpc_b64 s[78:79]\n"
    ".p2align 4\n"
    "RF20_%=:\n"
    "s_nop 4\n"
    "s_nop 4\n"
    "s_nop 4\n"
    "s_cmp_lt_u32 s42, 63\n"
    "s_barrier\n"
    "s_cbranch_scc1 RLP0_%=\n"
    "s_branch REND_%=\n"
    "RRT0_%=:\n"
    "s_add_u32 s85, s90, 52\n"
    "s_add_u32 s45, s45, 1\n"
    "s_cmp_gt_u32 s45, 0x40000\n"
    "s_cbranch_scc1 REND_%=\n"
    "s_mov_b32 s50, 56\n"
    "s_or_b32 s86, s30, 1048576\n"
    "s_or_b32 s87, s31, 1048576\n"
    "RT0_%=:\n"
    "s_memrealtime s[90:91]\n"
    "s_waitcnt lgkmcnt(0)\n"
    "s_sub_u32 s46, s90, s85\n"
    "s_cmp_lt_i32 s46, 0\n"
    "s_cbranch_scc1 RT0_%=\n"
    "buffer_load_dwordx4 v[0:3], v107, s[20:23], s86 offen offset:0 sc1\n"
    "ds_read_b128 v[32:35], v107 offset:0\n"
    "ds_read_b128 v[36:39], v107 offset:1024\n"
    "ds_read_b128 v[40:43], v107 offset:2048\n"
    "ds_read_b128 v[44:47], v107 offset:3072\n"
    "s_nop 7\n"
    "buffer_load_dwordx4 v[4:7], v107, s[20:23], s86 offen offset:1024 sc1\n"
    "ds_read_b128 v[48:51], v107 offset:16384\n"
    "ds_read_b128 v[52:55], v107 offset:17408\n"
    "ds_read_b128 v[56:59], v107 offset:18432\n"
    "ds_read_b128 v[60:63], v107 offset:19456\n"
    "s_nop 7\n"
    "buffer_load_dwordx4 v[8:11], v107, s[20:23], s86 offen offset:2048 sc1\n"
    "v_mov_b64 v[66:67], 0\n"
    "v_mov_b64 v[68:69], 0\n"
    "v_mov_b64 v[70:71], 0\n"
    "v_mov_b64 v[72:73], 0\n"
    "s_nop 7\n"
    "buffer_load_dwordx4 v[12:15], v107, s[20:23], s86 offen offset:3072 sc1\n"
    "v_mov_b64 v[74:75], 0\n"
    "v_mov_b64 v[76:77], 0\n"
    "v_mov_b64 v[78:79], 0\n"
    "v_mov_b64 v[80:81], 0\n"
    "s_nop 7\n"
    "buffer_load_dwordx4 v[16:19], v107, s[20:23], s87 offen offset:0 sc1\n"
    "s_nop 13\n"
    "buffer_load_dwordx4 v[20:23], v107, s[20:23], s87 offen offset:1024 sc1\n"
    "s_nop 13\n"
    "buffer_load_dwordx4 v[24:27], v107, s[20:23], s87 offen offset:2048 sc1\n"
    "s_nop 13\n"
    "buffer_load_dwordx4 v[28:31], v107, s[20:23], s87 offen offset:3072 sc1\n"
    "s_waitcnt lgkmcnt(0)\n"
    "s_branch RRB0_%=\n"
    "RI10_%=:\n"
    "s_waitcnt vmcnt(7)\n"
    "v_lshlrev_b32 v0, 3, v107\n"
    "v_and_b32 v0, 0xf80, v0\n"
    "buffer_load_dword v75, v0, s[20:23], s69 offen sc1\n"
    "s_branch RI00_%=\n"
    "RI20_%=:\n"
    "s_bitcmp0_b32 s34, 30\n"
    "s_cbranch_scc1 RIB0_%=\n"
    "s_waitcnt vmcnt(0)\n"
    "v_cmp_gt_u32 vcc, 1, v75\n"
    "s_cbranch_vccz RI40_%=\n"
    "v_lshlrev_b32 v0, 3, v107\n"
    "v_and_b32 v0, 0xf80, v0\n"
    "s_mov_b32 s46, 0\n"
    "s_branch RI30_%=b\n"
    "RI30_%=:\n"
    "s_sleep 2\n"
    "s_add_u32 s46, s46, 1\n"
    "s_cmp_gt_u32 s46, 262144\n"
    "s_cbranch_scc1 REND_%=\n"
    "RI30_%=b:\n"
    "buffer_load_dword v75, v0, s[20:23], s69 offen sc1\n"
    "s_waitcnt vmcnt(0)\n"
    "v_cmp_gt_u32 vcc, 1, v75\n"
    "s_cbranch_vccnz RI30_%=\n"
    "RI40_%=:\n"
    "v_subrev_u32 v1, s63, v75\n"
    "s_nop 1\n"
    "v_max_i32_dpp v1, v1, v1 row_ror:1 row_mask:0xf bank_mask:0xf\n"
    "s_nop 1\n"
    "v_max_i32_dpp v1, v1, v1 row_ror:2 row_mask:0xf bank_mask:0xf\n"
    "s_nop 1\n"
    "v_max_i32_dpp v1, v1, v1 row_ror:4 row_mask:0xf bank_mask:0xf\n"
    "s_nop 1\n"
    "v_max_i32_dpp v1, v1, v1 row_ror:8 row_mask:0xf bank_mask:0xf\n"
    "s_nop 0\n"
    "v_readlane_b32 s46, v1, 0\n"
    "v_readlane_b32 s84, v1, 16\n"
    "s_max_i32 s84, s84, s46\n"
    "s_min_i32 s84, s84, 4000\n"
    "s_add_u32 s84, s84, s63\n"
    "s_add_u32 s84, s84, 176\n"
    "s_mov_b32 s29, s84\n"
    "s_add_u32 s26, s26, 6\n"
    "s_add_u32 s27, s27, 6\n"
    "s_mov_b32 s34, s68\n"
    "s_add_u32 s35, s34, 4096\n"
    "s_memrealtime s[86:87]\n"
    "s_waitcnt lgkmcnt(0)\n"
    "s_mov_b32 s68, s86\n"
    "s_branch RIB0_%=\n"
    "RKW0_%=:\n"
    "s_waitcnt vmcnt(1)\n"
    "v_cmp_gt_u32 vcc, s43, v115\n"
    "s_cbranch_vccz RK30_%=\n"
    "s_mov_b32 s45, 0\n"
    "RK20_%=:\n"
    "buffer_load_dword v115, off, s[20:23], s40 sc1\n"
    "s_add_u32 s45, s45, 1\n"
    "s_cmp_gt_u32 s45, 0x40000\n"
    "s_cbranch_scc1 REND_%=\n"
    "s_waitcnt vmcnt(0)\n"
    "v_cmp_gt_u32 vcc, s43, v115\n"
    "s_cbranch_vccnz RK20_%=\n"
    "RK30_%=:\n"
    "buffer_load_dwordx4 v[90:93], v107, s[20:23], s38 offen offset:0 sc1\n"
    "buffer_load_dwordx4 v[94:97], v107, s[20:23], s38 offen offset:1024 sc1\n"
    "buffer_load_dwordx4 v[98:101], v107, s[20:23], s38 offen offset:2048 sc1\n"
    "buffer_load_dwordx4 v[102:105], v107, s[20:23], s38 offen offset:3072 sc1\n"
    "s_mov_b32 s45, 0\n"
    "s_branch RKB0_%=\n"
    "RW1_%=:\n"
    "s_mov_b64 s[76:77], s[10:11]\n"
    "s_and_b32 s77, s77, 0xffff\n"
    "s_mov_b32 s78, 4194304\n"
    "s_mov_b32 s79, 0x20000\n"
    "s_mov_b32 m0, 4096\n"
    "s_add_u32 s80, s61, 32768\n"
    "buffer_load_dwordx4 v24, s[76:79], s80 offen offset:0  lds\n"
    "s_add_u32 s80, s61, 35840\n"
    "buffer_load_dwordx4 v24, s[76:79], s80 offen offset:1024  lds\n"
    "s_add_u32 s80, s61, 47104\n"
    "buffer_load_dwordx4 v25, s[76:79], s80 offen offset:2048  lds\n"
    "s_add_u32 s80, s61, 50176\n"
    "buffer_load_dwordx4 v25, s[76:79], s80 offen offset:3072  lds\n"
    "s_mov_b32 m0, 20480\n"
    "s_add_u32 s80, s61, 40960\n"
    "buffer_load_dwordx4 v24, s[76:79], s80 offen offset:0  lds\n"
    "s_add_u32 s80, s61, 44032\n"
    "buffer_load_dwordx4 v24, s[76:79], s80 offen offset:1024  lds\n"
    "s_add_u32 s80, s61, 55296\n"
    "buffer_load_dwordx4 v25, s[76:79], s80 offen offset:2048  lds\n"
    "s_add_u32 s80, s61, 58368\n"
    "buffer_load_dwordx4 v25, s[76:79], s80 offen offset:3072  lds\n"
    "s_mov_b32 m0, 36864\n"
    "s_add_u32 s80, s61, 65536\n"
    "buffer_load_dwordx4 v24, s[76:79], s80 offen offset:0  lds\n"
    "s_add_u32 s80, s61, 68608\n"
    "buffer_load_dwordx4 v24, s[76:79], s80 offen offset:1024  lds\n"
    "s_add_u32 s80, s61, 79872\n"
    "buffer_load_dwordx4 v25, s[76:79], s80 offen offset:2048  lds\n"
    "s_add_u32 s80, s61, 82944\n"
    "buffer_load_dwordx4 v25, s[76:79], s80 offen offset:3072  lds\n"
    "s_mov_b32 m0, 53248\n"
    "s_add_u32 s80, s61, 73728\n"
    "buffer_load_dwordx4 v24, s[76:79], s80 offen offset:0  lds\n"
    "s_add_u32 s80, s61, 76800\n"
    "buffer_load_dwordx4 v24, s[76:79], s80 offen offset:1024  lds\n"
    "s_add_u32 s80, s61, 88064\n"
    "buffer_load_dwordx4 v25, s[76:79], s80 offen offset:2048  lds\n"
    "s_add_u32 s80, s61, 91136\n"
    "buffer_load_dwordx4 v25, s[76:79], s80 offen offset:3072  lds\n"
    "s_mov_b32 m0, 69632\n"
    "s_add_u32 s80, s61, 98304\n"
    "buffer_load_dwordx4 v24, s[76:79], s80 offen offset:0  lds\n"
    "s_add_u32 s80, s61, 101376\n"
    "buffer_load_dwordx4 v24, s[76:79], s80 offen offset:1024  lds\n"
    "s_add_u32 s80, s61, 112640\n"
    "buffer_load_dwordx4 v25, s[76:79], s80 offen offset:2048  lds\n"
    "s_add_u32 s80, s61, 115712\n"
    "buffer_load_dwordx4 v25, s[76:79], s80 offen offset:3072  lds\n"
    "s_mov_b32 m0, 86016\n"
    "s_add_u32 s80, s61, 106496\n"
    "buffer_load_dwordx4 v24, s[76:79], s80 offen offset:0  lds\n"
    "s_add_u32 s80, s61, 109568\n"
    "buffer_load_dwordx4 v24, s[76:79], s80 offen offset:1024  lds\n"
    "s_add_u32 s80, s61, 120832\n"
    "buffer_load_dwordx4 v25, s[76:79], s80 offen offset:2048  lds\n"
    "s_add_u32 s80, s61, 123904\n"
    "buffer_load_dwordx4 v25, s[76:79], s80 offen offset:3072  lds\n"
    "s_mov_b32 m0, 102400\n"
    "s_add_u32 s80, s61, 0\n"
    "buffer_load_dwordx4 v24, s[76:79], s80 offen offset:0  lds\n"
    "s_add_u32 s80, s61, 3072\n"
    "buffer_load_dwordx4 v24, s[76:79], s80 offen offset:1024  lds\n"
    "s_add_u32 s80, s61, 14336\n"
    "buffer_load_dwordx4 v25, s[76:79], s80 offen offset:2048  lds\n"
    "s_add_u32 s80, s61, 17408\n"
    "buffer_load_dwordx4 v25, s[76:79], s80 offen offset:3072  lds\n"
    "s_mov_b32 m0, 118784\n"
    "s_add_u32 s80, s61, 8192\n"
    "buffer_load_dwordx4 v24, s[76:79], s80 offen offset:0  lds\n"
    "s_add_u32 s80, s61, 11264\n"
    "buffer_load_dwordx4 v24, s[76:79], s80 offen offset:1024  lds\n"
    "s_add_u32 s80, s61, 22528\n"
    "buffer_load_dwordx4 v25, s[76:79], s80 offen offset:2048  lds\n"
    "s_add_u32 s80, s61, 25600\n"
    "buffer_load_dwordx4 v25, s[76:79], s80 offen offset:3072  lds\n"
    "s_bfe_u32 s24, %1, 0x10000\n"
    "s_and_b32 s26, s16, 0xffff\n"
    "s_and_b32 s27, s17, 0xffff\n"
    "s_mov_b32 s48, 0x3e0293ee\n"
    "s_mov_b32 s96, 0x00ff00ff\n"
    "s_mov_b32 s97, s96\n"
    "s_not_b64 s[98:99], s[96:97]\n"
    "v_lshlrev_b32 v107, 4, v0\n"
    "v_mov_b64 v[64:65], 0\n"
    "v_lshlrev_b32 v5, 4, v0\n"
    "v_add_u32 v117, 135168, v5\n"
    "v_and_b32 v3, 7, v0\n"
    "v_bfe_u32 v4, v0, 3, 1\n"
    "v_mov_b32 v5, 0x4444\n"
    "v_mov_b32 v6, 0xeeee\n"
    "v_cmp_eq_u32 vcc, 1, v4\n"
    "s_nop 1\n"
    "v_cndmask_b32 v106, v5, v6, vcc\n"
    "v_add_u32 v108, 0x10000, v107\n"
    "v_add_u32 v109, 131072, v107\n"
    "v_and_b32 v5, 31, v0\n"
    "v_lshlrev_b32 v5, 4, v5\n"
    "v_add_u32 v110, 135680, v5\n"
    "v_lshl_add_u32 v5, v2, 3, v3\n"
    "v_lshlrev_b32 v5, 4, v5\n"
    "v_add_u32 v112, 137216, v5\n"
    "v_lshl_add_u32 v111, v4, 3, v112\n"
    "v_lshrrev_b32 v5, 1, v2\n"
    "v_lshl_add_u32 v6, v5, 1, v4\n"
    "v_lshlrev_b32 v6, 14, v6\n"
    "v_and_b32 v7, 1, v2\n"
    "v_lshl_add_u32 v8, v7, 3, v3\n"
    "v_lshl_add_u32 v113, v8, 4, v6\n"
    "v_mul_u32_u24 v8, 2176, v7\n"
    "v_lshl_add_u32 v8, v5, 10, v8\n"
    "v_lshrrev_b32 v9, 1, v1\n"
    "v_lshl_add_u32 v26, v9, 3, v8\n"
    "v_xor_b32 v27, 128, v26\n"
    "v_xor_b32 v28, 2176, v26\n"
    "v_xor_b32 v29, 2048, v26\n"
    "v_and_b32 v10, 1, v0\n"
    "v_mul_u32_u24 v8, 0x020202, v10\n"
    "v_lshl_add_u32 v8, v10, 25, v8\n"
    "v_add_u32 v68, 0x05040100, v8\n"
    "v_and_b32 v8, 3, v0\n"
    "v_lshl_add_u32 v8, v5, 2, v8\n"
    "v_bfe_u32 v9, v0, 2, 1\n"
    "v_lshl_add_u32 v8, v9, 3, v8\n"
    "v_lshlrev_b32 v8, 4, v8\n"
    "v_lshl_add_u32 v8, v7, 3, v8\n"
    "v_lshl_add_u32 v114, v4, 20, v8\n"
    "v_lshlrev_b32 v10, 4, v10\n"
    "v_mov_b32 v11, 0x3f80\n"
    "v_lshlrev_b32 v11, v10, v11\n"
    "v_cmp_eq_u32 vcc, v9, v7\n"
    "s_nop 1\n"
    "v_cndmask_b32 v12, 0, v11, vcc\n"
    "v_bfe_u32 v13, v0, 1, 1\n"
    "v_lshl_add_u32 v13, v4, 1, v13\n"
    "v_cmp_eq_u32 vcc, 0, v13\n"
    "s_nop 1\n"
    "v_cndmask_b32 v120, 0, v12, vcc\n"
    "v_cmp_eq_u32 vcc, 1, v13\n"
    "s_nop 1\n"
    "v_cndmask_b32 v121, 0, v12, vcc\n"
    "v_cmp_eq_u32 vcc, 2, v13\n"
    "s_nop 1\n"
    "v_cndmask_b32 v122, 0, v12, vcc\n"
    "v_cmp_eq_u32 vcc, 3, v13\n"
    "s_nop 1\n"
    "v_cndmask_b32 v123, 0, v12, vcc\n"
    "v_bfe_u32 v13, v0, 1, 2\n"
    "v_cmp_eq_u32 vcc, 0, v13\n"
    "s_nop 1\n"
    "v_cndmask_b32 v16, 0, v11, vcc\n"
    "v_cmp_eq_u32 vcc, 1, v13\n"
    "s_nop 1\n"
    "v_cndmask_b32 v17, 0, v11, vcc\n"
    "v_cmp_eq_u32 vcc, 2, v13\n"
    "s_nop 1\n"
    "v_cndmask_b32 v18, 0, v11, vcc\n"
    "v_cmp_eq_u32 vcc, 3, v13\n"
    "s_nop 1\n"
    "v_cndmask_b32 v19, 0, v11, vcc\n"
    "v_mov_b32 v124, 0x3f803f80\n"
    "v_mov_b32 v125, 0x3f803f80\n"
    "v_mov_b32 v126, 0x3f003f00\n"
    "v_mov_b32 v127, 0\n"
    "v_mov_b64 v[118:119], 0\n"
    "s_waitcnt vmcnt(28) lgkmcnt(0)\n"
    "s_barrier\n"
    "ds_read_b64 v[70:71], v26 offset:4096\n"
    "ds_read_b64 v[72:73], v26 offset:4352\n"
    "ds_read_b64 v[74:75], v26 offset:4608\n"
    "ds_read_b64 v[76:77], v26 offset:4864\n"
    "ds_read_b64 v[78:79], v28 offset:4160\n"
    "ds_read_b64 v[80:81], v28 offset:4416\n"
    "ds_read_b64 v[82:83], v28 offset:4672\n"
    "ds_read_b64 v[84:85], v28 offset:4928\n"
    "ds_read_b64 v[86:87], v27 offset:4096\n"
    "ds_read_b64 v[88:89], v27 offset:4352\n"
    "ds_read_b64 v[90:91], v27 offset:4608\n"
    "ds_read_b64 v[92:93], v27 offset:4864\n"
    "ds_read_b64 v[94:95], v29 offset:4160\n"
    "ds_read_b64 v[96:97], v29 offset:4416\n"
    "ds_read_b64 v[98:99], v29 offset:4672\n"
    "ds_read_b64 v[100:101], v29 offset:4928\n"
    "s_waitcnt lgkmcnt(14)\n"
    "v_perm_b32 v128, v72, v70, v68\n"
    "v_perm_b32 v160, v73, v71, v68\n"
    "s_waitcnt lgkmcnt(12)\n"
    "v_perm_b32 v130, v76, v74, v68\n"
    "v_perm_b32 v162, v77, v75, v68\n"
    "s_waitcnt lgkmcnt(10)\n"
    "v_perm_b32 v129, v80, v78, v68\n"
    "v_perm_b32 v161, v81, v79, v68\n"
    "s_waitcnt lgkmcnt(8)\n"
    "v_perm_b32 v131, v84, v82, v68\n"
    "v_perm_b32 v163, v85, v83, v68\n"
    "s_waitcnt lgkmcnt(6)\n"
    "v_perm_b32 v192, v88, v86, v68\n"
    "v_perm_b32 v224, v89, v87, v68\n"
    "s_waitcnt lgkmcnt(4)\n"
    "v_perm_b32 v194, v92, v90, v68\n"
    "v_perm_b32 v226, v93, v91, v68\n"
    "s_waitcnt lgkmcnt(2)\n"
    "v_perm_b32 v193, v96, v94, v68\n"
    "v_perm_b32 v225, v97, v95, v68\n"
    "s_waitcnt lgkmcnt(0)\n"
    "v_perm_b32 v195, v100, v98, v68\n"
    "v_perm_b32 v227, v101, v99, v68\n"
    "s_waitcnt vmcnt(24) lgkmcnt(0)\n"
    "s_barrier\n"
    "s_mov_b64 s[76:77], s[4:5]\n"
    "s_and_b32 s77, s77, 0xffff\n"
    "s_mov_b32 s78, 4194304\n"
    "s_mov_b32 s79, 0x20000\n"
    "s_mov_b32 m0, 4096\n"
    "s_add_u32 s80, s60, 0\n"
    "buffer_load_dwordx4 v20, s[76:79], s80 offen offset:0  lds\n"
    "s_add_u32 s80, s60, 31744\n"
    "buffer_load_dwordx4 v20, s[76:79], s80 offen offset:1024  lds\n"
    "s_add_u32 s80, s60, 63488\n"
    "buffer_load_dwordx4 v21, s[76:79], s80 offen offset:2048  lds\n"
    "s_add_u32 s80, s60, 95232\n"
    "buffer_load_dwordx4 v21, s[76:79], s80 offen offset:3072  lds\n"
    "ds_read_b64 v[70:71], v26 offset:20480\n"
    "ds_read_b64 v[72:73], v26 offset:20736\n"
    "ds_read_b64 v[74:75], v26 offset:20992\n"
    "ds_read_b64 v[76:77], v26 offset:21248\n"
    "ds_read_b64 v[78:79], v28 offset:20544\n"
    "ds_read_b64 v[80:81], v28 offset:20800\n"
    "ds_read_b64 v[82:83], v28 offset:21056\n"
    "ds_read_b64 v[84:85], v28 offset:21312\n"
    "ds_read_b64 v[86:87], v27 offset:20480\n"
    "ds_read_b64 v[88:89], v27 offset:20736\n"
    "ds_read_b64 v[90:91], v27 offset:20992\n"
    "ds_read_b64 v[92:93], v27 offset:21248\n"
    "ds_read_b64 v[94:95], v29 offset:20544\n"
    "ds_read_b64 v[96:97], v29 offset:20800\n"
    "ds_read_b64 v[98:99], v29 offset:21056\n"
    "ds_read_b64 v[100:101], v29 offset:21312\n"
    "s_waitcnt lgkmcnt(14)\n"
    "v_perm_b32 v132, v72, v70, v68\n"
    "v_perm_b32 v164, v73, v71, v68\n"
    "s_waitcnt lgkmcnt(12)\n"
    "v_perm_b32 v134, v76, v74, v68\n"
    "v_perm_b32 v166, v77, v75, v68\n"
    "s_waitcnt lgkmcnt(10)\n"
    "v_perm_b32 v133, v80, v78, v68\n"
    "v_perm_b32 v165, v81, v79, v68\n"
    "s_waitcnt lgkmcnt(8)\n"
    "v_perm_b32 v135, v84, v82, v68\n"
    "v_perm_b32 v167, v85, v83, v68\n"
    "s_waitcnt lgkmcnt(6)\n"
    "v_perm_b32 v196, v88, v86, v68\n"
    "v_perm_b32 v228, v89, v87, v68\n"
    "s_waitcnt lgkmcnt(4)\n"
    "v_perm_b32 v198, v92, v90, v68\n"
    "v_perm_b32 v230, v93, v91, v68\n"
    "s_waitcnt lgkmcnt(2)\n"
    "v_perm_b32 v197, v96, v94, v68\n"
    "v_perm_b32 v229, v97, v95, v68\n"
    "s_waitcnt lgkmcnt(0)\n"
    "v_perm_b32 v199, v100, v98, v68\n"
    "v_perm_b32 v231, v101, v99, v68\n"
    "s_waitcnt vmcnt(24) lgkmcnt(0)\n"
    "s_barrier\n"
    "s_mov_b32 m0, 20480\n"
    "s_add_u32 s80, s60, 524288\n"
    "buffer_load_dwordx4 v20, s[76:79], s80 offen offset:0  lds\n"
    "s_add_u32 s80, s60, 556032\n"
    "buffer_load_dwordx4 v20, s[76:79], s80 offen offset:1024  lds\n"
    "s_add_u32 s80, s60, 587776\n"
    "buffer_load_dwordx4 v21, s[76:79], s80 offen offset:2048  lds\n"
    "s_add_u32 s80, s60, 619520\n"
    "buffer_load_dwordx4 v21, s[76:79], s80 offen offset:3072  lds\n"
    "ds_read_b64 v[70:71], v26 offset:36864\n"
    "ds_read_b64 v[72:73], v26 offset:37120\n"
    "ds_read_b64 v[74:75], v26 offset:37376\n"
    "ds_read_b64 v[76:77], v26 offset:37632\n"
    "ds_read_b64 v[78:79], v26 offset:36928\n"
    "ds_read_b64 v[80:81], v26 offset:37184\n"
    "ds_read_b64 v[82:83], v26 offset:37440\n"
    "ds_read_b64 v[84:85], v26 offset:37696\n"
    "ds_read_b64 v[86:87], v27 offset:36864\n"
    "ds_read_b64 v[88:89], v27 offset:37120\n"
    "ds_read_b64 v[90:91], v27 offset:37376\n"
    "ds_read_b64 v[92:93], v27 offset:37632\n"
    "ds_read_b64 v[94:95], v27 offset:36928\n"
    "ds_read_b64 v[96:97], v27 offset:37184\n"
    "ds_read_b64 v[98:99], v27 offset:37440\n"
    "ds_read_b64 v[100:101], v27 offset:37696\n"
    "s_waitcnt lgkmcnt(14)\n"
    "v_perm_b32 v136, v72, v70, v68\n"
    "v_perm_b32 v168, v73, v71, v68\n"
    "s_waitcnt lgkmcnt(12)\n"
    "v_perm_b32 v138, v76, v74, v68\n"
    "v_perm_b32 v170, v77, v75, v68\n"
    "s_waitcnt lgkmcnt(10)\n"
    "v_perm_b32 v137, v80, v78, v68\n"
    "v_perm_b32 v169, v81, v79, v68\n"
    "s_waitcnt lgkmcnt(8)\n"
    "v_perm_b32 v139, v84, v82, v68\n"
    "v_perm_b32 v171, v85, v83, v68\n"
    "s_waitcnt lgkmcnt(6)\n"
    "v_perm_b32 v200, v88, v86, v68\n"
    "v_perm_b32 v232, v89, v87, v68\n"
    "s_waitcnt lgkmcnt(4)\n"
    "v_perm_b32 v202, v92, v90, v68\n"
    "v_perm_b32 v234, v93, v91, v68\n"
    "s_waitcnt lgkmcnt(2)\n"
    "v_perm_b32 v201, v96, v94, v68\n"
    "v_perm_b32 v233, v97, v95, v68\n"
    "s_waitcnt lgkmcnt(0)\n"
    "v_perm_b32 v203, v100, v98, v68\n"
    "v_perm_b32 v235, v101, v99, v68\n"
    "s_waitcnt vmcnt(24) lgkmcnt(0)\n"
    "s_barrier\n"
    "s_mov_b32 m0, 36864\n"
    "s_add_u32 s80, s60, 1048576\n"
    "buffer_load_dwordx4 v20, s[76:79], s80 offen offset:0  lds\n"
    "s_add_u32 s80, s60, 1080320\n"
    "buffer_load_dwordx4 v20, s[76:79], s80 offen offset:1024  lds\n"
    "s_add_u32 s80, s60, 1112064\n"
    "buffer_load_dwordx4 v21, s[76:79], s80 offen offset:2048  lds\n"
    "s_add_u32 s80, s60, 1143808\n"
    "buffer_load_dwordx4 v21, s[76:79], s80 offen offset:3072  lds\n"
    "ds_read_b64 v[70:71], v26 offset:53248\n"
    "ds_read_b64 v[72:73], v26 offset:53504\n"
    "ds_read_b64 v[74:75], v26 offset:53760\n"
    "ds_read_b64 v[76:77], v26 offset:54016\n"
    "ds_read_b64 v[78:79], v26 offset:53312\n"
    "ds_read_b64 v[80:81], v26 offset:53568\n"
    "ds_read_b64 v[82:83], v26 offset:53824\n"
    "ds_read_b64 v[84:85], v26 offset:54080\n"
    "ds_read_b64 v[86:87], v27 offset:53248\n"
    "ds_read_b64 v[88:89], v27 offset:53504\n"
    "ds_read_b64 v[90:91], v27 offset:53760\n"
    "ds_read_b64 v[92:93], v27 offset:54016\n"
    "ds_read_b64 v[94:95], v27 offset:53312\n"
    "ds_read_b64 v[96:97], v27 offset:53568\n"
    "ds_read_b64 v[98:99], v27 offset:53824\n"
    "ds_read_b64 v[100:101], v27 offset:54080\n"
    "s_waitcnt lgkmcnt(14)\n"
    "v_perm_b32 v140, v72, v70, v68\n"
    "v_perm_b32 v172, v73, v71, v68\n"
    "s_waitcnt lgkmcnt(12)\n"
    "v_perm_b32 v142, v76, v74, v68\n"
    "v_perm_b32 v174, v77, v75, v68\n"
    "s_waitcnt lgkmcnt(10)\n"
    "v_perm_b32 v141, v80, v78, v68\n"
    "v_perm_b32 v173, v81, v79, v68\n"
    "s_waitcnt lgkmcnt(8)\n"
    "v_perm_b32 v143, v84, v82, v68\n"
    "v_perm_b32 v175, v85, v83, v68\n"
    "s_waitcnt lgkmcnt(6)\n"
    "v_perm_b32 v204, v88, v86, v68\n"
    "v_perm_b32 v236, v89, v87, v68\n"
    "s_waitcnt lgkmcnt(4)\n"
    "v_perm_b32 v206, v92, v90, v68\n"
    "v_perm_b32 v238, v93, v91, v68\n"
    "s_waitcnt lgkmcnt(2)\n"
    "v_perm_b32 v205, v96, v94, v68\n"
    "v_perm_b32 v237, v97, v95, v68\n"
    "s_waitcnt lgkmcnt(0)\n"
    "v_perm_b32 v207, v100, v98, v68\n"
    "v_perm_b32 v239, v101, v99, v68\n"
    "s_waitcnt vmcnt(24) lgkmcnt(0)\n"
    "s_barrier\n"
    "s_mov_b32 m0, 53248\n"
    "s_add_u32 s80, s60, 1572864\n"
    "buffer_load_dwordx4 v20, s[76:79], s80 offen offset:0  lds\n"
    "s_add_u32 s80, s60, 1604608\n"
    "buffer_load_dwordx4 v20, s[76:79], s80 offen offset:1024  lds\n"
    "s_add_u32 s80, s60, 1636352\n"
    "buffer_load_dwordx4 v21, s[76:79], s80 offen offset:2048  lds\n"
    "s_add_u32 s80, s60, 1668096\n"
    "buffer_load_dwordx4 v21, s[76:79], s80 offen offset:3072  lds\n"
    "v_add_u32 v22, 65536, v22\n"
    "v_add_u32 v23, 65536, v23\n"
    "v_add_u32 v26, 65536, v26\n"
    "v_add_u32 v27, 65536, v27\n"
    "v_add_u32 v28, 65536, v28\n"
    "v_add_u32 v29, 65536, v29\n"
    "ds_read_b64 v[70:71], v26 offset:4096\n"
    "ds_read_b64 v[72:73], v26 offset:4352\n"
    "ds_read_b64 v[74:75], v26 offset:4608\n"
    "ds_read_b64 v[76:77], v26 offset:4864\n"
    "ds_read_b64 v[78:79], v26 offset:4160\n"
    "ds_read_b64 v[80:81], v26 offset:4416\n"
    "ds_read_b64 v[82:83], v26 offset:4672\n"
    "ds_read_b64 v[84:85], v26 offset:4928\n"
    "ds_read_b64 v[86:87], v27 offset:4096\n"
    "ds_read_b64 v[88:89], v27 offset:4352\n"
    "ds_read_b64 v[90:91], v27 offset:4608\n"
    "ds_read_b64 v[92:93], v27 offset:4864\n"
    "ds_read_b64 v[94:95], v27 offset:4160\n"
    "ds_read_b64 v[96:97], v27 offset:4416\n"
    "ds_read_b64 v[98:99], v27 offset:4672\n"
    "ds_read_b64 v[100:101], v27 offset:4928\n"
    "s_waitcnt lgkmcnt(14)\n"
    "v_perm_b32 v144, v72, v70, v68\n"
    "v_perm_b32 v176, v73, v71, v68\n"
    "s_waitcnt lgkmcnt(12)\n"
    "v_perm_b32 v146, v76, v74, v68\n"
    "v_perm_b32 v178, v77, v75, v68\n"
    "s_waitcnt lgkmcnt(10)\n"
    "v_perm_b32 v145, v80, v78, v68\n"
    "v_perm_b32 v177, v81, v79, v68\n"
    "s_waitcnt lgkmcnt(8)\n"
    "v_perm_b32 v147, v84, v82, v68\n"
    "v_perm_b32 v179, v85, v83, v68\n"
    "s_waitcnt lgkmcnt(6)\n"
    "v_perm_b32 v208, v88, v86, v68\n"
    "v_perm_b32 v240, v89, v87, v68\n"
    "s_waitcnt lgkmcnt(4)\n"
    "v_perm_b32 v210, v92, v90, v68\n"
    "v_perm_b32 v242, v93, v91, v68\n"
    "s_waitcnt lgkmcnt(2)\n"
    "v_perm_b32 v209, v96, v94, v68\n"
    "v_perm_b32 v241, v97, v95, v68\n"
    "s_waitcnt lgkmcnt(0)\n"
    "v_perm_b32 v211, v100, v98, v68\n"
    "v_perm_b32 v243, v101, v99, v68\n"
    "s_waitcnt vmcnt(24) lgkmcnt(0)\n"
    "s_barrier\n"
    "s_mov_b32 m0, 69632\n"
    "s_add_u32 s80, s60, 2097152\n"
    "buffer_load_dwordx4 v20, s[76:79], s80 offen offset:0  lds\n"
    "s_add_u32 s80, s60, 2128896\n"
    "buffer_load_dwordx4 v20, s[76:79], s80 offen offset:1024  lds\n"
    "s_add_u32 s80, s60, 2160640\n"
    "buffer_load_dwordx4 v21, s[76:79], s80 offen offset:2048  lds\n"
    "s_add_u32 s80, s60, 2192384\n"
    "buffer_load_dwordx4 v21, s[76:79], s80 offen offset:3072  lds\n"
    "ds_read_b64 v[70:71], v26 offset:20480\n"
    "ds_read_b64 v[72:73], v26 offset:20736\n"
    "ds_read_b64 v[74:75], v26 offset:20992\n"
    "ds_read_b64 v[76:77], v26 offset:21248\n"
    "ds_read_b64 v[78:79], v26 offset:20544\n"
    "ds_read_b64 v[80:81], v26 offset:20800\n"
    "ds_read_b64 v[82:83], v26 offset:21056\n"
    "ds_read_b64 v[84:85], v26 offset:21312\n"
    "ds_read_b64 v[86:87], v27 offset:20480\n"
    "ds_read_b64 v[88:89], v27 offset:20736\n"
    "ds_read_b64 v[90:91], v27 offset:20992\n"
    "ds_read_b64 v[92:93], v27 offset:21248\n"
    "ds_read_b64 v[94:95], v27 offset:20544\n"
    "ds_read_b64 v[96:97], v27 offset:20800\n"
    "ds_read_b64 v[98:99], v27 offset:21056\n"
    "ds_read_b64 v[100:101], v27 offset:21312\n"
    "s_waitcnt lgkmcnt(14)\n"
    "v_perm_b32 v148, v72, v70, v68\n"
    "v_perm_b32 v180, v73, v71, v68\n"
    "s_waitcnt lgkmcnt(12)\n"
    "v_perm_b32 v150, v76, v74, v68\n"
    "v_perm_b32 v182, v77, v75, v68\n"
    "s_waitcnt lgkmcnt(10)\n"
    "v_perm_b32 v149, v80, v78, v68\n"
    "v_perm_b32 v181, v81, v79, v68\n"
    "s_waitcnt lgkmcnt(8)\n"
    "v_perm_b32 v151, v84, v82, v68\n"
    "v_perm_b32 v183, v85, v83, v68\n"
    "s_waitcnt lgkmcnt(6)\n"
    "v_perm_b32 v212, v88, v86, v68\n"
    "v_perm_b32 v244, v89, v87, v68\n"
    "s_waitcnt lgkmcnt(4)\n"
    "v_perm_b32 v214, v92, v90, v68\n"
    "v_perm_b32 v246, v93, v91, v68\n"
    "s_waitcnt lgkmcnt(2)\n"
    "v_perm_b32 v213, v96, v94, v68\n"
    "v_perm_b32 v245, v97, v95, v68\n"
    "s_waitcnt lgkmcnt(0)\n"
    "v_perm_b32 v215, v100, v98, v68\n"
    "v_perm_b32 v247, v101, v99, v68\n"
    "s_waitcnt vmcnt(24) lgkmcnt(0)\n"
    "s_barrier\n"
    "s_mov_b32 m0, 86016\n"
    "s_add_u32 s80, s60, 2621440\n"
    "buffer_load_dwordx4 v20, s[76:79], s80 offen offset:0  lds\n"
    "s_add_u32 s80, s60, 2653184\n"
    "buffer_load_dwordx4 v20, s[76:79], s80 offen offset:1024  lds\n"
    "s_add_u32 s80, s60, 2684928\n"
    "buffer_load_dwordx4 v21, s[76:79], s80 offen offset:2048  lds\n"
    "s_add_u32 s80, s60, 2716672\n"
    "buffer_load_dwordx4 v21, s[76:79], s80 offen offset:3072  lds\n"
    "ds_read_b64 v[70:71], v26 offset:36864\n"
    "ds_read_b64 v[72:73], v26 offset:37120\n"
    "ds_read_b64 v[74:75], v26 offset:37376\n"
    "ds_read_b64 v[76:77], v26 offset:37632\n"
    "ds_read_b64 v[78:79], v26 offset:36928\n"
    "ds_read_b64 v[80:81], v26 offset:37184\n"
    "ds_read_b64 v[82:83], v26 offset:37440\n"
    "ds_read_b64 v[84:85], v26 offset:37696\n"
    "ds_read_b64 v[86:87], v27 offset:36864\n"
    "ds_read_b64 v[88:89], v27 offset:37120\n"
    "ds_read_b64 v[90:91], v27 offset:37376\n"
    "ds_read_b64 v[92:93], v27 offset:37632\n"
    "ds_read_b64 v[94:95], v27 offset:36928\n"
    "ds_read_b64 v[96:97], v27 offset:37184\n"
    "ds_read_b64 v[98:99], v27 offset:37440\n"
    "ds_read_b64 v[100:101], v27 offset:37696\n"
    "s_waitcnt lgkmcnt(14)\n"
    "v_perm_b32 v152, v72, v70, v68\n"
    "v_perm_b32 v184, v73, v71, v68\n"
    "s_waitcnt lgkmcnt(12)\n"
    "v_perm_b32 v154, v76, v74, v68\n"
    "v_perm_b32 v186, v77, v75, v68\n"
    "s_waitcnt lgkmcnt(10)\n"
    "v_perm_b32 v153, v80, v78, v68\n"
    "v_perm_b32 v185, v81, v79, v68\n"
    "s_waitcnt lgkmcnt(8)\n"
    "v_perm_b32 v155, v84, v82, v68\n"
    "v_perm_b32 v187, v85, v83, v68\n"
    "s_waitcnt lgkmcnt(6)\n"
    "v_perm_b32 v216, v88, v86, v68\n"
    "v_perm_b32 v248, v89, v87, v68\n"
    "s_waitcnt lgkmcnt(4)\n"
    "v_perm_b32 v218, v92, v90, v68\n"
    "v_perm_b32 v250, v93, v91, v68\n"
    "s_waitcnt lgkmcnt(2)\n"
    "v_perm_b32 v217, v96, v94, v68\n"
    "v_perm_b32 v249, v97, v95, v68\n"
    "s_waitcnt lgkmcnt(0)\n"
    "v_perm_b32 v219, v100, v98, v68\n"
    "v_perm_b32 v251, v101, v99, v68\n"
    "s_waitcnt vmcnt(24) lgkmcnt(0)\n"
    "s_barrier\n"
    "s_mov_b32 m0, 102400\n"
    "s_add_u32 s80, s60, 3145728\n"
    "buffer_load_dwordx4 v20, s[76:79], s80 offen offset:0  lds\n"
    "s_add_u32 s80, s60, 3177472\n"
    "buffer_load_dwordx4 v20, s[76:79], s80 offen offset:1024  lds\n"
    "s_add_u32 s80, s60, 3209216\n"
    "buffer_load_dwordx4 v21, s[76:79], s80 offen offset:2048  lds\n"
    "s_add_u32 s80, s60, 3240960\n"
    "buffer_load_dwordx4 v21, s[76:79], s80 offen offset:3072  lds\n"
    "ds_read_b64 v[70:71], v26 offset:53248\n"
    "ds_read_b64 v[72:73], v26 offset:53504\n"
    "ds_read_b64 v[74:75], v26 offset:53760\n"
    "ds_read_b64 v[76:77], v26 offset:54016\n"
    "ds_read_b64 v[78:79], v26 offset:53312\n"
    "ds_read_b64 v[80:81], v26 offset:53568\n"
    "ds_read_b64 v[82:83], v26 offset:53824\n"
    "ds_read_b64 v[84:85], v26 offset:54080\n"
    "ds_read_b64 v[86:87], v27 offset:53248\n"
    "ds_read_b64 v[88:89], v27 offset:53504\n"
    "ds_read_b64 v[90:91], v27 offset:53760\n"
    "ds_read_b64 v[92:93], v27 offset:54016\n"
    "ds_read_b64 v[94:95], v27 offset:53312\n"
    "ds_read_b64 v[96:97], v27 offset:53568\n"
    "ds_read_b64 v[98:99], v27 offset:53824\n"
    "ds_read_b64 v[100:101], v27 offset:54080\n"
    "s_waitcnt lgkmcnt(14)\n"
    "v_perm_b32 v156, v72, v70, v68\n"
    "v_perm_b32 v188, v73, v71, v68\n"
    "s_waitcnt lgkmcnt(12)\n"
    "v_perm_b32 v158, v76, v74, v68\n"
    "v_perm_b32 v190, v77, v75, v68\n"
    "s_waitcnt lgkmcnt(10)\n"
    "v_perm_b32 v157, v80, v78, v68\n"
    "v_perm_b32 v189, v81, v79, v68\n"
    "s_waitcnt lgkmcnt(8)\n"
    "v_perm_b32 v159, v84, v82, v68\n"
    "v_perm_b32 v191, v85, v83, v68\n"
    "s_waitcnt lgkmcnt(6)\n"
    "v_perm_b32 v220, v88, v86, v68\n"
    "v_perm_b32 v252, v89, v87, v68\n"
    "s_waitcnt lgkmcnt(4)\n"
    "v_perm_b32 v222, v92, v90, v68\n"
    "v_perm_b32 v254, v93, v91, v68\n"
    "s_waitcnt lgkmcnt(2)\n"
    "v_perm_b32 v221, v96, v94, v68\n"
    "v_perm_b32 v253, v97, v95, v68\n"
    "s_waitcnt lgkmcnt(0)\n"
    "v_perm_b32 v223, v100, v98, v68\n"
    "v_perm_b32 v255, v101, v99, v68\n"
    "s_waitcnt vmcnt(24) lgkmcnt(0)\n"
    "s_barrier\n"
    "s_mov_b32 m0, 118784\n"
    "s_add_u32 s80, s60, 3670016\n"
    "buffer_load_dwordx4 v20, s[76:79], s80 offen offset:0  lds\n"
    "s_add_u32 s80, s60, 3701760\n"
    "buffer_load_dwordx4 v20, s[76:79], s80 offen offset:1024  lds\n"
    "s_add_u32 s80, s60, 3733504\n"
    "buffer_load_dwordx4 v21, s[76:79], s80 offen offset:2048  lds\n"
    "s_add_u32 s80, s60, 3765248\n"
    "buffer_load_dwordx4 v21, s[76:79], s80 offen offset:3072  lds\n"
    "buffer_load_dwordx2 v[4:5], off, s[20:23], s63 sc1\n"
    "v_add_u32 v22, 4294901760, v22\n"
    "v_add_u32 v23, 4294901760, v23\n"
    "v_add_u32 v26, 4294901760, v26\n"
    "v_add_u32 v27, 4294901760, v27\n"
    "v_add_u32 v28, 4294901760, v28\n"
    "v_add_u32 v29, 4294901760, v29\n"
    "ds_read_b64_tr_b16 a[48:49], v22 offset:0\n"
    "ds_read_b64_tr_b16 a[112:113], v23 offset:0\n"
    "ds_read_b64_tr_b16 a[50:51], v22 offset:1024\n"
    "ds_read_b64_tr_b16 a[114:115], v23 offset:1024\n"
    "ds_read_b64_tr_b16 a[52:53], v22 offset:8192\n"
    "ds_read_b64_tr_b16 a[116:117], v23 offset:8192\n"
    "ds_read_b64_tr_b16 a[54:55], v22 offset:9216\n"
    "ds_read_b64_tr_b16 a[118:119], v23 offset:9216\n"
    "s_waitcnt vmcnt(25) lgkmcnt(0)\n"
    "s_barrier\n"
    "s_mov_b64 s[76:77], s[8:9]\n"
    "s_and_b32 s77, s77, 0xffff\n"
    "s_mov_b32 s78, 4194304\n"
    "s_mov_b32 s79, 0x20000\n"
    "s_mov_b32 m0, 4096\n"
    "s_add_u32 s80, s60, 0\n"
    "buffer_load_dwordx4 v20, s[76:79], s80 offen offset:0  lds\n"
    "s_add_u32 s80, s60, 31744\n"
    "buffer_load_dwordx4 v20, s[76:79], s80 offen offset:1024  lds\n"
    "s_add_u32 s80, s60, 63488\n"
    "buffer_load_dwordx4 v21, s[76:79], s80 offen offset:2048  lds\n"
    "s_add_u32 s80, s60, 95232\n"
    "buffer_load_dwordx4 v21, s[76:79], s80 offen offset:3072  lds\n"
    "ds_read_b64_tr_b16 a[56:57], v22 offset:16384\n"
    "ds_read_b64_tr_b16 a[120:121], v23 offset:16384\n"
    "ds_read_b64_tr_b16 a[58:59], v22 offset:17408\n"
    "ds_read_b64_tr_b16 a[122:123], v23 offset:17408\n"
    "ds_read_b64_tr_b16 a[60:61], v22 offset:24576\n"
    "ds_read_b64_tr_b16 a[124:125], v23 offset:24576\n"
    "ds_read_b64_tr_b16 a[62:63], v22 offset:25600\n"
    "ds_read_b64_tr_b16 a[126:127], v23 offset:25600\n"
    "s_waitcnt vmcnt(25) lgkmcnt(0)\n"
    "s_barrier\n"
    "s_mov_b32 m0, 20480\n"
    "s_add_u32 s80, s60, 524288\n"
    "buffer_load_dwordx4 v20, s[76:79], s80 offen offset:0  lds\n"
    "s_add_u32 s80, s60, 556032\n"
    "buffer_load_dwordx4 v20, s[76:79], s80 offen offset:1024  lds\n"
    "s_add_u32 s80, s60, 587776\n"
    "buffer_load_dwordx4 v21, s[76:79], s80 offen offset:2048  lds\n"
    "s_add_u32 s80, s60, 619520\n"
    "buffer_load_dwordx4 v21, s[76:79], s80 offen offset:3072  lds\n"
    "ds_read_b64_tr_b16 a[0:1], v22 offset:32768\n"
    "ds_read_b64_tr_b16 a[64:65], v23 offset:32768\n"
    "ds_read_b64_tr_b16 a[2:3], v22 offset:33792\n"
    "ds_read_b64_tr_b16 a[66:67], v23 offset:33792\n"
    "ds_read_b64_tr_b16 a[4:5], v22 offset:40960\n"
    "ds_read_b64_tr_b16 a[68:69], v23 offset:40960\n"
    "ds_read_b64_tr_b16 a[6:7], v22 offset:41984\n"
    "ds_read_b64_tr_b16 a[70:71], v23 offset:41984\n"
    "s_waitcnt vmcnt(25) lgkmcnt(0)\n"
    "s_barrier\n"
    "s_mov_b32 m0, 36864\n"
    "s_add_u32 s80, s60, 1048576\n"
    "buffer_load_dwordx4 v20, s[76:79], s80 offen offset:0  lds\n"
    "s_add_u32 s80, s60, 1080320\n"
    "buffer_load_dwordx4 v20, s[76:79], s80 offen offset:1024  lds\n"
    "s_add_u32 s80, s60, 1112064\n"
    "buffer_load_dwordx4 v21, s[76:79], s80 offen offset:2048  lds\n"
    "s_add_u32 s80, s60, 1143808\n"
    "buffer_load_dwordx4 v21, s[76:79], s80 offen offset:3072  lds\n"
    "ds_read_b64_tr_b16 a[8:9], v22 offset:49152\n"
    "ds_read_b64_tr_b16 a[72:73], v23 offset:49152\n"
    "ds_read_b64_tr_b16 a[10:11], v22 offset:50176\n"
    "ds_read_b64_tr_b16 a[74:75], v23 offset:50176\n"
    "ds_read_b64_tr_b16 a[12:13], v22 offset:57344\n"
    "ds_read_b64_tr_b16 a[76:77], v23 offset:57344\n"
    "ds_read_b64_tr_b16 a[14:15], v22 offset:58368\n"
    "ds_read_b64_tr_b16 a[78:79], v23 offset:58368\n"
    "s_waitcnt vmcnt(25) lgkmcnt(0)\n"
    "s_barrier\n"
    "s_mov_b32 m0, 53248\n"
    "s_add_u32 s80, s60, 1572864\n"
    "buffer_load_dwordx4 v20, s[76:79], s80 offen offset:0  lds\n"
    "s_add_u32 s80, s60, 1604608\n"
    "buffer_load_dwordx4 v20, s[76:79], s80 offen offset:1024  lds\n"
    "s_add_u32 s80, s60, 1636352\n"
    "buffer_load_dwordx4 v21, s[76:79], s80 offen offset:2048  lds\n"
    "s_add_u32 s80, s60, 1668096\n"
    "buffer_load_dwordx4 v21, s[76:79], s80 offen offset:3072  lds\n"
    "s_getpc_b64 s[94:95]\n"
    "RCA12_1_%=:\n"
    "v_lshlrev_b32 v10, 7, v0\n"
    "v_min_u32 v10, .Lcode_end-RCA12_1_%=-4, v10\n"
    "global_load_dword v10, v10, s[94:95]\n"
    "v_add_u32 v22, 65536, v22\n"
    "v_add_u32 v23, 65536, v23\n"
    "v_add_u32 v26, 65536, v26\n"
    "v_add_u32 v27, 65536, v27\n"
    "v_add_u32 v28, 65536, v28\n"
    "v_add_u32 v29, 65536, v29\n"
    "ds_read_b64_tr_b16 a[16:17], v22 offset:0\n"
    "ds_read_b64_tr_b16 a[80:81], v23 offset:0\n"
    "ds_read_b64_tr_b16 a[18:19], v22 offset:1024\n"
    "ds_read_b64_tr_b16 a[82:83], v23 offset:1024\n"
    "ds_read_b64_tr_b16 a[20:21], v22 offset:8192\n"
    "ds_read_b64_tr_b16 a[84:85], v23 offset:8192\n"
    "ds_read_b64_tr_b16 a[22:23], v22 offset:9216\n"
    "ds_read_b64_tr_b16 a[86:87], v23 offset:9216\n"
    "s_waitcnt vmcnt(26) lgkmcnt(0)\n"
    "s_barrier\n"
    "s_mov_b32 m0, 69632\n"
    "s_add_u32 s80, s60, 2097152\n"
    "buffer_load_dwordx4 v20, s[76:79], s80 offen offset:0  lds\n"
    "s_add_u32 s80, s60, 2128896\n"
    "buffer_load_dwordx4 v20, s[76:79], s80 offen offset:1024  lds\n"
    "s_add_u32 s80, s60, 2160640\n"
    "buffer_load_dwordx4 v21, s[76:79], s80 offen offset:2048  lds\n"
    "s_add_u32 s80, s60, 2192384\n"
    "buffer_load_dwordx4 v21, s[76:79], s80 offen offset:3072  lds\n"
    "ds_read_b64_tr_b16 a[24:25], v22 offset:16384\n"
    "ds_read_b64_tr_b16 a[88:89], v23 offset:16384\n"
    "ds_read_b64_tr_b16 a[26:27], v22 offset:17408\n"
    "ds_read_b64_tr_b16 a[90:91], v23 offset:17408\n"
    "ds_read_b64_tr_b16 a[28:29], v22 offset:24576\n"
    "ds_read_b64_tr_b16 a[92:93], v23 offset:24576\n"
    "ds_read_b64_tr_b16 a[30:31], v22 offset:25600\n"
    "ds_read_b64_tr_b16 a[94:95], v23 offset:25600\n"
    "s_waitcnt vmcnt(26) lgkmcnt(0)\n"
    "s_barrier\n"
    "s_mov_b32 m0, 86016\n"
    "s_add_u32 s80, s60, 2621440\n"
    "buffer_load_dwordx4 v20, s[76:79], s80 offen offset:0  lds\n"
    "s_add_u32 s80, s60, 2653184\n"
    "buffer_load_dwordx4 v20, s[76:79], s80 offen offset:1024  lds\n"
    "s_add_u32 s80, s60, 2684928\n"
    "buffer_load_dwordx4 v21, s[76:79], s80 offen offset:2048  lds\n"
    "s_add_u32 s80, s60, 2716672\n"
    "buffer_load_dwordx4 v21, s[76:79], s80 offen offset:3072  lds\n"
    "ds_read_b64_tr_b16 a[32:33], v22 offset:32768\n"
    "ds_read_b64_tr_b16 a[96:97], v23 offset:32768\n"
    "ds_read_b64_tr_b16 a[34:35], v22 offset:33792\n"
    "ds_read_b64_tr_b16 a[98:99], v23 offset:33792\n"
    "ds_read_b64_tr_b16 a[36:37], v22 offset:40960\n"
    "ds_read_b64_tr_b16 a[100:101], v23 offset:40960\n"
    "ds_read_b64_tr_b16 a[38:39], v22 offset:41984\n"
    "ds_read_b64_tr_b16 a[102:103], v23 offset:41984\n"
    "s_waitcnt vmcnt(26) lgkmcnt(0)\n"
    "s_barrier\n"
    "s_mov_b32 m0, 102400\n"
    "s_add_u32 s80, s60, 3145728\n"
    "buffer_load_dwordx4 v20, s[76:79], s80 offen offset:0  lds\n"
    "s_add_u32 s80, s60, 3177472\n"
    "buffer_load_dwordx4 v20, s[76:79], s80 offen offset:1024  lds\n"
    "s_add_u32 s80, s60, 3209216\n"
    "buffer_load_dwordx4 v21, s[76:79], s80 offen offset:2048  lds\n"
    "s_add_u32 s80, s60, 3240960\n"
    "buffer_load_dwordx4 v21, s[76:79], s80 offen offset:3072  lds\n"
    "ds_read_b64_tr_b16 a[40:41], v22 offset:49152\n"
    "ds_read_b64_tr_b16 a[104:105], v23 offset:49152\n"
    "ds_read_b64_tr_b16 a[42:43], v22 offset:50176\n"
    "ds_read_b64_tr_b16 a[106:107], v23 offset:50176\n"
    "ds_read_b64_tr_b16 a[44:45], v22 offset:57344\n"
    "ds_read_b64_tr_b16 a[108:109], v23 offset:57344\n"
    "ds_read_b64_tr_b16 a[46:47], v22 offset:58368\n"
    "ds_read_b64_tr_b16 a[110:111], v23 offset:58368\n"
    "s_waitcnt vmcnt(25) lgkmcnt(0)\n"
    "s_barrier\n"
    "s_mov_b32 m0, 118784\n"
    "s_add_u32 s80, s60, 3670016\n"
    "buffer_load_dwordx4 v20, s[76:79], s80 offen offset:0  lds\n"
    "s_add_u32 s80, s60, 3701760\n"
    "buffer_load_dwordx4 v20, s[76:79], s80 offen offset:1024  lds\n"
    "s_add_u32 s80, s60, 3733504\n"
    "buffer_load_dwordx4 v21, s[76:79], s80 offen offset:2048  lds\n"
    "s_add_u32 s80, s60, 3765248\n"
    "buffer_load_dwordx4 v21, s[76:79], s80 offen offset:3072  lds\n"
    "v_readfirstlane_b32 s46, v67\n"
    "v_readfirstlane_b32 s68, v5\n"
    "s_cmp_lg_u32 s24, 0\n"
    "s_cselect_b32 s46, %2, s46\n"
    "s_cmp_lg_u32 s46, s68\n"
    "s_cselect_b32 s68, -1, 0\n"
    "buffer_load_dwordx2 v[4:5], off, s[20:23], s63 sc1\n"
    "v_lshrrev_b32 v6, 2, v0\n"
    "v_and_b32 v7, 3, v0\n"
    "v_lshl_add_u32 v6, v6, 3, v7\n"
    "v_lshlrev_b32 v6, 7, v6\n"
    "s_mov_b32 s46, 81793536\n"
    "buffer_load_dword v7, v6, s[20:23], s46 offen sc1\n"
    "s_mov_b32 s46, 81809920\n"
    "buffer_load_dword v8, v6, s[20:23], s46 offen sc1\n"
    "v_add_u32 v22, 4294901760, v22\n"
    "v_add_u32 v23, 4294901760, v23\n"
    "v_add_u32 v26, 4294901760, v26\n"
    "v_add_u32 v27, 4294901760, v27\n"
    "v_add_u32 v28, 4294901760, v28\n"
    "v_add_u32 v29, 4294901760, v29\n"
    "ds_read_b64_tr_b16 a[192:193], v22 offset:0\n"
    "ds_read_b64_tr_b16 a[240:241], v23 offset:0\n"
    "ds_read_b64_tr_b16 a[194:195], v22 offset:1024\n"
    "ds_read_b64_tr_b16 a[242:243], v23 offset:1024\n"
    "ds_read_b64_tr_b16 a[196:197], v22 offset:8192\n"
    "ds_read_b64_tr_b16 a[244:245], v23 offset:8192\n"
    "ds_read_b64_tr_b16 a[198:199], v22 offset:9216\n"
    "ds_read_b64_tr_b16 a[246:247], v23 offset:9216\n"
    "s_waitcnt vmcnt(28) lgkmcnt(0)\n"
    "s_barrier\n"
    "s_mov_b64 s[76:77], s[6:7]\n"
    "s_and_b32 s77, s77, 0xffff\n"
    "s_mov_b32 s78, 4194304\n"
    "s_mov_b32 s79, 0x20000\n"
    "s_mov_b32 m0, 4096\n"
    "s_add_u32 s80, s60, 0\n"
    "buffer_load_dwordx4 v20, s[76:79], s80 offen offset:0  lds\n"
    "s_add_u32 s80, s60, 31744\n"
    "buffer_load_dwordx4 v20, s[76:79], s80 offen offset:1024  lds\n"
    "s_add_u32 s80, s60, 63488\n"
    "buffer_load_dwordx4 v21, s[76:79], s80 offen offset:2048  lds\n"
    "s_add_u32 s80, s60, 95232\n"
    "buffer_load_dwordx4 v21, s[76:79], s80 offen offset:3072  lds\n"
    "ds_read_b64_tr_b16 a[200:201], v22 offset:16384\n"
    "ds_read_b64_tr_b16 a[248:249], v23 offset:16384\n"
    "ds_read_b64_tr_b16 a[202:203], v22 offset:17408\n"
    "ds_read_b64_tr_b16 a[250:251], v23 offset:17408\n"
    "ds_read_b64_tr_b16 a[204:205], v22 offset:24576\n"
    "ds_read_b64_tr_b16 a[252:253], v23 offset:24576\n"
    "ds_read_b64_tr_b16 a[206:207], v22 offset:25600\n"
    "ds_read_b64_tr_b16 a[254:255], v23 offset:25600\n"
    "s_waitcnt vmcnt(28) lgkmcnt(0)\n"
    "s_barrier\n"
    "s_mov_b32 m0, 20480\n"
    "s_add_u32 s80, s60, 524288\n"
    "buffer_load_dwordx4 v20, s[76:79], s80 offen offset:0  lds\n"
    "s_add_u32 s80, s60, 556032\n"
    "buffer_load_dwordx4 v20, s[76:79], s80 offen offset:1024  lds\n"
    "s_add_u32 s80, s60, 587776\n"
    "buffer_load_dwordx4 v21, s[76:79], s80 offen offset:2048  lds\n"
    "s_add_u32 s80, s60, 619520\n"
    "buffer_load_dwordx4 v21, s[76:79], s80 offen offset:3072  lds\n"
    "ds_read_b64_tr_b16 v[32:33], v22 offset:32768\n"
    "ds_read_b64_tr_b16 v[40:41], v23 offset:32768\n"
    "ds_read_b64_tr_b16 v[34:35], v22 offset:33792\n"
    "ds_read_b64_tr_b16 v[42:43], v23 offset:33792\n"
    "ds_read_b64_tr_b16 v[36:37], v22 offset:40960\n"
    "ds_read_b64_tr_b16 v[44:45], v23 offset:40960\n"
    "ds_read_b64_tr_b16 v[38:39], v22 offset:41984\n"
    "ds_read_b64_tr_b16 v[46:47], v23 offset:41984\n"
    "s_waitcnt vmcnt(28) lgkmcnt(0)\n"
    "s_barrier\n"
    "s_mov_b32 m0, 36864\n"
    "s_add_u32 s80, s60, 1048576\n"
    "buffer_load_dwordx4 v20, s[76:79], s80 offen offset:0  lds\n"
    "s_add_u32 s80, s60, 1080320\n"
    "buffer_load_dwordx4 v20, s[76:79], s80 offen offset:1024  lds\n"
    "s_add_u32 s80, s60, 1112064\n"
    "buffer_load_dwordx4 v21, s[76:79], s80 offen offset:2048  lds\n"
    "s_add_u32 s80, s60, 1143808\n"
    "buffer_load_dwordx4 v21, s[76:79], s80 offen offset:3072  lds\n"
    "ds_read_b64_tr_b16 v[48:49], v22 offset:49152\n"
    "ds_read_b64_tr_b16 v[56:57], v23 offset:49152\n"
    "ds_read_b64_tr_b16 v[50:51], v22 offset:50176\n"
    "ds_read_b64_tr_b16 v[58:59], v23 offset:50176\n"
    "ds_read_b64_tr_b16 v[52:53], v22 offset:57344\n"
    "ds_read_b64_tr_b16 v[60:61], v23 offset:57344\n"
    "ds_read_b64_tr_b16 v[54:55], v22 offset:58368\n"
    "ds_read_b64_tr_b16 v[62:63], v23 offset:58368\n"
    "s_waitcnt vmcnt(27) lgkmcnt(0)\n"
    "s_barrier\n"
    "s_mov_b32 m0, 53248\n"
    "s_add_u32 s80, s60, 1572864\n"
    "buffer_load_dwordx4 v20, s[76:79], s80 offen offset:0  lds\n"
    "s_add_u32 s80, s60, 1604608\n"
    "buffer_load_dwordx4 v20, s[76:79], s80 offen offset:1024  lds\n"
    "s_add_u32 s80, s60, 1636352\n"
    "buffer_load_dwordx4 v21, s[76:79], s80 offen offset:2048  lds\n"
    "s_add_u32 s80, s60, 1668096\n"
    "buffer_load_dwordx4 v21, s[76:79], s80 offen offset:3072  lds\n"
    "v_add_u32 v22, 65536, v22\n"
    "v_add_u32 v23, 65536, v23\n"
    "v_add_u32 v26, 65536, v26\n"
    "v_add_u32 v27, 65536, v27\n"
    "v_add_u32 v28, 65536, v28\n"
    "v_add_u32 v29, 65536, v29\n"
    "ds_read_b64_tr_b16 a[160:161], v22 offset:0\n"
    "ds_read_b64_tr_b16 a[208:209], v23 offset:0\n"
    "ds_read_b64_tr_b16 a[162:163], v22 offset:1024\n"
    "ds_read_b64_tr_b16 a[210:211], v23 offset:1024\n"
    "ds_read_b64_tr_b16 a[164:165], v22 offset:8192\n"
    "ds_read_b64_tr_b16 a[212:213], v23 offset:8192\n"
    "ds_read_b64_tr_b16 a[166:167], v22 offset:9216\n"
    "ds_read_b64_tr_b16 a[214:215], v23 offset:9216\n"
    "s_waitcnt vmcnt(27) lgkmcnt(0)\n"
    "s_barrier\n"
    "s_mov_b32 m0, 69632\n"
    "s_add_u32 s80, s60, 2097152\n"
    "buffer_load_dwordx4 v20, s[76:79], s80 offen offset:0  lds\n"
    "s_add_u32 s80, s60, 2128896\n"
    "buffer_load_dwordx4 v20, s[76:79], s80 offen offset:1024  lds\n"
    "s_add_u32 s80, s60, 2160640\n"
    "buffer_load_dwordx4 v21, s[76:79], s80 offen offset:2048  lds\n"
    "s_add_u32 s80, s60, 2192384\n"
    "buffer_load_dwordx4 v21, s[76:79], s80 offen offset:3072  lds\n"
    "ds_read_b64_tr_b16 a[168:169], v22 offset:16384\n"
    "ds_read_b64_tr_b16 a[216:217], v23 offset:16384\n"
    "ds_read_b64_tr_b16 a[170:171], v22 offset:17408\n"
    "ds_read_b64_tr_b16 a[218:219], v23 offset:17408\n"
    "ds_read_b64_tr_b16 a[172:173], v22 offset:24576\n"
    "ds_read_b64_tr_b16 a[220:221], v23 offset:24576\n"
    "ds_read_b64_tr_b16 a[174:175], v22 offset:25600\n"
    "ds_read_b64_tr_b16 a[222:223], v23 offset:25600\n"
    "s_waitcnt vmcnt(27) lgkmcnt(0)\n"
    "s_barrier\n"
    "s_mov_b32 m0, 86016\n"
    "s_add_u32 s80, s60, 2621440\n"
    "buffer_load_dwordx4 v20, s[76:79], s80 offen offset:0  lds\n"
    "s_add_u32 s80, s60, 2653184\n"
    "buffer_load_dwordx4 v20, s[76:79], s80 offen offset:1024  lds\n"
    "s_add_u32 s80, s60, 2684928\n"
    "buffer_load_dwordx4 v21, s[76:79], s80 offen offset:2048  lds\n"
    "s_add_u32 s80, s60, 2716672\n"
    "buffer_load_dwordx4 v21, s[76:79], s80 offen offset:3072  lds\n"
    "ds_read_b64_tr_b16 a[176:177], v22 offset:32768\n"
    "ds_read_b64_tr_b16 a[224:225], v23 offset:32768\n"
    "ds_read_b64_tr_b16 a[178:179], v22 offset:33792\n"
    "ds_read_b64_tr_b16 a[226:227], v23 offset:33792\n"
    "ds_read_b64_tr_b16 a[180:181], v22 offset:40960\n"
    "ds_read_b64_tr_b16 a[228:229], v23 offset:40960\n"
    "ds_read_b64_tr_b16 a[182:183], v22 offset:41984\n"
    "ds_read_b64_tr_b16 a[230:231], v23 offset:41984\n"
    "s_waitcnt vmcnt(27) lgkmcnt(0)\n"
    "s_barrier\n"
    "s_mov_b32 m0, 102400\n"
    "s_add_u32 s80, s60, 3145728\n"
    "buffer_load_dwordx4 v20, s[76:79], s80 offen offset:0  lds\n"
    "s_add_u32 s80, s60, 3177472\n"
    "buffer_load_dwordx4 v20, s[76:79], s80 offen offset:1024  lds\n"
    "s_add_u32 s80, s60, 3209216\n"
    "buffer_load_dwordx4 v21, s[76:79], s80 offen offset:2048  lds\n"
    "s_add_u32 s80, s60, 3240960\n"
    "buffer_load_dwordx4 v21, s[76:79], s80 offen offset:3072  lds\n"
    "ds_read_b64_tr_b16 a[184:185], v22 offset:49152\n"
    "ds_read_b64_tr_b16 a[232:233], v23 offset:49152\n"
    "ds_read_b64_tr_b16 a[186:187], v22 offset:50176\n"
    "ds_read_b64_tr_b16 a[234:235], v23 offset:50176\n"
    "ds_read_b64_tr_b16 a[188:189], v22 offset:57344\n"
    "ds_read_b64_tr_b16 a[236:237], v23 offset:57344\n"
    "ds_read_b64_tr_b16 a[190:191], v22 offset:58368\n"
    "ds_read_b64_tr_b16 a[238:239], v23 offset:58368\n"
    "s_waitcnt vmcnt(24) lgkmcnt(0)\n"
    "s_barrier\n"
    "s_mov_b32 m0, 118784\n"
    "s_add_u32 s80, s60, 3670016\n"
    "buffer_load_dwordx4 v20, s[76:79], s80 offen offset:0  lds\n"
    "s_add_u32 s80, s60, 3701760\n"
    "buffer_load_dwordx4 v20, s[76:79], s80 offen offset:1024  lds\n"
    "s_add_u32 s80, s60, 3733504\n"
    "buffer_load_dwordx4 v21, s[76:79], s80 offen offset:2048  lds\n"
    "s_add_u32 s80, s60, 3765248\n"
    "buffer_load_dwordx4 v21, s[76:79], s80 offen offset:3072  lds\n"
    "v_readfirstlane_b32 s62, v4\n"
    "s_bitcmp1_b32 s62, 1\n"
    "s_cselect_b32 s68, s68, 0\n"
    "s_xor_b32 s62, s62, s24\n"
    "s_and_b32 s62, s62, 1\n"
    "s_lshl_b32 s62, s62, 13\n"
    "v_min_u32 v7, v7, v8\n"
    "v_cmp_gt_u32 vcc, 1, v7\n"
    "s_cmp_eq_u64 vcc, 0\n"
    "s_cselect_b32 s68, s68, 0\n"
    "s_add_u32 s30, s62, 0\n"
    "s_add_u32 s31, s30, 4096\n"
    "s_lshl_b32 s47, s25, 8\n"
    "s_add_u32 s36, s62, s47\n"
    "s_mov_b32 s50, 0\n"
    "s_mov_b32 s28, s30\n"
    "s_add_u32 s32, s62, s47\n"
    "s_add_u32 s32, s32, 67239936\n"
    "s_add_u32 s33, s32, 65536\n"
    "s_lshl_b32 s47, s25, 14\n"
    "s_add_u32 s34, s62, s47\n"
    "s_add_u32 s34, s34, 67108864\n"
    "s_add_u32 s35, s34, 4096\n"
    "s_add_u32 s44, s34, 4196352\n"
    "s_lshl_b32 s39, s25, 15\n"
    "s_add_u32 s39, s39, s62\n"
    "s_add_u32 s39, s39, 16781312\n"
    "s_xor_b32 s69, s62, 0x2000\n"
    "s_lshr_b32 s41, s25, 3\n"
    "s_lshl_b32 s41, s41, 14\n"
    "s_and_b32 s47, s25, 7\n"
    "s_lshl_b32 s47, s47, 10\n"
    "s_add_u32 s41, s41, s47\n"
    "s_add_u32 s41, s41, s69\n"
    "s_add_u32 s41, s41, 3145856\n"
    "s_add_u32 s69, s69, 3309568\n"
    "s_mov_b32 s37, 0x7f000000\n"
    "s_mov_b32 s42, 0\n"
    "s_cmp_eq_u32 s25, 0\n"
    "s_cselect_b32 s37, s69, s37\n"
    "s_add_u32 s69, s69, 16384\n"
    "s_cmp_eq_u32 s25, 11\n"
    "s_cselect_b32 s37, s69, s37\n"
    "s_add_u32 s69, s69, 16384\n"
    "s_cmp_eq_u32 s25, 22\n"
    "s_cselect_b32 s37, s69, s37\n"
    "s_add_u32 s69, s69, 16384\n"
    "v_add_u32 v22, 4294901760, v22\n"
    "v_add_u32 v23, 4294901760, v23\n"
    "v_add_u32 v26, 4294901760, v26\n"
    "v_add_u32 v27, 4294901760, v27\n"
    "v_add_u32 v28, 4294901760, v28\n"
    "v_add_u32 v29, 4294901760, v29\n"
    "ds_read_b64_tr_b16 a[128:129], v22 offset:0\n"
    "ds_read_b64_tr_b16 a[144:145], v23 offset:0\n"
    "ds_read_b64_tr_b16 a[130:131], v22 offset:1024\n"
    "ds_read_b64_tr_b16 a[146:147], v23 offset:1024\n"
    "ds_read_b64_tr_b16 a[132:133], v22 offset:8192\n"
    "ds_read_b64_tr_b16 a[148:149], v23 offset:8192\n"
    "ds_read_b64_tr_b16 a[134:135], v22 offset:9216\n"
    "ds_read_b64_tr_b16 a[150:151], v23 offset:9216\n"
    "s_waitcnt vmcnt(24) lgkmcnt(0)\n"
    "s_barrier\n"
    "s_getpc_b64 s[94:95]\n"
    "RCA25_1_%=:\n"
    "v_lshlrev_b32 v11, 7, v0\n"
    "v_min_u32 v11, .Lcode_end-RCA25_1_%=-4, v11\n"
    "global_load_dword v11, v11, s[94:95]\n"
    "ds_read_b64_tr_b16 a[136:137], v22 offset:16384\n"
    "ds_read_b64_tr_b16 a[152:153], v23 offset:16384\n"
    "ds_read_b64_tr_b16 a[138:139], v22 offset:17408\n"
    "ds_read_b64_tr_b16 a[154:155], v23 offset:17408\n"
    "ds_read_b64_tr_b16 a[140:141], v22 offset:24576\n"
    "ds_read_b64_tr_b16 a[156:157], v23 offset:24576\n"
    "ds_read_b64_tr_b16 a[142:143], v22 offset:25600\n"
    "ds_read_b64_tr_b16 a[158:159], v23 offset:25600\n"
    "ds_write_b128 v107, v[32:35] offset:4096\n"
    "ds_write_b128 v107, v[36:39] offset:5120\n"
    "ds_write_b128 v107, v[40:43] offset:6144\n"
    "ds_write_b128 v107, v[44:47] offset:7168\n"
    "s_waitcnt vmcnt(21) lgkmcnt(0)\n"
    "s_barrier\n"
    "ds_read_b64_tr_b16 v[70:71], v22 offset:32768\n"
    "ds_read_b64_tr_b16 v[78:79], v23 offset:32768\n"
    "ds_read_b64_tr_b16 v[72:73], v22 offset:33792\n"
    "ds_read_b64_tr_b16 v[80:81], v23 offset:33792\n"
    "ds_read_b64_tr_b16 v[74:75], v22 offset:40960\n"
    "ds_read_b64_tr_b16 v[82:83], v23 offset:40960\n"
    "ds_read_b64_tr_b16 v[76:77], v22 offset:41984\n"
    "ds_read_b64_tr_b16 v[84:85], v23 offset:41984\n"
    "ds_write_b128 v107, v[48:51] offset:20480\n"
    "ds_write_b128 v107, v[52:55] offset:21504\n"
    "ds_write_b128 v107, v[56:59] offset:22528\n"
    "ds_write_b128 v107, v[60:63] offset:23552\n"
    "s_waitcnt vmcnt(17) lgkmcnt(0)\n"
    "s_barrier\n"
    "ds_read_b64_tr_b16 v[86:87], v22 offset:49152\n"
    "ds_read_b64_tr_b16 v[94:95], v23 offset:49152\n"
    "ds_read_b64_tr_b16 v[88:89], v22 offset:50176\n"
    "ds_read_b64_tr_b16 v[96:97], v23 offset:50176\n"
    "ds_read_b64_tr_b16 v[90:91], v22 offset:57344\n"
    "ds_read_b64_tr_b16 v[98:99], v23 offset:57344\n"
    "ds_read_b64_tr_b16 v[92:93], v22 offset:58368\n"
    "ds_read_b64_tr_b16 v[100:101], v23 offset:58368\n"
    "ds_write_b128 v107, v[70:73] offset:36864\n"
    "ds_write_b128 v107, v[74:77] offset:37888\n"
    "ds_write_b128 v107, v[78:81] offset:38912\n"
    "ds_write_b128 v107, v[82:85] offset:39936\n"
    "s_waitcnt vmcnt(13) lgkmcnt(0)\n"
    "s_barrier\n"
    "v_add_u32 v22, 65536, v22\n"
    "v_add_u32 v23, 65536, v23\n"
    "v_add_u32 v26, 65536, v26\n"
    "v_add_u32 v27, 65536, v27\n"
    "v_add_u32 v28, 65536, v28\n"
    "v_add_u32 v29, 65536, v29\n"
    "ds_read_b64_tr_b16 v[70:71], v22 offset:0\n"
    "ds_read_b64_tr_b16 v[78:79], v23 offset:0\n"
    "ds_read_b64_tr_b16 v[72:73], v22 offset:1024\n"
    "ds_read_b64_tr_b16 v[80:81], v23 offset:1024\n"
    "ds_read_b64_tr_b16 v[74:75], v22 offset:8192\n"
    "ds_read_b64_tr_b16 v[82:83], v23 offset:8192\n"
    "ds_read_b64_tr_b16 v[76:77], v22 offset:9216\n"
    "ds_read_b64_tr_b16 v[84:85], v23 offset:9216\n"
    "ds_write_b128 v107, v[86:89] offset:53248\n"
    "ds_write_b128 v107, v[90:93] offset:54272\n"
    "ds_write_b128 v107, v[94:97] offset:55296\n"
    "ds_write_b128 v107, v[98:101] offset:56320\n"
    "s_waitcnt vmcnt(9) lgkmcnt(0)\n"
    "s_barrier\n"
    "v_lshrrev_b32 v6, 2, v0\n"
    "v_and_b32 v7, 3, v0\n"
    "v_lshl_add_u32 v6, v6, 3, v7\n"
    "v_lshlrev_b32 v6, 7, v6\n"
    "s_mov_b32 s46, 81793536\n"
    "buffer_load_dword v7, v6, s[20:23], s46 offen sc1\n"
    "s_mov_b32 s46, 81809920\n"
    "buffer_load_dword v8, v6, s[20:23], s46 offen sc1\n"
    "buffer_load_dword v115, off, s[20:23], s41 sc1\n"
    "ds_read_b64_tr_b16 v[86:87], v22 offset:16384\n"
    "ds_read_b64_tr_b16 v[94:95], v23 offset:16384\n"
    "ds_read_b64_tr_b16 v[88:89], v22 offset:17408\n"
    "ds_read_b64_tr_b16 v[96:97], v23 offset:17408\n"
    "ds_read_b64_tr_b16 v[90:91], v22 offset:24576\n"
    "ds_read_b64_tr_b16 v[98:99], v23 offset:24576\n"
    "ds_read_b64_tr_b16 v[92:93], v22 offset:25600\n"
    "ds_read_b64_tr_b16 v[100:101], v23 offset:25600\n"
    "ds_write_b128 v108, v[70:73] offset:4096\n"
    "ds_write_b128 v108, v[74:77] offset:5120\n"
    "ds_write_b128 v108, v[78:81] offset:6144\n"
    "ds_write_b128 v108, v[82:85] offset:7168\n"
    "s_waitcnt vmcnt(8) lgkmcnt(0)\n"
    "s_barrier\n"
    "ds_read_b64_tr_b16 v[70:71], v22 offset:32768\n"
    "ds_read_b64_tr_b16 v[78:79], v23 offset:32768\n"
    "ds_read_b64_tr_b16 v[72:73], v22 offset:33792\n"
    "ds_read_b64_tr_b16 v[80:81], v23 offset:33792\n"
    "ds_read_b64_tr_b16 v[74:75], v22 offset:40960\n"
    "ds_read_b64_tr_b16 v[82:83], v23 offset:40960\n"
    "ds_read_b64_tr_b16 v[76:77], v22 offset:41984\n"
    "ds_read_b64_tr_b16 v[84:85], v23 offset:41984\n"
    "ds_write_b128 v108, v[86:89] offset:20480\n"
    "ds_write_b128 v108, v[90:93] offset:21504\n"
    "ds_write_b128 v108, v[94:97] offset:22528\n"
    "ds_write_b128 v108, v[98:101] offset:23552\n"
    "s_waitcnt vmcnt(4) lgkmcnt(0)\n"
    "s_barrier\n"
    "ds_read_b64_tr_b16 v[86:87], v22 offset:49152\n"
    "ds_read_b64_tr_b16 v[94:95], v23 offset:49152\n"
    "ds_read_b64_tr_b16 v[88:89], v22 offset:50176\n"
    "ds_read_b64_tr_b16 v[96:97], v23 offset:50176\n"
    "ds_read_b64_tr_b16 v[90:91], v22 offset:57344\n"
    "ds_read_b64_tr_b16 v[98:99], v23 offset:57344\n"
    "ds_read_b64_tr_b16 v[92:93], v22 offset:58368\n"
    "ds_read_b64_tr_b16 v[100:101], v23 offset:58368\n"
    "ds_write_b128 v108, v[70:73] offset:36864\n"
    "ds_write_b128 v108, v[74:77] offset:37888\n"
    "ds_write_b128 v108, v[78:81] offset:38912\n"
    "ds_write_b128 v108, v[82:85] offset:39936\n"
    "s_waitcnt lgkmcnt(0)\n"
    "s_barrier\n"
    "ds_write_b128 v108, v[86:89] offset:53248\n"
    "ds_write_b128 v108, v[90:93] offset:54272\n"
    "ds_write_b128 v108, v[94:97] offset:55296\n"
    "ds_write_b128 v108, v[98:101] offset:56320\n"
    "ds_write_b128 v109, v[16:19] offset:8192\n"
    "s_waitcnt vmcnt(1)\n"
    "v_min_u32 v7, v7, v8\n"
    "v_cmp_gt_u32 vcc, 2, v7\n"
    "s_cmp_eq_u64 vcc, 0\n"
    "s_cselect_b32 s68, s68, 0\n"
    "s_waitcnt vmcnt(0)\n"
    "s_cmp_lg_u32 s68, 0\n"
    "s_cbranch_scc1 RF01_%=\n"
    "v_readfirstlane_b32 s46, v67\n"
    "s_cmp_lg_u32 s24, 0\n"
    "s_cselect_b32 s46, %2, s46\n"
    "s_mov_b32 s45, 0\n"
    "RBW1_%=:\n"
    "buffer_load_dwordx2 v[66:67], off, s[20:23], s63 sc1\n"
    "s_add_u32 s45, s45, 1\n"
    "s_cmp_gt_u32 s45, 262144\n"
    "s_cbranch_scc1 REND_%=\n"
    "s_waitcnt vmcnt(0)\n"
    "v_readfirstlane_b32 s62, v66\n"
    "v_cmp_eq_u32 vcc, s46, v67\n"
    "s_cbranch_vccnz RBW1_%=\n"
    "s_bitcmp1_b32 s62, 1\n"
    "s_cbranch_scc0 RBW1_%=\n"
    "s_xor_b32 s62, s62, s24\n"
    "s_and_b32 s62, s62, 1\n"
    "s_lshl_b32 s62, s62, 13\n"
    "v_lshrrev_b32 v4, 2, v0\n"
    "v_and_b32 v5, 3, v0\n"
    "v_lshl_add_u32 v4, v4, 3, v5\n"
    "v_lshlrev_b32 v4, 7, v4\n"
    "s_mov_b32 s45, 0\n"
    "RIW1_%=:\n"
    "s_mov_b32 s46, 81793536\n"
    "buffer_load_dword v5, v4, s[20:23], s46 offen sc1\n"
    "s_mov_b32 s46, 81809920\n"
    "buffer_load_dword v6, v4, s[20:23], s46 offen sc1\n"
    "s_add_u32 s45, s45, 1\n"
    "s_cmp_gt_u32 s45, 262144\n"
    "s_cbranch_scc1 REND_%=\n"
    "s_waitcnt vmcnt(0)\n"
    "v_min_u32 v5, v5, v6\n"
    "v_cmp_gt_u32 vcc, 2, v5\n"
    "s_cbranch_vccnz RIW1_%=\n"
    "s_add_u32 s30, s62, 0\n"
    "s_add_u32 s31, s30, 4096\n"
    "s_lshl_b32 s47, s25, 8\n"
    "s_add_u32 s36, s62, s47\n"
    "s_mov_b32 s50, 0\n"
    "s_mov_b32 s28, s30\n"
    "s_add_u32 s32, s62, s47\n"
    "s_add_u32 s32, s32, 67239936\n"
    "s_add_u32 s33, s32, 65536\n"
    "s_lshl_b32 s47, s25, 14\n"
    "s_add_u32 s34, s62, s47\n"
    "s_add_u32 s34, s34, 67108864\n"
    "s_add_u32 s35, s34, 4096\n"
    "s_add_u32 s44, s34, 4196352\n"
    "s_lshl_b32 s39, s25, 15\n"
    "s_add_u32 s39, s39, s62\n"
    "s_add_u32 s39, s39, 16781312\n"
    "s_xor_b32 s69, s62, 0x2000\n"
    "s_lshr_b32 s41, s25, 3\n"
    "s_lshl_b32 s41, s41, 14\n"
    "s_and_b32 s47, s25, 7\n"
    "s_lshl_b32 s47, s47, 10\n"
    "s_add_u32 s41, s41, s47\n"
    "s_add_u32 s41, s41, s69\n"
    "s_add_u32 s41, s41, 3145856\n"
    "s_add_u32 s69, s69, 3309568\n"
    "s_mov_b32 s37, 0x7f000000\n"
    "s_mov_b32 s42, 0\n"
    "s_cmp_eq_u32 s25, 0\n"
    "s_cselect_b32 s37, s69, s37\n"
    "s_add_u32 s69, s69, 16384\n"
    "s_cmp_eq_u32 s25, 11\n"
    "s_cselect_b32 s37, s69, s37\n"
    "s_add_u32 s69, s69, 16384\n"
    "s_cmp_eq_u32 s25, 22\n"
    "s_cselect_b32 s37, s69, s37\n"
    "s_add_u32 s69, s69, 16384\n"
    "v_mov_b32 v115, 0\n"
    "RF01_%=:\n"
    "v_cmp_gt_u32 vcc, 1, v115\n"
    "s_cbranch_vccz RF31_%=\n"
    "s_mov_b32 s45, 0\n"
    "RPK1_%=:\n"
    "buffer_load_dword v115, off, s[20:23], s41 sc1\n"
    "s_add_u32 s45, s45, 1\n"
    "s_cmp_gt_u32 s45, 0x40000\n"
    "s_cbranch_scc1 REND_%=\n"
    "s_waitcnt vmcnt(0)\n"
    "v_cmp_gt_u32 vcc, 1, v115\n"
    "s_cbranch_vccnz RPK1_%=\n"
    "RF31_%=:\n"
    "buffer_load_dwordx4 v[90:93], v107, s[20:23], s39 offen offset:0 sc1\n"
    "buffer_load_dwordx4 v[94:97], v107, s[20:23], s39 offen offset:1024 sc1\n"
    "buffer_load_dwordx4 v[98:101], v107, s[20:23], s39 offen offset:2048 sc1\n"
    "buffer_load_dwordx4 v[102:105], v107, s[20:23], s39 offen offset:3072 sc1\n"
    "s_mov_b32 s45, 0\n"
    "s_memrealtime s[86:87]\n"
    "s_waitcnt lgkmcnt(0)\n"
    "ds_read_b128 v[32:35], v107 offset:36864\n"
    "ds_read_b128 v[36:39], v107 offset:37888\n"
    "ds_read_b128 v[40:43], v107 offset:38912\n"
    "ds_read_b128 v[44:47], v107 offset:39936\n"
    "ds_read_b128 v[48:51], v107 offset:53248\n"
    "ds_read_b128 v[52:55], v107 offset:54272\n"
    "ds_read_b128 v[56:59], v107 offset:55296\n"
    "ds_read_b128 v[60:63], v107 offset:56320\n"
    "v_mov_b64 v[66:67], 0\n"
    "v_mov_b64 v[68:69], 0\n"
    "v_mov_b64 v[70:71], 0\n"
    "v_mov_b64 v[72:73], 0\n"
    "v_mov_b64 v[74:75], 0\n"
    "v_mov_b64 v[76:77], 0\n"
    "v_mov_b64 v[78:79], 0\n"
    "v_mov_b64 v[80:81], 0\n"
    "v_mov_b64 v[82:83], 0\n"
    "v_mov_b64 v[84:85], 0\n"
    "v_mov_b64 v[86:87], 0\n"
    "v_mov_b64 v[88:89], 0\n"
    "s_waitcnt lgkmcnt(0)\n"
    ".p2align 6\n"
    "RLP1_%=:\n"
    "buffer_load_dwordx4 v[0:3], v107, s[20:23], s30 offen offset:2048 sc1\n"
    "s_memrealtime s[90:91]\n"
    "s_add_u32 s42, s42, 1\n"
    "s_add_u32 s43, s42, 1\n"
    "s_and_b32 s46, s42, 1\n"
    "s_lshl_b32 s46, s46, 9\n"
    "s_add_u32 s40, s41, s46\n"
    "s_and_b32 s46, s42, 7\n"
    "s_lshl_b32 s46, s46, 20\n"
    "s_add_u32 s38, s39, s46\n"
    "s_nop 4\n"
    "buffer_load_dwordx4 v[4:7], v107, s[20:23], s30 offen offset:3072 sc1\n"
    "s_lshr_b32 s46, s38, 8\n"
    "s_and_b32 s46, s46, 0x2000\n"
    "s_xor_b32 s38, s38, s46\n"
    "s_cmp_eq_u32 s42, 63\n"
    "s_cselect_b32 s43, 0, s43\n"
    "s_nop 8\n"
    "buffer_load_dwordx4 v[8:11], v107, s[20:23], s31 offen offset:0 sc1\n"
    "s_nop 13\n"
    "buffer_load_dwordx4 v[12:15], v107, s[20:23], s31 offen offset:1024 sc1\n"
    "s_nop 13\n"
    "buffer_load_dwordx4 v[16:19], v107, s[20:23], s31 offen offset:2048 sc1\n"
    "s_waitcnt lgkmcnt(0)\n"
    "s_nop 12\n"
    "buffer_load_dwordx4 v[20:23], v107, s[20:23], s31 offen offset:3072 sc1\n"
    "s_nop 13\n"
    "buffer_load_dwordx4 v[24:27], v107, s[20:23], s30 offen offset:0 sc1\n"
    "s_nop 13\n"
    "buffer_load_dwordx4 v[28:31], v107, s[20:23], s30 offen offset:1024 sc1\n"
    "RRB1_%=:\n"
    "s_waitcnt vmcnt(7)\n"
    "v_smfmac_f32_16x16x64_bf16 v[66:69], v[0:3], a[0:7], v106\n"
    "v_smfmac_f32_16x16x64_bf16 v[70:73], v[0:3], v[32:39], v106\n"
    "ds_read_b128 v[32:35], v108 offset:4096\n"
    "v_smfmac_f32_16x16x64_bf16 v[74:77], v[0:3], a[64:71], v106\n"
    "ds_read_b128 v[36:39], v108 offset:5120\n"
    "v_smfmac_f32_16x16x64_bf16 v[78:81], v[0:3], v[40:47], v106\n"
    "ds_read_b128 v[40:43], v108 offset:6144\n"
    "s_waitcnt vmcnt(6)\n"
    "v_smfmac_f32_16x16x64_bf16 v[66:69], v[4:7], a[8:15], v106\n"
    "ds_read_b128 v[44:47], v108 offset:7168\n"
    "v_smfmac_f32_16x16x64_bf16 v[70:73], v[4:7], v[48:55], v106\n"
    "ds_read_b128 v[48:51], v108 offset:20480\n"
    "v_max_u32 v116, v0, v2\n"
    "v_smfmac_f32_16x16x64_bf16 v[74:77], v[4:7], a[72:79], v106\n"
    "ds_read_b128 v[52:55], v108 offset:21504\n"
    "v_smfmac_f32_16x16x64_bf16 v[78:81], v[4:7], v[56:63], v106\n"
    "ds_read_b128 v[56:59], v108 offset:22528\n"
    "s_waitcnt vmcnt(5)\n"
    "v_smfmac_f32_16x16x64_bf16 v[66:69], v[8:11], a[16:23], v106\n"
    "ds_read_b128 v[60:63], v108 offset:23552\n"
    "s_waitcnt lgkmcnt(6)\n"
    "v_smfmac_f32_16x16x64_bf16 v[70:73], v[8:11], v[32:39], v106\n"
    "ds_read_b128 v[32:35], v108 offset:36864\n"
    "v_max3_u32 v116, v116, v4, v6\n"
    "v_smfmac_f32_16x16x64_bf16 v[74:77], v[8:11], a[80:87], v106\n"
    "ds_read_b128 v[36:39], v108 offset:37888\n"
    "s_waitcnt lgkmcnt(6)\n"
    "v_smfmac_f32_16x16x64_bf16 v[78:81], v[8:11], v[40:47], v106\n"
    "ds_read_b128 v[40:43], v108 offset:38912\n"
    "s_waitcnt vmcnt(4)\n"
    "v_smfmac_f32_16x16x64_bf16 v[66:69], v[12:15], a[24:31], v106\n"
    "ds_read_b128 v[44:47], v108 offset:39936\n"
    "s_waitcnt lgkmcnt(6)\n"
    "v_smfmac_f32_16x16x64_bf16 v[70:73], v[12:15], v[48:55], v106\n"
    "ds_read_b128 v[48:51], v108 offset:53248\n"
    "v_max3_u32 v116, v116, v8, v10\n"
    "v_smfmac_f32_16x16x64_bf16 v[74:77], v[12:15], a[88:95], v106\n"
    "ds_read_b128 v[52:55], v108 offset:54272\n"
    "s_waitcnt lgkmcnt(6)\n"
    "v_smfmac_f32_16x16x64_bf16 v[78:81], v[12:15], v[56:63], v106\n"
    "ds_read_b128 v[56:59], v108 offset:55296\n"
    "s_waitcnt vmcnt(3)\n"
    "v_smfmac_f32_16x16x64_bf16 v[66:69], v[16:19], a[32:39], v106\n"
    "ds_read_b128 v[60:63], v108 offset:56320\n"
    "s_waitcnt lgkmcnt(6)\n"
    "v_smfmac_f32_16x16x64_bf16 v[70:73], v[16:19], v[32:39], v106\n"
    "ds_read_b128 v[32:35], v107 offset:4096\n"
    "v_max3_u32 v116, v116, v12, v14\n"
    "v_smfmac_f32_16x16x64_bf16 v[74:77], v[16:19], a[96:103], v106\n"
    "ds_read_b128 v[36:39], v107 offset:5120\n"
    "s_waitcnt lgkmcnt(6)\n"
    "v_smfmac_f32_16x16x64_bf16 v[78:81], v[16:19], v[40:47], v106\n"
    "ds_read_b128 v[40:43], v107 offset:6144\n"
    "s_waitcnt vmcnt(2)\n"
    "v_smfmac_f32_16x16x64_bf16 v[66:69], v[20:23], a[40:47], v106\n"
    "ds_read_b128 v[44:47], v107 offset:7168\n"
    "s_waitcnt lgkmcnt(6)\n"
    "v_smfmac_f32_16x16x64_bf16 v[70:73], v[20:23], v[48:55], v106\n"
    "ds_read_b128 v[48:51], v107 offset:20480\n"
    "v_max3_u32 v116, v116, v16, v18\n"
    "v_smfmac_f32_16x16x64_bf16 v[74:77], v[20:23], a[104:111], v106\n"
    "ds_read_b128 v[52:55], v107 offset:21504\n"
    "s_waitcnt lgkmcnt(6)\n"
    "v_smfmac_f32_16x16x64_bf16 v[78:81], v[20:23], v[56:63], v106\n"
    "ds_read_b128 v[56:59], v107 offset:22528\n"
    "s_waitcnt vmcnt(1)\n"
    "v_smfmac_f32_16x16x64_bf16 v[66:69], v[24:27], a[48:55], v106\n"
    "ds_read_b128 v[60:63], v107 offset:23552\n"
    "v_smfmac_f32_16x16x64_bf16 v[70:73], v[24:27], a[128:135], v106\n"
    "v_max3_u32 v116, v116, v20, v22\n"
    "v_smfmac_f32_16x16x64_bf16 v[74:77], v[24:27], a[112:119], v106\n"
    "v_max3_u32 v116, v116, v24, v26\n"
    "v_smfmac_f32_16x16x64_bf16 v[78:81], v[24:27], a[144:151], v106\n"
    "s_waitcnt vmcnt(0)\n"
    "v_smfmac_f32_16x16x64_bf16 v[66:69], v[28:31], a[56:63], v106\n"
    "v_max3_u32 v116, v116, v28, v30\n"
    "v_cmp_eq_u32 vcc, -1, v116\n"
    "v_smfmac_f32_16x16x64_bf16 v[70:73], v[28:31], a[136:143], v106\n"
    "s_cbranch_vccnz RRT1_%=\n"
    "v_smfmac_f32_16x16x64_bf16 v[74:77], v[28:31], a[120:127], v106\n"
    "s_nop 1\n"
    "s_nop 0\n"
    "v_cvt_pk_bf16_f32 v69, v68, v69\n"
    "v_smfmac_f32_16x16x64_bf16 v[78:81], v[28:31], a[152:159], v106\n"
    "s_nop 0\n"
    "v_cvt_pk_bf16_f32 v68, v66, v67\n"
    "v_cvt_pk_bf16_f32 v70, v70, v71\n"
    "v_cvt_pk_bf16_f32 v71, v72, v73\n"
    "v_cvt_pk_bf16_f32 v77, v76, v77\n"
    "v_cvt_pk_bf16_f32 v76, v74, v75\n"
    "v_mfma_f32_16x16x32_bf16 v[68:71], v[68:71], v[120:123], 0\n"
    "s_nop 0\n"
    "v_cvt_pk_bf16_f32 v78, v78, v79\n"
    "v_cvt_pk_bf16_f32 v79, v80, v81\n"
    "s_nop 1\n"
    "v_mfma_f32_16x16x32_bf16 v[76:79], v[76:79], v[120:123], 0\n"
    "s_waitcnt lgkmcnt(6)\n"
    "v_smfmac_f32_16x16x64_bf16 v[82:85], v[0:3], v[32:39], v106\n"
    "v_cvt_pk_bf16_f32 v72, v68, v69\n"
    "v_cvt_pk_bf16_f32 v73, v70, v71\n"
    "s_waitcnt lgkmcnt(4)\n"
    "v_smfmac_f32_16x16x64_bf16 v[86:89], v[0:3], v[40:47], v106\n"
    "s_nop 1\n"
    "v_cvt_pk_bf16_f32 v74, v76, v77\n"
    "v_cvt_pk_bf16_f32 v75, v78, v79\n"
    "s_nop 1\n"
    "v_mfma_f32_16x16x32_bf16 v[66:69], v[90:93], v[72:75], 0\n"
    "v_mfma_f32_16x16x32_bf16 v[78:81], v[94:97], v[72:75], 0\n"
    "s_nop 0\n"
    "v_mfma_f32_16x16x32_bf16 v[72:75], v[72:75], v[72:75], 0\n"
    "s_nop 1\n"
    "s_waitcnt lgkmcnt(2)\n"
    "v_smfmac_f32_16x16x64_bf16 v[82:85], v[4:7], v[48:55], v106\n"
    "s_nop 1\n"
    "v_mov_b32_dpp v66, v78 row_ror:8 row_mask:0xf bank_mask:0xc\n"
    "v_mov_b32_dpp v67, v79 row_ror:8 row_mask:0xf bank_mask:0xc\n"
    "v_mov_b32_dpp v68, v80 row_ror:8 row_mask:0xf bank_mask:0xc\n"
    "v_mov_b32_dpp v69, v81 row_ror:8 row_mask:0xf bank_mask:0xc\n"
    "ds_write_b128 v109, v[66:69] offset:1024\n"
    "v_mov_b32_dpp v72, v74 row_ror:8 row_mask:0xf bank_mask:0xc\n"
    "v_mov_b32_dpp v73, v75 row_ror:8 row_mask:0xf bank_mask:0xc\n"
    "ds_write_b64 v117, v[72:73] offset:8\n"
    "s_waitcnt lgkmcnt(2)\n"
    "v_smfmac_f32_16x16x64_bf16 v[86:89], v[4:7], v[56:63], v106\n"
    "v_smfmac_f32_16x16x64_bf16 v[82:85], v[8:11], a[160:167], v106\n"
    "v_smfmac_f32_16x16x64_bf16 v[86:89], v[8:11], a[208:215], v106\n"
    "v_smfmac_f32_16x16x64_bf16 v[82:85], v[12:15], a[168:175], v106\n"
    "s_waitcnt lgkmcnt(0)\n"
    "s_barrier\n"
    "ds_read_b128 v[32:35], v109 offset:2048\n"
    "v_smfmac_f32_16x16x64_bf16 v[86:89], v[12:15], a[216:223], v106\n"
    "ds_read_b128 v[36:39], v109 offset:3072\n"
    "v_smfmac_f32_16x16x64_bf16 v[82:85], v[16:19], a[176:183], v106\n"
    "ds_read_b128 v[40:43], v109 offset:0\n"
    "v_smfmac_f32_16x16x64_bf16 v[86:89], v[16:19], a[224:231], v106\n"
    "ds_read_b128 v[44:47], v110 offset:0\n"
    "v_smfmac_f32_16x16x64_bf16 v[82:85], v[20:23], a[184:191], v106\n"
    "ds_read_b128 v[48:51], v110 offset:1024\n"
    "v_smfmac_f32_16x16x64_bf16 v[86:89], v[20:23], a[232:239], v106\n"
    "s_waitcnt lgkmcnt(2)\n"
    "v_pk_add_f32 v[66:67], v[66:67], v[32:33]\n"
    "v_pk_add_f32 v[68:69], v[68:69], v[34:35]\n"
    "v_pk_add_f32 v[66:67], v[66:67], v[36:37]\n"
    "v_pk_add_f32 v[68:69], v[68:69], v[38:39]\n"
    "v_pk_add_f32 v[66:67], v[66:67], v[40:41]\n"
    "v_pk_add_f32 v[68:69], v[68:69], v[42:43]\n"
    "s_waitcnt lgkmcnt(0)\n"
    "v_pk_add_f32 v[70:71], v[44:45], v[46:47]\n"
    "v_pk_add_f32 v[70:71], v[70:71], v[48:49]\n"
    "v_pk_add_f32 v[70:71], v[70:71], v[50:51]\n"
    "v_max3_f32 v72, v66, v67, v68\n"
    "v_max3_f32 v72, v72, v69, v70\n"
    "v_max_f32 v72, v72, v71\n"
    "v_smfmac_f32_16x16x64_bf16 v[82:85], v[24:27], a[192:199], v106\n"
    "s_nop 0\n"
    "v_max_f32_dpp v73, v72, v72 row_ror:8 row_mask:0xf bank_mask:0xf\n"
    "v_max_f32_dpp v72, v72, v72 row_ror:8 row_mask:0xf bank_mask:0xf\n"
    "v_smfmac_f32_16x16x64_bf16 v[86:89], v[24:27], a[240:247], v106\n"
    "s_nop 0\n"
    "v_permlane16_swap_b32 v72, v73\n"
    "v_max_f32 v74, v72, v73\n"
    "v_max_f32 v72, v72, v73\n"
    "v_smfmac_f32_16x16x64_bf16 v[82:85], v[28:31], a[200:207], v106\n"
    "s_nop 0\n"
    "v_permlane32_swap_b32 v72, v74\n"
    "v_max_f32 v72, v72, v74\n"
    "v_mul_f32 v72, s48, v72\n"
    "v_fma_f32 v66, v66, s48, -v72\n"
    "v_fma_f32 v67, v67, s48, -v72\n"
    "v_fma_f32 v68, v68, s48, -v72\n"
    "v_fma_f32 v69, v69, s48, -v72\n"
    "v_exp_f32 v66, v66\n"
    "v_exp_f32 v67, v67\n"
    "v_exp_f32 v68, v68\n"
    "v_cvt_pk_bf16_f32 v62, v66, v67\n"
    "v_exp_f32 v69, v69\n"
    "v_smfmac_f32_16x16x64_bf16 v[86:89], v[28:31], a[248:255], v106\n"
    "v_mov_b32_dpp v60, v62 row_ror:8 row_mask:0xf bank_mask:0xf\n"
    "v_cvt_pk_bf16_f32 v63, v68, v69\n"
    "v_fma_f32 v70, v70, s48, -v72\n"
    "v_fma_f32 v71, v71, s48, -v72\n"
    "v_mov_b32_dpp v61, v63 row_ror:8 row_mask:0xf bank_mask:0xf\n"
    "v_exp_f32 v70, v70\n"
    "v_exp_f32 v71, v71\n"
    "v_mfma_f32_16x16x32_bf16 v[36:39], v[98:101], v[60:63], 0\n"
    "v_cvt_pk_bf16_f32 v64, v70, v71\n"
    "v_cvt_pk_bf16_f32 v82, v82, v83\n"
    "v_mfma_f32_16x16x32_bf16 v[40:43], v[102:105], v[60:63], 0\n"
    "v_cvt_pk_bf16_f32 v83, v84, v85\n"
    "v_cvt_pk_bf16_f32 v84, v88, v89\n"
    "v_mfma_f32_16x16x32_bf16 v[32:35], v[124:127], v[62:65], 0\n"
    "v_mov_b32_dpp v65, v64 row_ror:8 row_mask:0xf bank_mask:0xf\n"
    "v_cvt_pk_bf16_f32 v85, v86, v87\n"
    "s_nop 0\n"
    "v_mfma_f32_16x16x16_bf16 v[36:39], v[82:83], v[64:65], v[36:39]\n"
    "s_nop 0\n"
    "s_nop 1\n"
    "v_mfma_f32_16x16x16_bf16 v[40:43], v[84:85], v[64:65], v[40:43]\n"
    "s_nop 0\n"
    "v_add_f32_dpp v32, v32, v32 row_ror:8 row_mask:0xf bank_mask:0xf\n"
    "v_rcp_f32 v32, v32\n"
    "s_nop 0\n"
    "v_pk_mul_f32 v[36:37], v[36:37], v[32:33] op_sel_hi:[1,0]\n"
    "v_pk_mul_f32 v[38:39], v[38:39], v[32:33] op_sel_hi:[1,0]\n"
    "s_mov_b64 exec, s[98:99]\n"
    "s_nop 0\n"
    "v_pk_mul_f32 v[36:37], v[40:41], v[32:33] op_sel_hi:[1,0]\n"
    "v_pk_mul_f32 v[38:39], v[42:43], v[32:33] op_sel_hi:[1,0]\n"
    "s_mov_b64 exec, -1\n"
    "v_cvt_pk_bf16_f32 v0, v36, v37\n"
    "v_cvt_pk_bf16_f32 v1, v38, v39\n"
    "ds_write_b64 v111, v[0:1] offset:512\n"
    "v_mov_b32_dpp v2, v0 row_ror:8 row_mask:0xf bank_mask:0xf\n"
    "v_mov_b32_dpp v3, v1 row_ror:8 row_mask:0xf bank_mask:0xf\n"
    "v_mov_b64 v[16:17], 0\n"
    "v_mov_b64 v[18:19], 0\n"
    "v_mov_b64 v[20:21], 0\n"
    "v_mov_b64 v[22:23], 0\n"
    "v_mov_b64 v[24:25], 0\n"
    "v_mov_b64 v[26:27], 0\n"
    "v_mov_b64 v[28:29], 0\n"
    "v_mov_b64 v[30:31], 0\n"
    "v_smfmac_f32_16x16x64_bf16 v[16:19], v[0:3], v[128:135], v106\n"
    "s_waitcnt lgkmcnt(0)\n"
    "s_barrier\n"
    "ds_read_b128 v[4:7], v112 offset:1024\n"
    "ds_read_b128 v[8:11], v112 offset:1536\n"
    "ds_read_b128 v[12:15], v112 offset:0\n"
    "v_smfmac_f32_16x16x64_bf16 v[20:23], v[0:3], v[160:167], v106\n"
    "v_smfmac_f32_16x16x64_bf16 v[24:27], v[0:3], v[192:199], v106\n"
    "v_smfmac_f32_16x16x64_bf16 v[28:31], v[0:3], v[224:231], v106\n"
    "s_waitcnt lgkmcnt(2)\n"
    "v_smfmac_f32_16x16x64_bf16 v[16:19], v[4:7], v[136:143], v106\n"
    "v_smfmac_f32_16x16x64_bf16 v[20:23], v[4:7], v[168:175], v106\n"
    "s_waitcnt lgkmcnt(1)\n"
    "v_smfmac_f32_16x16x64_bf16 v[16:19], v[8:11], v[144:151], v106\n"
    "v_smfmac_f32_16x16x64_bf16 v[20:23], v[8:11], v[176:183], v106\n"
    "buffer_load_dword v115, off, s[20:23], s40 sc1\n"
    "s_waitcnt lgkmcnt(0)\n"
    "v_smfmac_f32_16x16x64_bf16 v[16:19], v[12:15], v[152:159], v106\n"
    "s_nop 0\n"
    "v_smfmac_f32_16x16x64_bf16 v[20:23], v[12:15], v[184:191], v106\n"
    "s_nop 2\n"
    "v_smfmac_f32_16x16x64_bf16 v[24:27], v[4:7], v[200:207], v106\n"
    "s_nop 1\n"
    "v_cvt_pk_bf16_f32 v19, v18, v19\n"
    "v_smfmac_f32_16x16x64_bf16 v[24:27], v[8:11], v[208:215], v106\n"
    "v_cvt_pk_bf16_f32 v18, v16, v17\n"
    "v_smfmac_f32_16x16x64_bf16 v[24:27], v[12:15], v[216:223], v106\n"
    "v_or_b32 v18, 1, v18\n"
    "v_smfmac_f32_16x16x64_bf16 v[28:31], v[4:7], v[232:239], v106\n"
    "v_cvt_pk_bf16_f32 v20, v20, v21\n"
    "v_smfmac_f32_16x16x64_bf16 v[28:31], v[8:11], v[240:247], v106\n"
    "v_cvt_pk_bf16_f32 v21, v22, v23\n"
    "v_or_b32 v20, 1, v20\n"
    "buffer_store_dwordx4 v[18:21], v113, s[20:23], s32 offen sc1\n"
    "v_smfmac_f32_16x16x64_bf16 v[28:31], v[12:15], v[248:255], v106\n"
    "s_nop 1\n"
    "v_cvt_pk_bf16_f32 v27, v26, v27\n"
    "v_cvt_pk_bf16_f32 v26, v24, v25\n"
    "v_or_b32 v26, 1, v26\n"
    "s_nop 2\n"
    "v_cvt_pk_bf16_f32 v28, v28, v29\n"
    "v_cvt_pk_bf16_f32 v29, v30, v31\n"
    "v_or_b32 v28, 1, v28\n"
    "buffer_store_dwordx4 v[26:29], v113, s[20:23], s33 offen sc1\n"
    "s_mov_b32 s45, 0\n"
    "s_add_u32 s36, s36, 16384\n"
    "s_bitcmp1_b32 s32, 19\n"
    "s_cselect_b32 s46, 4194304, 0\n"
    "s_bitset1_b32 s46, 19\n"
    "s_xor_b32 s32, s32, s46\n"
    "s_add_u32 s33, s32, 65536\n"
    "ds_read_b128 v[32:35], v107 offset:36864\n"
    "ds_read_b128 v[36:39], v107 offset:37888\n"
    "ds_read_b128 v[40:43], v107 offset:38912\n"
    "ds_read_b128 v[44:47], v107 offset:39936\n"
    "ds_read_b128 v[48:51], v107 offset:53248\n"
    "ds_read_b128 v[52:55], v107 offset:54272\n"
    "ds_read_b128 v[56:59], v107 offset:55296\n"
    "ds_read_b128 v[60:63], v107 offset:56320\n"
    "v_mov_b64 v[66:67], 0\n"
    "v_mov_b64 v[68:69], 0\n"
    "v_mov_b64 v[70:71], 0\n"
    "v_mov_b64 v[72:73], 0\n"
    "v_mov_b64 v[74:75], 0\n"
    "v_mov_b64 v[76:77], 0\n"
    "v_mov_b64 v[78:79], 0\n"
    "v_mov_b64 v[80:81], 0\n"
    "v_mov_b64 v[82:83], 0\n"
    "v_mov_b64 v[84:85], 0\n"
    "v_mov_b64 v[86:87], 0\n"
    "v_mov_b64 v[88:89], 0\n"
    "s_waitcnt vmcnt(2)\n"
    "v_cmp_gt_u32 vcc, s43, v115\n"
    "s_cbranch_vccnz RKW1_%=\n"
    "RKB1_%=:\n"
    "buffer_load_dwordx4 v[90:93], v107, s[20:23], s38 offen offset:0 sc1\n"
    "buffer_load_dwordx4 v[94:97], v107, s[20:23], s38 offen offset:1024 sc1\n"
    "buffer_load_dwordx4 v[98:101], v107, s[20:23], s38 offen offset:2048 sc1\n"
    "buffer_load_dwordx4 v[102:105], v107, s[20:23], s38 offen offset:3072 sc1\n"
    "s_mov_b32 s45, 0\n"
    "s_add_u32 s37, s37, 128\n"
    "s_barrier\n"
    "ds_read_b64 v[118:119], v109 offset:9216\n"
    "s_waitcnt lgkmcnt(0)\n"
    "v_readfirstlane_b32 s30, v119\n"
    "s_mov_b64 exec, 1\n"
    "buffer_store_dword v118, off, s[20:23], s37 sc1\n"
    "s_mov_b64 exec, -1\n"
    "s_add_u32 s31, s30, 4096\n"
    "s_cmp_lt_u32 s42, 63\n"
    "s_barrier\n"
    "s_cbranch_scc1 RLP1_%=\n"
    "s_branch REND_%=\n"
    "RRT1_%=:\n"
    "s_add_u32 s85, s90, 52\n"
    "s_add_u32 s45, s45, 1\n"
    "s_cmp_gt_u32 s45, 0x40000\n"
    "s_cbranch_scc1 REND_%=\n"
    "s_mov_b32 s50, 56\n"
    "s_or_b32 s86, s30, 1048576\n"
    "s_or_b32 s87, s31, 1048576\n"
    "RT1_%=:\n"
    "s_memrealtime s[90:91]\n"
    "s_waitcnt lgkmcnt(0)\n"
    "s_sub_u32 s46, s90, s85\n"
    "s_cmp_lt_i32 s46, 0\n"
    "s_cbranch_scc1 RT1_%=\n"
    "buffer_load_dwordx4 v[0:3], v107, s[20:23], s86 offen offset:2048 sc1\n"
    "ds_read_b128 v[32:35], v107 offset:36864\n"
    "ds_read_b128 v[36:39], v107 offset:37888\n"
    "ds_read_b128 v[40:43], v107 offset:38912\n"
    "ds_read_b128 v[44:47], v107 offset:39936\n"
    "s_nop 7\n"
    "buffer_load_dwordx4 v[4:7], v107, s[20:23], s86 offen offset:3072 sc1\n"
    "ds_read_b128 v[48:51], v107 offset:53248\n"
    "ds_read_b128 v[52:55], v107 offset:54272\n"
    "ds_read_b128 v[56:59], v107 offset:55296\n"
    "ds_read_b128 v[60:63], v107 offset:56320\n"
    "s_nop 7\n"
    "buffer_load_dwordx4 v[8:11], v107, s[20:23], s87 offen offset:0 sc1\n"
    "v_mov_b64 v[66:67], 0\n"
    "v_mov_b64 v[68:69], 0\n"
    "v_mov_b64 v[70:71], 0\n"
    "v_mov_b64 v[72:73], 0\n"
    "s_nop 7\n"
    "buffer_load_dwordx4 v[12:15], v107, s[20:23], s87 offen offset:1024 sc1\n"
    "v_mov_b64 v[74:75], 0\n"
    "v_mov_b64 v[76:77], 0\n"
    "v_mov_b64 v[78:79], 0\n"
    "v_mov_b64 v[80:81], 0\n"
    "s_nop 7\n"
    "buffer_load_dwordx4 v[16:19], v107, s[20:23], s87 offen offset:2048 sc1\n"
    "s_nop 13\n"
    "buffer_load_dwordx4 v[20:23], v107, s[20:23], s87 offen offset:3072 sc1\n"
    "s_nop 13\n"
    "buffer_load_dwordx4 v[24:27], v107, s[20:23], s86 offen offset:0 sc1\n"
    "s_nop 13\n"
    "buffer_load_dwordx4 v[28:31], v107, s[20:23], s86 offen offset:1024 sc1\n"
    "s_waitcnt lgkmcnt(0)\n"
    "s_branch RRB1_%=\n"
    "RKW1_%=:\n"
    "s_mov_b32 s45, 0\n"
    "RK21_%=:\n"
    "buffer_load_dword v115, off, s[20:23], s40 sc1\n"
    "s_add_u32 s45, s45, 1\n"
    "s_cmp_gt_u32 s45, 0x40000\n"
    "s_cbranch_scc1 REND_%=\n"
    "s_waitcnt vmcnt(0)\n"
    "v_cmp_gt_u32 vcc, s43, v115\n"
    "s_cbranch_vccnz RK21_%=\n"
    "s_branch RKB1_%=\n"
    "RW2_%=:\n"
    "s_mov_b64 s[76:77], s[10:11]\n"
    "s_and_b32 s77, s77, 0xffff\n"
    "s_mov_b32 s78, 4194304\n"
    "s_mov_b32 s79, 0x20000\n"
    "s_mov_b32 m0, 8192\n"
    "s_add_u32 s80, s61, 65536\n"
    "buffer_load_dwordx4 v24, s[76:79], s80 offen offset:0  lds\n"
    "s_add_u32 s80, s61, 68608\n"
    "buffer_load_dwordx4 v24, s[76:79], s80 offen offset:1024  lds\n"
    "s_add_u32 s80, s61, 79872\n"
    "buffer_load_dwordx4 v25, s[76:79], s80 offen offset:2048  lds\n"
    "s_add_u32 s80, s61, 82944\n"
    "buffer_load_dwordx4 v25, s[76:79], s80 offen offset:3072  lds\n"
    "s_mov_b32 m0, 24576\n"
    "s_add_u32 s80, s61, 73728\n"
    "buffer_load_dwordx4 v24, s[76:79], s80 offen offset:0  lds\n"
    "s_add_u32 s80, s61, 76800\n"
    "buffer_load_dwordx4 v24, s[76:79], s80 offen offset:1024  lds\n"
    "s_add_u32 s80, s61, 88064\n"
    "buffer_load_dwordx4 v25, s[76:79], s80 offen offset:2048  lds\n"
    "s_add_u32 s80, s61, 91136\n"
    "buffer_load_dwordx4 v25, s[76:79], s80 offen offset:3072  lds\n"
    "s_mov_b32 m0, 40960\n"
    "s_add_u32 s80, s61, 98304\n"
    "buffer_load_dwordx4 v24, s[76:79], s80 offen offset:0  lds\n"
    "s_add_u32 s80, s61, 101376\n"
    "buffer_load_dwordx4 v24, s[76:79], s80 offen offset:1024  lds\n"
    "s_add_u32 s80, s61, 112640\n"
    "buffer_load_dwordx4 v25, s[76:79], s80 offen offset:2048  lds\n"
    "s_add_u32 s80, s61, 115712\n"
    "buffer_load_dwordx4 v25, s[76:79], s80 offen offset:3072  lds\n"
    "s_mov_b32 m0, 57344\n"
    "s_add_u32 s80, s61, 106496\n"
    "buffer_load_dwordx4 v24, s[76:79], s80 offen offset:0  lds\n"
    "s_add_u32 s80, s61, 109568\n"
    "buffer_load_dwordx4 v24, s[76:79], s80 offen offset:1024  lds\n"
    "s_add_u32 s80, s61, 120832\n"
    "buffer_load_dwordx4 v25, s[76:79], s80 offen offset:2048  lds\n"
    "s_add_u32 s80, s61, 123904\n"
    "buffer_load_dwordx4 v25, s[76:79], s80 offen offset:3072  lds\n"
    "s_mov_b32 m0, 73728\n"
    "s_add_u32 s80, s61, 0\n"
    "buffer_load_dwordx4 v24, s[76:79], s80 offen offset:0  lds\n"
    "s_add_u32 s80, s61, 3072\n"
    "buffer_load_dwordx4 v24, s[76:79], s80 offen offset:1024  lds\n"
    "s_add_u32 s80, s61, 14336\n"
    "buffer_load_dwordx4 v25, s[76:79], s80 offen offset:2048  lds\n"
    "s_add_u32 s80, s61, 17408\n"
    "buffer_load_dwordx4 v25, s[76:79], s80 offen offset:3072  lds\n"
    "s_mov_b32 m0, 90112\n"
    "s_add_u32 s80, s61, 8192\n"
    "buffer_load_dwordx4 v24, s[76:79], s80 offen offset:0  lds\n"
    "s_add_u32 s80, s61, 11264\n"
    "buffer_load_dwordx4 v24, s[76:79], s80 offen offset:1024  lds\n"
    "s_add_u32 s80, s61, 22528\n"
    "buffer_load_dwordx4 v25, s[76:79], s80 offen offset:2048  lds\n"
    "s_add_u32 s80, s61, 25600\n"
    "buffer_load_dwordx4 v25, s[76:79], s80 offen offset:3072  lds\n"
    "s_mov_b32 m0, 106496\n"
    "s_add_u32 s80, s61, 32768\n"
    "buffer_load_dwordx4 v24, s[76:79], s80 offen offset:0  lds\n"
    "s_add_u32 s80, s61, 35840\n"
    "buffer_load_dwordx4 v24, s[76:79], s80 offen offset:1024  lds\n"
    "s_add_u32 s80, s61, 47104\n"
    "buffer_load_dwordx4 v25, s[76:79], s80 offen offset:2048  lds\n"
    "s_add_u32 s80, s61, 50176\n"
    "buffer_load_dwordx4 v25, s[76:79], s80 offen offset:3072  lds\n"
    "s_mov_b32 m0, 122880\n"
    "s_add_u32 s80, s61, 40960\n"
    "buffer_load_dwordx4 v24, s[76:79], s80 offen offset:0  lds\n"
    "s_add_u32 s80, s61, 44032\n"
    "buffer_load_dwordx4 v24, s[76:79], s80 offen offset:1024  lds\n"
    "s_add_u32 s80, s61, 55296\n"
    "buffer_load_dwordx4 v25, s[76:79], s80 offen offset:2048  lds\n"
    "s_add_u32 s80, s61, 58368\n"
    "buffer_load_dwordx4 v25, s[76:79], s80 offen offset:3072  lds\n"
    "s_bfe_u32 s24, %1, 0x10000\n"
    "s_and_b32 s26, s16, 0xffff\n"
    "s_and_b32 s27, s17, 0xffff\n"
    "s_mov_b32 s48, 0x3e0293ee\n"
    "s_mov_b32 s96, 0x00ff00ff\n"
    "s_mov_b32 s97, s96\n"
    "s_not_b64 s[98:99], s[96:97]\n"
    "v_lshlrev_b32 v107, 4, v0\n"
    "v_mov_b64 v[64:65], 0\n"
    "v_lshlrev_b32 v5, 4, v0\n"
    "v_add_u32 v117, 135168, v5\n"
    "v_and_b32 v3, 7, v0\n"
    "v_bfe_u32 v4, v0, 3, 1\n"
    "v_mov_b32 v5, 0x4444\n"
    "v_mov_b32 v6, 0xeeee\n"
    "v_cmp_eq_u32 vcc, 1, v4\n"
    "s_nop 1\n"
    "v_cndmask_b32 v106, v5, v6, vcc\n"
    "v_add_u32 v108, 0x10000, v107\n"
    "v_add_u32 v109, 131072, v107\n"
    "v_and_b32 v5, 31, v0\n"
    "v_lshlrev_b32 v5, 4, v5\n"
    "v_add_u32 v110, 135680, v5\n"
    "v_lshl_add_u32 v5, v2, 3, v3\n"
    "v_lshlrev_b32 v5, 4, v5\n"
    "v_add_u32 v112, 137216, v5\n"
    "v_lshl_add_u32 v111, v4, 3, v112\n"
    "v_lshrrev_b32 v5, 1, v2\n"
    "v_lshl_add_u32 v6, v5, 1, v4\n"
    "v_lshlrev_b32 v6, 14, v6\n"
    "v_and_b32 v7, 1, v2\n"
    "v_lshl_add_u32 v8, v7, 3, v3\n"
    "v_lshl_add_u32 v113, v8, 4, v6\n"
    "v_mul_u32_u24 v8, 2176, v7\n"
    "v_lshl_add_u32 v8, v5, 10, v8\n"
    "v_lshrrev_b32 v9, 1, v1\n"
    "v_lshl_add_u32 v26, v9, 3, v8\n"
    "v_xor_b32 v27, 128, v26\n"
    "v_xor_b32 v28, 2176, v26\n"
    "v_xor_b32 v29, 2048, v26\n"
    "v_and_b32 v10, 1, v0\n"
    "v_mul_u32_u24 v8, 0x020202, v10\n"
    "v_lshl_add_u32 v8, v10, 25, v8\n"
    "v_add_u32 v68, 0x05040100, v8\n"
    "v_and_b32 v8, 3, v0\n"
    "v_lshl_add_u32 v8, v5, 2, v8\n"
    "v_bfe_u32 v9, v0, 2, 1\n"
    "v_lshl_add_u32 v8, v9, 3, v8\n"
    "v_lshlrev_b32 v8, 4, v8\n"
    "v_lshl_add_u32 v8, v7, 3, v8\n"
    "v_lshl_add_u32 v114, v4, 20, v8\n"
    "v_lshlrev_b32 v10, 4, v10\n"
    "v_mov_b32 v11, 0x3f80\n"
    "v_lshlrev_b32 v11, v10, v11\n"
    "v_cmp_eq_u32 vcc, v9, v7\n"
    "s_nop 1\n"
    "v_cndmask_b32 v12, 0, v11, vcc\n"
    "v_bfe_u32 v13, v0, 1, 1\n"
    "v_lshl_add_u32 v13, v4, 1, v13\n"
    "v_cmp_eq_u32 vcc, 0, v13\n"
    "s_nop 1\n"
    "v_cndmask_b32 v120, 0, v12, vcc\n"
    "v_cmp_eq_u32 vcc, 1, v13\n"
    "s_nop 1\n"
    "v_cndmask_b32 v121, 0, v12, vcc\n"
    "v_cmp_eq_u32 vcc, 2, v13\n"
    "s_nop 1\n"
    "v_cndmask_b32 v122, 0, v12, vcc\n"
    "v_cmp_eq_u32 vcc, 3, v13\n"
    "s_nop 1\n"
    "v_cndmask_b32 v123, 0, v12, vcc\n"
    "v_bfe_u32 v13, v0, 1, 2\n"
    "v_cmp_eq_u32 vcc, 0, v13\n"
    "s_nop 1\n"
    "v_cndmask_b32 v16, 0, v11, vcc\n"
    "v_cmp_eq_u32 vcc, 1, v13\n"
    "s_nop 1\n"
    "v_cndmask_b32 v17, 0, v11, vcc\n"
    "v_cmp_eq_u32 vcc, 2, v13\n"
    "s_nop 1\n"
    "v_cndmask_b32 v18, 0, v11, vcc\n"
    "v_cmp_eq_u32 vcc, 3, v13\n"
    "s_nop 1\n"
    "v_cndmask_b32 v19, 0, v11, vcc\n"
    "v_mov_b32 v124, 0x3f803f80\n"
    "v_mov_b32 v125, 0x3f803f80\n"
    "v_mov_b32 v126, 0x3f003f00\n"
    "v_mov_b32 v127, 0\n"
    "v_mov_b64 v[118:119], 0\n"
    "s_waitcnt vmcnt(28) lgkmcnt(0)\n"
    "s_barrier\n"
    "ds_read_b64 v[70:71], v26 offset:8192\n"
    "ds_read_b64 v[72:73], v26 offset:8448\n"
    "ds_read_b64 v[74:75], v26 offset:8704\n"
    "ds_read_b64 v[76:77], v26 offset:8960\n"
    "ds_read_b64 v[78:79], v28 offset:8256\n"
    "ds_read_b64 v[80:81], v28 offset:8512\n"
    "ds_read_b64 v[82:83], v28 offset:8768\n"
    "ds_read_b64 v[84:85], v28 offset:9024\n"
    "ds_read_b64 v[86:87], v27 offset:8192\n"
    "ds_read_b64 v[88:89], v27 offset:8448\n"
    "ds_read_b64 v[90:91], v27 offset:8704\n"
    "ds_read_b64 v[92:93], v27 offset:8960\n"
    "ds_read_b64 v[94:95], v29 offset:8256\n"
    "ds_read_b64 v[96:97], v29 offset:8512\n"
    "ds_read_b64 v[98:99], v29 offset:8768\n"
    "ds_read_b64 v[100:101], v29 offset:9024\n"
    "s_waitcnt lgkmcnt(14)\n"
    "v_perm_b32 v128, v72, v70, v68\n"
    "v_perm_b32 v160, v73, v71, v68\n"
    "s_waitcnt lgkmcnt(12)\n"
    "v_perm_b32 v130, v76, v74, v68\n"
    "v_perm_b32 v162, v77, v75, v68\n"
    "s_waitcnt lgkmcnt(10)\n"
    "v_perm_b32 v129, v80, v78, v68\n"
    "v_perm_b32 v161, v81, v79, v68\n"
    "s_waitcnt lgkmcnt(8)\n"
    "v_perm_b32 v131, v84, v82, v68\n"
    "v_perm_b32 v163, v85, v83, v68\n"
    "s_waitcnt lgkmcnt(6)\n"
    "v_perm_b32 v192, v88, v86, v68\n"
    "v_perm_b32 v224, v89, v87, v68\n"
    "s_waitcnt lgkmcnt(4)\n"
    "v_perm_b32 v194, v92, v90, v68\n"
    "v_perm_b32 v226, v93, v91, v68\n"
    "s_waitcnt lgkmcnt(2)\n"
    "v_perm_b32 v193, v96, v94, v68\n"
    "v_perm_b32 v225, v97, v95, v68\n"
    "s_waitcnt lgkmcnt(0)\n"
    "v_perm_b32 v195, v100, v98, v68\n"
    "v_perm_b32 v227, v101, v99, v68\n"
    "s_waitcnt vmcnt(24) lgkmcnt(0)\n"
    "s_barrier\n"
    "s_mov_b64 s[76:77], s[4:5]\n"
    "s_and_b32 s77, s77, 0xffff\n"
    "s_mov_b32 s78, 4194304\n"
    "s_mov_b32 s79, 0x20000\n"
    "s_mov_b32 m0, 8192\n"
    "s_add_u32 s80, s60, 0\n"
    "buffer_load_dwordx4 v20, s[76:79], s80 offen offset:0  lds\n"
    "s_add_u32 s80, s60, 31744\n"
    "buffer_load_dwordx4 v20, s[76:79], s80 offen offset:1024  lds\n"
    "s_add_u32 s80, s60, 63488\n"
    "buffer_load_dwordx4 v21, s[76:79], s80 offen offset:2048  lds\n"
    "s_add_u32 s80, s60, 95232\n"
    "buffer_load_dwordx4 v21, s[76:79], s80 offen offset:3072  lds\n"
    "ds_read_b64 v[70:71], v26 offset:24576\n"
    "ds_read_b64 v[72:73], v26 offset:24832\n"
    "ds_read_b64 v[74:75], v26 offset:25088\n"
    "ds_read_b64 v[76:77], v26 offset:25344\n"
    "ds_read_b64 v[78:79], v28 offset:24640\n"
    "ds_read_b64 v[80:81], v28 offset:24896\n"
    "ds_read_b64 v[82:83], v28 offset:25152\n"
    "ds_read_b64 v[84:85], v28 offset:25408\n"
    "ds_read_b64 v[86:87], v27 offset:24576\n"
    "ds_read_b64 v[88:89], v27 offset:24832\n"
    "ds_read_b64 v[90:91], v27 offset:25088\n"
    "ds_read_b64 v[92:93], v27 offset:25344\n"
    "ds_read_b64 v[94:95], v29 offset:24640\n"
    "ds_read_b64 v[96:97], v29 offset:24896\n"
    "ds_read_b64 v[98:99], v29 offset:25152\n"
    "ds_read_b64 v[100:101], v29 offset:25408\n"
    "s_waitcnt lgkmcnt(14)\n"
    "v_perm_b32 v132, v72, v70, v68\n"
    "v_perm_b32 v164, v73, v71, v68\n"
    "s_waitcnt lgkmcnt(12)\n"
    "v_perm_b32 v134, v76, v74, v68\n"
    "v_perm_b32 v166, v77, v75, v68\n"
    "s_waitcnt lgkmcnt(10)\n"
    "v_perm_b32 v133, v80, v78, v68\n"
    "v_perm_b32 v165, v81, v79, v68\n"
    "s_waitcnt lgkmcnt(8)\n"
    "v_perm_b32 v135, v84, v82, v68\n"
    "v_perm_b32 v167, v85, v83, v68\n"
    "s_waitcnt lgkmcnt(6)\n"
    "v_perm_b32 v196, v88, v86, v68\n"
    "v_perm_b32 v228, v89, v87, v68\n"
    "s_waitcnt lgkmcnt(4)\n"
    "v_perm_b32 v198, v92, v90, v68\n"
    "v_perm_b32 v230, v93, v91, v68\n"
    "s_waitcnt lgkmcnt(2)\n"
    "v_perm_b32 v197, v96, v94, v68\n"
    "v_perm_b32 v229, v97, v95, v68\n"
    "s_waitcnt lgkmcnt(0)\n"
    "v_perm_b32 v199, v100, v98, v68\n"
    "v_perm_b32 v231, v101, v99, v68\n"
    "s_waitcnt vmcnt(24) lgkmcnt(0)\n"
    "s_barrier\n"
    "s_mov_b32 m0, 24576\n"
    "s_add_u32 s80, s60, 524288\n"
    "buffer_load_dwordx4 v20, s[76:79], s80 offen offset:0  lds\n"
    "s_add_u32 s80, s60, 556032\n"
    "buffer_load_dwordx4 v20, s[76:79], s80 offen offset:1024  lds\n"
    "s_add_u32 s80, s60, 587776\n"
    "buffer_load_dwordx4 v21, s[76:79], s80 offen offset:2048  lds\n"
    "s_add_u32 s80, s60, 619520\n"
    "buffer_load_dwordx4 v21, s[76:79], s80 offen offset:3072  lds\n"
    "ds_read_b64 v[70:71], v26 offset:40960\n"
    "ds_read_b64 v[72:73], v26 offset:41216\n"
    "ds_read_b64 v[74:75], v26 offset:41472\n"
    "ds_read_b64 v[76:77], v26 offset:41728\n"
    "ds_read_b64 v[78:79], v26 offset:41024\n"
    "ds_read_b64 v[80:81], v26 offset:41280\n"
    "ds_read_b64 v[82:83], v26 offset:41536\n"
    "ds_read_b64 v[84:85], v26 offset:41792\n"
    "ds_read_b64 v[86:87], v27 offset:40960\n"
    "ds_read_b64 v[88:89], v27 offset:41216\n"
    "ds_read_b64 v[90:91], v27 offset:41472\n"
    "ds_read_b64 v[92:93], v27 offset:41728\n"
    "ds_read_b64 v[94:95], v27 offset:41024\n"
    "ds_read_b64 v[96:97], v27 offset:41280\n"
    "ds_read_b64 v[98:99], v27 offset:41536\n"
    "ds_read_b64 v[100:101], v27 offset:41792\n"
    "s_waitcnt lgkmcnt(14)\n"
    "v_perm_b32 v136, v72, v70, v68\n"
    "v_perm_b32 v168, v73, v71, v68\n"
    "s_waitcnt lgkmcnt(12)\n"
    "v_perm_b32 v138, v76, v74, v68\n"
    "v_perm_b32 v170, v77, v75, v68\n"
    "s_waitcnt lgkmcnt(10)\n"
    "v_perm_b32 v137, v80, v78, v68\n"
    "v_perm_b32 v169, v81, v79, v68\n"
    "s_waitcnt lgkmcnt(8)\n"
    "v_perm_b32 v139, v84, v82, v68\n"
    "v_perm_b32 v171, v85, v83, v68\n"
    "s_waitcnt lgkmcnt(6)\n"
    "v_perm_b32 v200, v88, v86, v68\n"
    "v_perm_b32 v232, v89, v87, v68\n"
    "s_waitcnt lgkmcnt(4)\n"
    "v_perm_b32 v202, v92, v90, v68\n"
    "v_perm_b32 v234, v93, v91, v68\n"
    "s_waitcnt lgkmcnt(2)\n"
    "v_perm_b32 v201, v96, v94, v68\n"
    "v_perm_b32 v233, v97, v95, v68\n"
    "s_waitcnt lgkmcnt(0)\n"
    "v_perm_b32 v203, v100, v98, v68\n"
    "v_perm_b32 v235, v101, v99, v68\n"
    "s_waitcnt vmcnt(24) lgkmcnt(0)\n"
    "s_barrier\n"
    "s_mov_b32 m0, 40960\n"
    "s_add_u32 s80, s60, 1048576\n"
    "buffer_load_dwordx4 v20, s[76:79], s80 offen offset:0  lds\n"
    "s_add_u32 s80, s60, 1080320\n"
    "buffer_load_dwordx4 v20, s[76:79], s80 offen offset:1024  lds\n"
    "s_add_u32 s80, s60, 1112064\n"
    "buffer_load_dwordx4 v21, s[76:79], s80 offen offset:2048  lds\n"
    "s_add_u32 s80, s60, 1143808\n"
    "buffer_load_dwordx4 v21, s[76:79], s80 offen offset:3072  lds\n"
    "ds_read_b64 v[70:71], v26 offset:57344\n"
    "ds_read_b64 v[72:73], v26 offset:57600\n"
    "ds_read_b64 v[74:75], v26 offset:57856\n"
    "ds_read_b64 v[76:77], v26 offset:58112\n"
    "ds_read_b64 v[78:79], v26 offset:57408\n"
    "ds_read_b64 v[80:81], v26 offset:57664\n"
    "ds_read_b64 v[82:83], v26 offset:57920\n"
    "ds_read_b64 v[84:85], v26 offset:58176\n"
    "ds_read_b64 v[86:87], v27 offset:57344\n"
    "ds_read_b64 v[88:89], v27 offset:57600\n"
    "ds_read_b64 v[90:91], v27 offset:57856\n"
    "ds_read_b64 v[92:93], v27 offset:58112\n"
    "ds_read_b64 v[94:95], v27 offset:57408\n"
    "ds_read_b64 v[96:97], v27 offset:57664\n"
    "ds_read_b64 v[98:99], v27 offset:57920\n"
    "ds_read_b64 v[100:101], v27 offset:58176\n"
    "s_waitcnt lgkmcnt(14)\n"
    "v_perm_b32 v140, v72, v70, v68\n"
    "v_perm_b32 v172, v73, v71, v68\n"
    "s_waitcnt lgkmcnt(12)\n"
    "v_perm_b32 v142, v76, v74, v68\n"
    "v_perm_b32 v174, v77, v75, v68\n"
    "s_waitcnt lgkmcnt(10)\n"
    "v_perm_b32 v141, v80, v78, v68\n"
    "v_perm_b32 v173, v81, v79, v68\n"
    "s_waitcnt lgkmcnt(8)\n"
    "v_perm_b32 v143, v84, v82, v68\n"
    "v_perm_b32 v175, v85, v83, v68\n"
    "s_waitcnt lgkmcnt(6)\n"
    "v_perm_b32 v204, v88, v86, v68\n"
    "v_perm_b32 v236, v89, v87, v68\n"
    "s_waitcnt lgkmcnt(4)\n"
    "v_perm_b32 v206, v92, v90, v68\n"
    "v_perm_b32 v238, v93, v91, v68\n"
    "s_waitcnt lgkmcnt(2)\n"
    "v_perm_b32 v205, v96, v94, v68\n"
    "v_perm_b32 v237, v97, v95, v68\n"
    "s_waitcnt lgkmcnt(0)\n"
    "v_perm_b32 v207, v100, v98, v68\n"
    "v_perm_b32 v239, v101, v99, v68\n"
    "s_waitcnt vmcnt(24) lgkmcnt(0)\n"
    "s_barrier\n"
    "s_mov_b32 m0, 57344\n"
    "s_add_u32 s80, s60, 1572864\n"
    "buffer_load_dwordx4 v20, s[76:79], s80 offen offset:0  lds\n"
    "s_add_u32 s80, s60, 1604608\n"
    "buffer_load_dwordx4 v20, s[76:79], s80 offen offset:1024  lds\n"
    "s_add_u32 s80, s60, 1636352\n"
    "buffer_load_dwordx4 v21, s[76:79], s80 offen offset:2048  lds\n"
    "s_add_u32 s80, s60, 1668096\n"
    "buffer_load_dwordx4 v21, s[76:79], s80 offen offset:3072  lds\n"
    "v_add_u32 v22, 65536, v22\n"
    "v_add_u32 v23, 65536, v23\n"
    "v_add_u32 v26, 65536, v26\n"
    "v_add_u32 v27, 65536, v27\n"
    "v_add_u32 v28, 65536, v28\n"
    "v_add_u32 v29, 65536, v29\n"
    "ds_read_b64 v[70:71], v26 offset:8192\n"
    "ds_read_b64 v[72:73], v26 offset:8448\n"
    "ds_read_b64 v[74:75], v26 offset:8704\n"
    "ds_read_b64 v[76:77], v26 offset:8960\n"
    "ds_read_b64 v[78:79], v26 offset:8256\n"
    "ds_read_b64 v[80:81], v26 offset:8512\n"
    "ds_read_b64 v[82:83], v26 offset:8768\n"
    "ds_read_b64 v[84:85], v26 offset:9024\n"
    "ds_read_b64 v[86:87], v27 offset:8192\n"
    "ds_read_b64 v[88:89], v27 offset:8448\n"
    "ds_read_b64 v[90:91], v27 offset:8704\n"
    "ds_read_b64 v[92:93], v27 offset:8960\n"
    "ds_read_b64 v[94:95], v27 offset:8256\n"
    "ds_read_b64 v[96:97], v27 offset:8512\n"
    "ds_read_b64 v[98:99], v27 offset:8768\n"
    "ds_read_b64 v[100:101], v27 offset:9024\n"
    "s_waitcnt lgkmcnt(14)\n"
    "v_perm_b32 v144, v72, v70, v68\n"
    "v_perm_b32 v176, v73, v71, v68\n"
    "s_waitcnt lgkmcnt(12)\n"
    "v_perm_b32 v146, v76, v74, v68\n"
    "v_perm_b32 v178, v77, v75, v68\n"
    "s_waitcnt lgkmcnt(10)\n"
    "v_perm_b32 v145, v80, v78, v68\n"
    "v_perm_b32 v177, v81, v79, v68\n"
    "s_waitcnt lgkmcnt(8)\n"
    "v_perm_b32 v147, v84, v82, v68\n"
    "v_perm_b32 v179, v85, v83, v68\n"
    "s_waitcnt lgkmcnt(6)\n"
    "v_perm_b32 v208, v88, v86, v68\n"
    "v_perm_b32 v240, v89, v87, v68\n"
    "s_waitcnt lgkmcnt(4)\n"
    "v_perm_b32 v210, v92, v90, v68\n"
    "v_perm_b32 v242, v93, v91, v68\n"
    "s_waitcnt lgkmcnt(2)\n"
    "v_perm_b32 v209, v96, v94, v68\n"
    "v_perm_b32 v241, v97, v95, v68\n"
    "s_waitcnt lgkmcnt(0)\n"
    "v_perm_b32 v211, v100, v98, v68\n"
    "v_perm_b32 v243, v101, v99, v68\n"
    "s_waitcnt vmcnt(24) lgkmcnt(0)\n"
    "s_barrier\n"
    "s_mov_b32 m0, 73728\n"
    "s_add_u32 s80, s60, 2097152\n"
    "buffer_load_dwordx4 v20, s[76:79], s80 offen offset:0  lds\n"
    "s_add_u32 s80, s60, 2128896\n"
    "buffer_load_dwordx4 v20, s[76:79], s80 offen offset:1024  lds\n"
    "s_add_u32 s80, s60, 2160640\n"
    "buffer_load_dwordx4 v21, s[76:79], s80 offen offset:2048  lds\n"
    "s_add_u32 s80, s60, 2192384\n"
    "buffer_load_dwordx4 v21, s[76:79], s80 offen offset:3072  lds\n"
    "ds_read_b64 v[70:71], v26 offset:24576\n"
    "ds_read_b64 v[72:73], v26 offset:24832\n"
    "ds_read_b64 v[74:75], v26 offset:25088\n"
    "ds_read_b64 v[76:77], v26 offset:25344\n"
    "ds_read_b64 v[78:79], v26 offset:24640\n"
    "ds_read_b64 v[80:81], v26 offset:24896\n"
    "ds_read_b64 v[82:83], v26 offset:25152\n"
    "ds_read_b64 v[84:85], v26 offset:25408\n"
    "ds_read_b64 v[86:87], v27 offset:24576\n"
    "ds_read_b64 v[88:89], v27 offset:24832\n"
    "ds_read_b64 v[90:91], v27 offset:25088\n"
    "ds_read_b64 v[92:93], v27 offset:25344\n"
    "ds_read_b64 v[94:95], v27 offset:24640\n"
    "ds_read_b64 v[96:97], v27 offset:24896\n"
    "ds_read_b64 v[98:99], v27 offset:25152\n"
    "ds_read_b64 v[100:101], v27 offset:25408\n"
    "s_waitcnt lgkmcnt(14)\n"
    "v_perm_b32 v148, v72, v70, v68\n"
    "v_perm_b32 v180, v73, v71, v68\n"
    "s_waitcnt lgkmcnt(12)\n"
    "v_perm_b32 v150, v76, v74, v68\n"
    "v_perm_b32 v182, v77, v75, v68\n"
    "s_waitcnt lgkmcnt(10)\n"
    "v_perm_b32 v149, v80, v78, v68\n"
    "v_perm_b32 v181, v81, v79, v68\n"
    "s_waitcnt lgkmcnt(8)\n"
    "v_perm_b32 v151, v84, v82, v68\n"
    "v_perm_b32 v183, v85, v83, v68\n"
    "s_waitcnt lgkmcnt(6)\n"
    "v_perm_b32 v212, v88, v86, v68\n"
    "v_perm_b32 v244, v89, v87, v68\n"
    "s_waitcnt lgkmcnt(4)\n"
    "v_perm_b32 v214, v92, v90, v68\n"
    "v_perm_b32 v246, v93, v91, v68\n"
    "s_waitcnt lgkmcnt(2)\n"
    "v_perm_b32 v213, v96, v94, v68\n"
    "v_perm_b32 v245, v97, v95, v68\n"
    "s_waitcnt lgkmcnt(0)\n"
    "v_perm_b32 v215, v100, v98, v68\n"
    "v_perm_b32 v247, v101, v99, v68\n"
    "s_waitcnt vmcnt(24) lgkmcnt(0)\n"
    "s_barrier\n"
    "s_mov_b32 m0, 90112\n"
    "s_add_u32 s80, s60, 2621440\n"
    "buffer_load_dwordx4 v20, s[76:79], s80 offen offset:0  lds\n"
    "s_add_u32 s80, s60, 2653184\n"
    "buffer_load_dwordx4 v20, s[76:79], s80 offen offset:1024  lds\n"
    "s_add_u32 s80, s60, 2684928\n"
    "buffer_load_dwordx4 v21, s[76:79], s80 offen offset:2048  lds\n"
    "s_add_u32 s80, s60, 2716672\n"
    "buffer_load_dwordx4 v21, s[76:79], s80 offen offset:3072  lds\n"
    "ds_read_b64 v[70:71], v26 offset:40960\n"
    "ds_read_b64 v[72:73], v26 offset:41216\n"
    "ds_read_b64 v[74:75], v26 offset:41472\n"
    "ds_read_b64 v[76:77], v26 offset:41728\n"
    "ds_read_b64 v[78:79], v26 offset:41024\n"
    "ds_read_b64 v[80:81], v26 offset:41280\n"
    "ds_read_b64 v[82:83], v26 offset:41536\n"
    "ds_read_b64 v[84:85], v26 offset:41792\n"
    "ds_read_b64 v[86:87], v27 offset:40960\n"
    "ds_read_b64 v[88:89], v27 offset:41216\n"
    "ds_read_b64 v[90:91], v27 offset:41472\n"
    "ds_read_b64 v[92:93], v27 offset:41728\n"
    "ds_read_b64 v[94:95], v27 offset:41024\n"
    "ds_read_b64 v[96:97], v27 offset:41280\n"
    "ds_read_b64 v[98:99], v27 offset:41536\n"
    "ds_read_b64 v[100:101], v27 offset:41792\n"
    "s_waitcnt lgkmcnt(14)\n"
    "v_perm_b32 v152, v72, v70, v68\n"
    "v_perm_b32 v184, v73, v71, v68\n"
    "s_waitcnt lgkmcnt(12)\n"
    "v_perm_b32 v154, v76, v74, v68\n"
    "v_perm_b32 v186, v77, v75, v68\n"
    "s_waitcnt lgkmcnt(10)\n"
    "v_perm_b32 v153, v80, v78, v68\n"
    "v_perm_b32 v185, v81, v79, v68\n"
    "s_waitcnt lgkmcnt(8)\n"
    "v_perm_b32 v155, v84, v82, v68\n"
    "v_perm_b32 v187, v85, v83, v68\n"
    "s_waitcnt lgkmcnt(6)\n"
    "v_perm_b32 v216, v88, v86, v68\n"
    "v_perm_b32 v248, v89, v87, v68\n"
    "s_waitcnt lgkmcnt(4)\n"
    "v_perm_b32 v218, v92, v90, v68\n"
    "v_perm_b32 v250, v93, v91, v68\n"
    "s_waitcnt lgkmcnt(2)\n"
    "v_perm_b32 v217, v96, v94, v68\n"
    "v_perm_b32 v249, v97, v95, v68\n"
    "s_waitcnt lgkmcnt(0)\n"
    "v_perm_b32 v219, v100, v98, v68\n"
    "v_perm_b32 v251, v101, v99, v68\n"
    "s_waitcnt vmcnt(24) lgkmcnt(0)\n"
    "s_barrier\n"
    "s_mov_b32 m0, 106496\n"
    "s_add_u32 s80, s60, 3145728\n"
    "buffer_load_dwordx4 v20, s[76:79], s80 offen offset:0  lds\n"
    "s_add_u32 s80, s60, 3177472\n"
    "buffer_load_dwordx4 v20, s[76:79], s80 offen offset:1024  lds\n"
    "s_add_u32 s80, s60, 3209216\n"
    "buffer_load_dwordx4 v21, s[76:79], s80 offen offset:2048  lds\n"
    "s_add_u32 s80, s60, 3240960\n"
    "buffer_load_dwordx4 v21, s[76:79], s80 offen offset:3072  lds\n"
    "ds_read_b64 v[70:71], v26 offset:57344\n"
    "ds_read_b64 v[72:73], v26 offset:57600\n"
    "ds_read_b64 v[74:75], v26 offset:57856\n"
    "ds_read_b64 v[76:77], v26 offset:58112\n"
    "ds_read_b64 v[78:79], v26 offset:57408\n"
    "ds_read_b64 v[80:81], v26 offset:57664\n"
    "ds_read_b64 v[82:83], v26 offset:57920\n"
    "ds_read_b64 v[84:85], v26 offset:58176\n"
    "ds_read_b64 v[86:87], v27 offset:57344\n"
    "ds_read_b64 v[88:89], v27 offset:57600\n"
    "ds_read_b64 v[90:91], v27 offset:57856\n"
    "ds_read_b64 v[92:93], v27 offset:58112\n"
    "ds_read_b64 v[94:95], v27 offset:57408\n"
    "ds_read_b64 v[96:97], v27 offset:57664\n"
    "ds_read_b64 v[98:99], v27 offset:57920\n"
    "ds_read_b64 v[100:101], v27 offset:58176\n"
    "s_waitcnt lgkmcnt(14)\n"
    "v_perm_b32 v156, v72, v70, v68\n"
    "v_perm_b32 v188, v73, v71, v68\n"
    "s_waitcnt lgkmcnt(12)\n"
    "v_perm_b32 v158, v76, v74, v68\n"
    "v_perm_b32 v190, v77, v75, v68\n"
    "s_waitcnt lgkmcnt(10)\n"
    "v_perm_b32 v157, v80, v78, v68\n"
    "v_perm_b32 v189, v81, v79, v68\n"
    "s_waitcnt lgkmcnt(8)\n"
    "v_perm_b32 v159, v84, v82, v68\n"
    "v_perm_b32 v191, v85, v83, v68\n"
    "s_waitcnt lgkmcnt(6)\n"
    "v_perm_b32 v220, v88, v86, v68\n"
    "v_perm_b32 v252, v89, v87, v68\n"
    "s_waitcnt lgkmcnt(4)\n"
    "v_perm_b32 v222, v92, v90, v68\n"
    "v_perm_b32 v254, v93, v91, v68\n"
    "s_waitcnt lgkmcnt(2)\n"
    "v_perm_b32 v221, v96, v94, v68\n"
    "v_perm_b32 v253, v97, v95, v68\n"
    "s_waitcnt lgkmcnt(0)\n"
    "v_perm_b32 v223, v100, v98, v68\n"
    "v_perm_b32 v255, v101, v99, v68\n"
    "s_waitcnt vmcnt(24) lgkmcnt(0)\n"
    "s_barrier\n"
    "s_mov_b32 m0, 122880\n"
    "s_add_u32 s80, s60, 3670016\n"
    "buffer_load_dwordx4 v20, s[76:79], s80 offen offset:0  lds\n"
    "s_add_u32 s80, s60, 3701760\n"
    "buffer_load_dwordx4 v20, s[76:79], s80 offen offset:1024  lds\n"
    "s_add_u32 s80, s60, 3733504\n"
    "buffer_load_dwordx4 v21, s[76:79], s80 offen offset:2048  lds\n"
    "s_add_u32 s80, s60, 3765248\n"
    "buffer_load_dwordx4 v21, s[76:79], s80 offen offset:3072  lds\n"
    "buffer_load_dwordx2 v[4:5], off, s[20:23], s63 sc1\n"
    "v_add_u32 v22, 4294901760, v22\n"
    "v_add_u32 v23, 4294901760, v23\n"
    "v_add_u32 v26, 4294901760, v26\n"
    "v_add_u32 v27, 4294901760, v27\n"
    "v_add_u32 v28, 4294901760, v28\n"
    "v_add_u32 v29, 4294901760, v29\n"
    "ds_read_b64_tr_b16 a[32:33], v22 offset:0\n"
    "ds_read_b64_tr_b16 a[96:97], v23 offset:0\n"
    "ds_read_b64_tr_b16 a[34:35], v22 offset:1024\n"
    "ds_read_b64_tr_b16 a[98:99], v23 offset:1024\n"
    "ds_read_b64_tr_b16 a[36:37], v22 offset:8192\n"
    "ds_read_b64_tr_b16 a[100:101], v23 offset:8192\n"
    "ds_read_b64_tr_b16 a[38:39], v22 offset:9216\n"
    "ds_read_b64_tr_b16 a[102:103], v23 offset:9216\n"
    "s_waitcnt vmcnt(25) lgkmcnt(0)\n"
    "s_barrier\n"
    "s_mov_b64 s[76:77], s[8:9]\n"
    "s_and_b32 s77, s77, 0xffff\n"
    "s_mov_b32 s78, 4194304\n"
    "s_mov_b32 s79, 0x20000\n"
    "s_mov_b32 m0, 8192\n"
    "s_add_u32 s80, s60, 0\n"
    "buffer_load_dwordx4 v20, s[76:79], s80 offen offset:0  lds\n"
    "s_add_u32 s80, s60, 31744\n"
    "buffer_load_dwordx4 v20, s[76:79], s80 offen offset:1024  lds\n"
    "s_add_u32 s80, s60, 63488\n"
    "buffer_load_dwordx4 v21, s[76:79], s80 offen offset:2048  lds\n"
    "s_add_u32 s80, s60, 95232\n"
    "buffer_load_dwordx4 v21, s[76:79], s80 offen offset:3072  lds\n"
    "ds_read_b64_tr_b16 a[40:41], v22 offset:16384\n"
    "ds_read_b64_tr_b16 a[104:105], v23 offset:16384\n"
    "ds_read_b64_tr_b16 a[42:43], v22 offset:17408\n"
    "ds_read_b64_tr_b16 a[106:107], v23 offset:17408\n"
    "ds_read_b64_tr_b16 a[44:45], v22 offset:24576\n"
    "ds_read_b64_tr_b16 a[108:109], v23 offset:24576\n"
    "ds_read_b64_tr_b16 a[46:47], v22 offset:25600\n"
    "ds_read_b64_tr_b16 a[110:111], v23 offset:25600\n"
    "s_waitcnt vmcnt(25) lgkmcnt(0)\n"
    "s_barrier\n"
    "s_mov_b32 m0, 24576\n"
    "s_add_u32 s80, s60, 524288\n"
    "buffer_load_dwordx4 v20, s[76:79], s80 offen offset:0  lds\n"
    "s_add_u32 s80, s60, 556032\n"
    "buffer_load_dwordx4 v20, s[76:79], s80 offen offset:1024  lds\n"
    "s_add_u32 s80, s60, 587776\n"
    "buffer_load_dwordx4 v21, s[76:79], s80 offen offset:2048  lds\n"
    "s_add_u32 s80, s60, 619520\n"
    "buffer_load_dwordx4 v21, s[76:79], s80 offen offset:3072  lds\n"
    "ds_read_b64_tr_b16 a[48:49], v22 offset:32768\n"
    "ds_read_b64_tr_b16 a[112:113], v23 offset:32768\n"
    "ds_read_b64_tr_b16 a[50:51], v22 offset:33792\n"
    "ds_read_b64_tr_b16 a[114:115], v23 offset:33792\n"
    "ds_read_b64_tr_b16 a[52:53], v22 offset:40960\n"
    "ds_read_b64_tr_b16 a[116:117], v23 offset:40960\n"
    "ds_read_b64_tr_b16 a[54:55], v22 offset:41984\n"
    "ds_read_b64_tr_b16 a[118:119], v23 offset:41984\n"
    "s_waitcnt vmcnt(25) lgkmcnt(0)\n"
    "s_barrier\n"
    "s_mov_b32 m0, 40960\n"
    "s_add_u32 s80, s60, 1048576\n"
    "buffer_load_dwordx4 v20, s[76:79], s80 offen offset:0  lds\n"
    "s_add_u32 s80, s60, 1080320\n"
    "buffer_load_dwordx4 v20, s[76:79], s80 offen offset:1024  lds\n"
    "s_add_u32 s80, s60, 1112064\n"
    "buffer_load_dwordx4 v21, s[76:79], s80 offen offset:2048  lds\n"
    "s_add_u32 s80, s60, 1143808\n"
    "buffer_load_dwordx4 v21, s[76:79], s80 offen offset:3072  lds\n"
    "ds_read_b64_tr_b16 a[56:57], v22 offset:49152\n"
    "ds_read_b64_tr_b16 a[120:121], v23 offset:49152\n"
    "ds_read_b64_tr_b16 a[58:59], v22 offset:50176\n"
    "ds_read_b64_tr_b16 a[122:123], v23 offset:50176\n"
    "ds_read_b64_tr_b16 a[60:61], v22 offset:57344\n"
    "ds_read_b64_tr_b16 a[124:125], v23 offset:57344\n"
    "ds_read_b64_tr_b16 a[62:63], v22 offset:58368\n"
    "ds_read_b64_tr_b16 a[126:127], v23 offset:58368\n"
    "s_waitcnt vmcnt(25) lgkmcnt(0)\n"
    "s_barrier\n"
    "s_mov_b32 m0, 57344\n"
    "s_add_u32 s80, s60, 1572864\n"
    "buffer_load_dwordx4 v20, s[76:79], s80 offen offset:0  lds\n"
    "s_add_u32 s80, s60, 1604608\n"
    "buffer_load_dwordx4 v20, s[76:79], s80 offen offset:1024  lds\n"
    "s_add_u32 s80, s60, 1636352\n"
    "buffer_load_dwordx4 v21, s[76:79], s80 offen offset:2048  lds\n"
    "s_add_u32 s80, s60, 1668096\n"
    "buffer_load_dwordx4 v21, s[76:79], s80 offen offset:3072  lds\n"
    "s_getpc_b64 s[94:95]\n"
    "RCA12_2_%=:\n"
    "v_lshlrev_b32 v10, 7, v0\n"
    "v_min_u32 v10, .Lcode_end-RCA12_2_%=-4, v10\n"
    "global_load_dword v10, v10, s[94:95]\n"
    "v_add_u32 v22, 65536, v22\n"
    "v_add_u32 v23, 65536, v23\n"
    "v_add_u32 v26, 65536, v26\n"
    "v_add_u32 v27, 65536, v27\n"
    "v_add_u32 v28, 65536, v28\n"
    "v_add_u32 v29, 65536, v29\n"
    "ds_read_b64_tr_b16 a[0:1], v22 offset:0\n"
    "ds_read_b64_tr_b16 a[64:65], v23 offset:0\n"
    "ds_read_b64_tr_b16 a[2:3], v22 offset:1024\n"
    "ds_read_b64_tr_b16 a[66:67], v23 offset:1024\n"
    "ds_read_b64_tr_b16 a[4:5], v22 offset:8192\n"
    "ds_read_b64_tr_b16 a[68:69], v23 offset:8192\n"
    "ds_read_b64_tr_b16 a[6:7], v22 offset:9216\n"
    "ds_read_b64_tr_b16 a[70:71], v23 offset:9216\n"
    "s_waitcnt vmcnt(26) lgkmcnt(0)\n"
    "s_barrier\n"
    "s_mov_b32 m0, 73728\n"
    "s_add_u32 s80, s60, 2097152\n"
    "buffer_load_dwordx4 v20, s[76:79], s80 offen offset:0  lds\n"
    "s_add_u32 s80, s60, 2128896\n"
    "buffer_load_dwordx4 v20, s[76:79], s80 offen offset:1024  lds\n"
    "s_add_u32 s80, s60, 2160640\n"
    "buffer_load_dwordx4 v21, s[76:79], s80 offen offset:2048  lds\n"
    "s_add_u32 s80, s60, 2192384\n"
    "buffer_load_dwordx4 v21, s[76:79], s80 offen offset:3072  lds\n"
    "ds_read_b64_tr_b16 a[8:9], v22 offset:16384\n"
    "ds_read_b64_tr_b16 a[72:73], v23 offset:16384\n"
    "ds_read_b64_tr_b16 a[10:11], v22 offset:17408\n"
    "ds_read_b64_tr_b16 a[74:75], v23 offset:17408\n"
    "ds_read_b64_tr_b16 a[12:13], v22 offset:24576\n"
    "ds_read_b64_tr_b16 a[76:77], v23 offset:24576\n"
    "ds_read_b64_tr_b16 a[14:15], v22 offset:25600\n"
    "ds_read_b64_tr_b16 a[78:79], v23 offset:25600\n"
    "s_waitcnt vmcnt(26) lgkmcnt(0)\n"
    "s_barrier\n"
    "s_mov_b32 m0, 90112\n"
    "s_add_u32 s80, s60, 2621440\n"
    "buffer_load_dwordx4 v20, s[76:79], s80 offen offset:0  lds\n"
    "s_add_u32 s80, s60, 2653184\n"
    "buffer_load_dwordx4 v20, s[76:79], s80 offen offset:1024  lds\n"
    "s_add_u32 s80, s60, 2684928\n"
    "buffer_load_dwordx4 v21, s[76:79], s80 offen offset:2048  lds\n"
    "s_add_u32 s80, s60, 2716672\n"
    "buffer_load_dwordx4 v21, s[76:79], s80 offen offset:3072  lds\n"
    "ds_read_b64_tr_b16 a[16:17], v22 offset:32768\n"
    "ds_read_b64_tr_b16 a[80:81], v23 offset:32768\n"
    "ds_read_b64_tr_b16 a[18:19], v22 offset:33792\n"
    "ds_read_b64_tr_b16 a[82:83], v23 offset:33792\n"
    "ds_read_b64_tr_b16 a[20:21], v22 offset:40960\n"
    "ds_read_b64_tr_b16 a[84:85], v23 offset:40960\n"
    "ds_read_b64_tr_b16 a[22:23], v22 offset:41984\n"
    "ds_read_b64_tr_b16 a[86:87], v23 offset:41984\n"
    "s_waitcnt vmcnt(26) lgkmcnt(0)\n"
    "s_barrier\n"
    "s_mov_b32 m0, 106496\n"
    "s_add_u32 s80, s60, 3145728\n"
    "buffer_load_dwordx4 v20, s[76:79], s80 offen offset:0  lds\n"
    "s_add_u32 s80, s60, 3177472\n"
    "buffer_load_dwordx4 v20, s[76:79], s80 offen offset:1024  lds\n"
    "s_add_u32 s80, s60, 3209216\n"
    "buffer_load_dwordx4 v21, s[76:79], s80 offen offset:2048  lds\n"
    "s_add_u32 s80, s60, 3240960\n"
    "buffer_load_dwordx4 v21, s[76:79], s80 offen offset:3072  lds\n"
    "ds_read_b64_tr_b16 a[24:25], v22 offset:49152\n"
    "ds_read_b64_tr_b16 a[88:89], v23 offset:49152\n"
    "ds_read_b64_tr_b16 a[26:27], v22 offset:50176\n"
    "ds_read_b64_tr_b16 a[90:91], v23 offset:50176\n"
    "ds_read_b64_tr_b16 a[28:29], v22 offset:57344\n"
    "ds_read_b64_tr_b16 a[92:93], v23 offset:57344\n"
    "ds_read_b64_tr_b16 a[30:31], v22 offset:58368\n"
    "ds_read_b64_tr_b16 a[94:95], v23 offset:58368\n"
    "s_waitcnt vmcnt(25) lgkmcnt(0)\n"
    "s_barrier\n"
    "s_mov_b32 m0, 122880\n"
    "s_add_u32 s80, s60, 3670016\n"
    "buffer_load_dwordx4 v20, s[76:79], s80 offen offset:0  lds\n"
    "s_add_u32 s80, s60, 3701760\n"
    "buffer_load_dwordx4 v20, s[76:79], s80 offen offset:1024  lds\n"
    "s_add_u32 s80, s60, 3733504\n"
    "buffer_load_dwordx4 v21, s[76:79], s80 offen offset:2048  lds\n"
    "s_add_u32 s80, s60, 3765248\n"
    "buffer_load_dwordx4 v21, s[76:79], s80 offen offset:3072  lds\n"
    "v_readfirstlane_b32 s46, v67\n"
    "v_readfirstlane_b32 s68, v5\n"
    "s_cmp_lg_u32 s24, 0\n"
    "s_cselect_b32 s46, %2, s46\n"
    "s_cmp_lg_u32 s46, s68\n"
    "s_cselect_b32 s68, -1, 0\n"
    "buffer_load_dwordx2 v[4:5], off, s[20:23], s63 sc1\n"
    "v_lshrrev_b32 v6, 2, v0\n"
    "v_and_b32 v7, 3, v0\n"
    "v_lshl_add_u32 v6, v6, 3, v7\n"
    "v_lshlrev_b32 v6, 7, v6\n"
    "s_mov_b32 s46, 81793536\n"
    "buffer_load_dword v7, v6, s[20:23], s46 offen sc1\n"
    "s_mov_b32 s46, 81809920\n"
    "buffer_load_dword v8, v6, s[20:23], s46 offen sc1\n"
    "v_add_u32 v22, 4294901760, v22\n"
    "v_add_u32 v23, 4294901760, v23\n"
    "v_add_u32 v26, 4294901760, v26\n"
    "v_add_u32 v27, 4294901760, v27\n"
    "v_add_u32 v28, 4294901760, v28\n"
    "v_add_u32 v29, 4294901760, v29\n"
    "ds_read_b64_tr_b16 a[176:177], v22 offset:0\n"
    "ds_read_b64_tr_b16 a[224:225], v23 offset:0\n"
    "ds_read_b64_tr_b16 a[178:179], v22 offset:1024\n"
    "ds_read_b64_tr_b16 a[226:227], v23 offset:1024\n"
    "ds_read_b64_tr_b16 a[180:181], v22 offset:8192\n"
    "ds_read_b64_tr_b16 a[228:229], v23 offset:8192\n"
    "ds_read_b64_tr_b16 a[182:183], v22 offset:9216\n"
    "ds_read_b64_tr_b16 a[230:231], v23 offset:9216\n"
    "s_waitcnt vmcnt(28) lgkmcnt(0)\n"
    "s_barrier\n"
    "s_mov_b64 s[76:77], s[6:7]\n"
    "s_and_b32 s77, s77, 0xffff\n"
    "s_mov_b32 s78, 4194304\n"
    "s_mov_b32 s79, 0x20000\n"
    "s_mov_b32 m0, 8192\n"
    "s_add_u32 s80, s60, 0\n"
    "buffer_load_dwordx4 v20, s[76:79], s80 offen offset:0  lds\n"
    "s_add_u32 s80, s60, 31744\n"
    "buffer_load_dwordx4 v20, s[76:79], s80 offen offset:1024  lds\n"
    "s_add_u32 s80, s60, 63488\n"
    "buffer_load_dwordx4 v21, s[76:79], s80 offen offset:2048  lds\n"
    "s_add_u32 s80, s60, 95232\n"
    "buffer_load_dwordx4 v21, s[76:79], s80 offen offset:3072  lds\n"
    "ds_read_b64_tr_b16 a[184:185], v22 offset:16384\n"
    "ds_read_b64_tr_b16 a[232:233], v23 offset:16384\n"
    "ds_read_b64_tr_b16 a[186:187], v22 offset:17408\n"
    "ds_read_b64_tr_b16 a[234:235], v23 offset:17408\n"
    "ds_read_b64_tr_b16 a[188:189], v22 offset:24576\n"
    "ds_read_b64_tr_b16 a[236:237], v23 offset:24576\n"
    "ds_read_b64_tr_b16 a[190:191], v22 offset:25600\n"
    "ds_read_b64_tr_b16 a[238:239], v23 offset:25600\n"
    "s_waitcnt vmcnt(28) lgkmcnt(0)\n"
    "s_barrier\n"
    "s_mov_b32 m0, 24576\n"
    "s_add_u32 s80, s60, 524288\n"
    "buffer_load_dwordx4 v20, s[76:79], s80 offen offset:0  lds\n"
    "s_add_u32 s80, s60, 556032\n"
    "buffer_load_dwordx4 v20, s[76:79], s80 offen offset:1024  lds\n"
    "s_add_u32 s80, s60, 587776\n"
    "buffer_load_dwordx4 v21, s[76:79], s80 offen offset:2048  lds\n"
    "s_add_u32 s80, s60, 619520\n"
    "buffer_load_dwordx4 v21, s[76:79], s80 offen offset:3072  lds\n"
    "ds_read_b64_tr_b16 a[192:193], v22 offset:32768\n"
    "ds_read_b64_tr_b16 a[240:241], v23 offset:32768\n"
    "ds_read_b64_tr_b16 a[194:195], v22 offset:33792\n"
    "ds_read_b64_tr_b16 a[242:243], v23 offset:33792\n"
    "ds_read_b64_tr_b16 a[196:197], v22 offset:40960\n"
    "ds_read_b64_tr_b16 a[244:245], v23 offset:40960\n"
    "ds_read_b64_tr_b16 a[198:199], v22 offset:41984\n"
    "ds_read_b64_tr_b16 a[246:247], v23 offset:41984\n"
    "s_waitcnt vmcnt(28) lgkmcnt(0)\n"
    "s_barrier\n"
    "s_mov_b32 m0, 40960\n"
    "s_add_u32 s80, s60, 1048576\n"
    "buffer_load_dwordx4 v20, s[76:79], s80 offen offset:0  lds\n"
    "s_add_u32 s80, s60, 1080320\n"
    "buffer_load_dwordx4 v20, s[76:79], s80 offen offset:1024  lds\n"
    "s_add_u32 s80, s60, 1112064\n"
    "buffer_load_dwordx4 v21, s[76:79], s80 offen offset:2048  lds\n"
    "s_add_u32 s80, s60, 1143808\n"
    "buffer_load_dwordx4 v21, s[76:79], s80 offen offset:3072  lds\n"
    "ds_read_b64_tr_b16 a[200:201], v22 offset:49152\n"
    "ds_read_b64_tr_b16 a[248:249], v23 offset:49152\n"
    "ds_read_b64_tr_b16 a[202:203], v22 offset:50176\n"
    "ds_read_b64_tr_b16 a[250:251], v23 offset:50176\n"
    "ds_read_b64_tr_b16 a[204:205], v22 offset:57344\n"
    "ds_read_b64_tr_b16 a[252:253], v23 offset:57344\n"
    "ds_read_b64_tr_b16 a[206:207], v22 offset:58368\n"
    "ds_read_b64_tr_b16 a[254:255], v23 offset:58368\n"
    "s_waitcnt vmcnt(27) lgkmcnt(0)\n"
    "s_barrier\n"
    "s_mov_b32 m0, 57344\n"
    "s_add_u32 s80, s60, 1572864\n"
    "buffer_load_dwordx4 v20, s[76:79], s80 offen offset:0  lds\n"
    "s_add_u32 s80, s60, 1604608\n"
    "buffer_load_dwordx4 v20, s[76:79], s80 offen offset:1024  lds\n"
    "s_add_u32 s80, s60, 1636352\n"
    "buffer_load_dwordx4 v21, s[76:79], s80 offen offset:2048  lds\n"
    "s_add_u32 s80, s60, 1668096\n"
    "buffer_load_dwordx4 v21, s[76:79], s80 offen offset:3072  lds\n"
    "v_add_u32 v22, 65536, v22\n"
    "v_add_u32 v23, 65536, v23\n"
    "v_add_u32 v26, 65536, v26\n"
    "v_add_u32 v27, 65536, v27\n"
    "v_add_u32 v28, 65536, v28\n"
    "v_add_u32 v29, 65536, v29\n"
    "ds_read_b64_tr_b16 v[32:33], v22 offset:0\n"
    "ds_read_b64_tr_b16 v[40:41], v23 offset:0\n"
    "ds_read_b64_tr_b16 v[34:35], v22 offset:1024\n"
    "ds_read_b64_tr_b16 v[42:43], v23 offset:1024\n"
    "ds_read_b64_tr_b16 v[36:37], v22 offset:8192\n"
    "ds_read_b64_tr_b16 v[44:45], v23 offset:8192\n"
    "ds_read_b64_tr_b16 v[38:39], v22 offset:9216\n"
    "ds_read_b64_tr_b16 v[46:47], v23 offset:9216\n"
    "s_waitcnt vmcnt(27) lgkmcnt(0)\n"
    "s_barrier\n"
    "s_mov_b32 m0, 73728\n"
    "s_add_u32 s80, s60, 2097152\n"
    "buffer_load_dwordx4 v20, s[76:79], s80 offen offset:0  lds\n"
    "s_add_u32 s80, s60, 2128896\n"
    "buffer_load_dwordx4 v20, s[76:79], s80 offen offset:1024  lds\n"
    "s_add_u32 s80, s60, 2160640\n"
    "buffer_load_dwordx4 v21, s[76:79], s80 offen offset:2048  lds\n"
    "s_add_u32 s80, s60, 2192384\n"
    "buffer_load_dwordx4 v21, s[76:79], s80 offen offset:3072  lds\n"
    "ds_read_b64_tr_b16 v[48:49], v22 offset:16384\n"
    "ds_read_b64_tr_b16 v[56:57], v23 offset:16384\n"
    "ds_read_b64_tr_b16 v[50:51], v22 offset:17408\n"
    "ds_read_b64_tr_b16 v[58:59], v23 offset:17408\n"
    "ds_read_b64_tr_b16 v[52:53], v22 offset:24576\n"
    "ds_read_b64_tr_b16 v[60:61], v23 offset:24576\n"
    "ds_read_b64_tr_b16 v[54:55], v22 offset:25600\n"
    "ds_read_b64_tr_b16 v[62:63], v23 offset:25600\n"
    "s_waitcnt vmcnt(27) lgkmcnt(0)\n"
    "s_barrier\n"
    "s_mov_b32 m0, 90112\n"
    "s_add_u32 s80, s60, 2621440\n"
    "buffer_load_dwordx4 v20, s[76:79], s80 offen offset:0  lds\n"
    "s_add_u32 s80, s60, 2653184\n"
    "buffer_load_dwordx4 v20, s[76:79], s80 offen offset:1024  lds\n"
    "s_add_u32 s80, s60, 2684928\n"
    "buffer_load_dwordx4 v21, s[76:79], s80 offen offset:2048  lds\n"
    "s_add_u32 s80, s60, 2716672\n"
    "buffer_load_dwordx4 v21, s[76:79], s80 offen offset:3072  lds\n"
    "ds_read_b64_tr_b16 a[160:161], v22 offset:32768\n"
    "ds_read_b64_tr_b16 a[208:209], v23 offset:32768\n"
    "ds_read_b64_tr_b16 a[162:163], v22 offset:33792\n"
    "ds_read_b64_tr_b16 a[210:211], v23 offset:33792\n"
    "ds_read_b64_tr_b16 a[164:165], v22 offset:40960\n"
    "ds_read_b64_tr_b16 a[212:213], v23 offset:40960\n"
    "ds_read_b64_tr_b16 a[166:167], v22 offset:41984\n"
    "ds_read_b64_tr_b16 a[214:215], v23 offset:41984\n"
    "s_waitcnt vmcnt(27) lgkmcnt(0)\n"
    "s_barrier\n"
    "s_mov_b32 m0, 106496\n"
    "s_add_u32 s80, s60, 3145728\n"
    "buffer_load_dwordx4 v20, s[76:79], s80 offen offset:0  lds\n"
    "s_add_u32 s80, s60, 3177472\n"
    "buffer_load_dwordx4 v20, s[76:79], s80 offen offset:1024  lds\n"
    "s_add_u32 s80, s60, 3209216\n"
    "buffer_load_dwordx4 v21, s[76:79], s80 offen offset:2048  lds\n"
    "s_add_u32 s80, s60, 3240960\n"
    "buffer_load_dwordx4 v21, s[76:79], s80 offen offset:3072  lds\n"
    "ds_read_b64_tr_b16 a[168:169], v22 offset:49152\n"
    "ds_read_b64_tr_b16 a[216:217], v23 offset:49152\n"
    "ds_read_b64_tr_b16 a[170:171], v22 offset:50176\n"
    "ds_read_b64_tr_b16 a[218:219], v23 offset:50176\n"
    "ds_read_b64_tr_b16 a[172:173], v22 offset:57344\n"
    "ds_read_b64_tr_b16 a[220:221], v23 offset:57344\n"
    "ds_read_b64_tr_b16 a[174:175], v22 offset:58368\n"
    "ds_read_b64_tr_b16 a[222:223], v23 offset:58368\n"
    "s_waitcnt vmcnt(24) lgkmcnt(0)\n"
    "s_barrier\n"
    "s_mov_b32 m0, 122880\n"
    "s_add_u32 s80, s60, 3670016\n"
    "buffer_load_dwordx4 v20, s[76:79], s80 offen offset:0  lds\n"
    "s_add_u32 s80, s60, 3701760\n"
    "buffer_load_dwordx4 v20, s[76:79], s80 offen offset:1024  lds\n"
    "s_add_u32 s80, s60, 3733504\n"
    "buffer_load_dwordx4 v21, s[76:79], s80 offen offset:2048  lds\n"
    "s_add_u32 s80, s60, 3765248\n"
    "buffer_load_dwordx4 v21, s[76:79], s80 offen offset:3072  lds\n"
    "v_readfirstlane_b32 s62, v4\n"
    "s_bitcmp1_b32 s62, 1\n"
    "s_cselect_b32 s68, s68, 0\n"
    "s_xor_b32 s62, s62, s24\n"
    "s_and_b32 s62, s62, 1\n"
    "s_lshl_b32 s62, s62, 13\n"
    "v_min_u32 v7, v7, v8\n"
    "v_cmp_gt_u32 vcc, 1, v7\n"
    "s_cmp_eq_u64 vcc, 0\n"
    "s_cselect_b32 s68, s68, 0\n"
    "s_add_u32 s30, s62, 0\n"
    "s_add_u32 s31, s30, 4096\n"
    "s_lshl_b32 s47, s25, 8\n"
    "s_add_u32 s36, s62, s47\n"
    "s_mov_b32 s50, 0\n"
    "s_mov_b32 s28, s30\n"
    "s_add_u32 s32, s62, s47\n"
    "s_add_u32 s32, s32, 67371008\n"
    "s_add_u32 s33, s32, 65536\n"
    "s_lshl_b32 s47, s25, 14\n"
    "s_add_u32 s34, s62, s47\n"
    "s_add_u32 s34, s34, 67108864\n"
    "s_add_u32 s35, s34, 4096\n"
    "s_add_u32 s44, s34, 4198400\n"
    "s_lshl_b32 s39, s25, 15\n"
    "s_add_u32 s39, s39, s62\n"
    "s_add_u32 s39, s39, 16793600\n"
    "s_xor_b32 s69, s62, 0x2000\n"
    "s_lshr_b32 s41, s25, 3\n"
    "s_lshl_b32 s41, s41, 14\n"
    "s_and_b32 s47, s25, 7\n"
    "s_lshl_b32 s47, s47, 10\n"
    "s_add_u32 s41, s41, s47\n"
    "s_add_u32 s41, s41, s69\n"
    "s_add_u32 s41, s41, 3145984\n"
    "s_add_u32 s69, s69, 3309568\n"
    "s_mov_b32 s37, 0x7f000000\n"
    "s_mov_b32 s42, 0\n"
    "s_cmp_eq_u32 s25, 0\n"
    "s_cselect_b32 s37, s69, s37\n"
    "s_add_u32 s69, s69, 16384\n"
    "s_cmp_eq_u32 s25, 11\n"
    "s_cselect_b32 s37, s69, s37\n"
    "s_add_u32 s69, s69, 16384\n"
    "s_cmp_eq_u32 s25, 22\n"
    "s_cselect_b32 s37, s69, s37\n"
    "s_add_u32 s69, s69, 16384\n"
    "v_add_u32 v22, 4294901760, v22\n"
    "v_add_u32 v23, 4294901760, v23\n"
    "v_add_u32 v26, 4294901760, v26\n"
    "v_add_u32 v27, 4294901760, v27\n"
    "v_add_u32 v28, 4294901760, v28\n"
    "v_add_u32 v29, 4294901760, v29\n"
    "ds_read_b64_tr_b16 v[70:71], v22 offset:0\n"
    "ds_read_b64_tr_b16 v[78:79], v23 offset:0\n"
    "ds_read_b64_tr_b16 v[72:73], v22 offset:1024\n"
    "ds_read_b64_tr_b16 v[80:81], v23 offset:1024\n"
    "ds_read_b64_tr_b16 v[74:75], v22 offset:8192\n"
    "ds_read_b64_tr_b16 v[82:83], v23 offset:8192\n"
    "ds_read_b64_tr_b16 v[76:77], v22 offset:9216\n"
    "ds_read_b64_tr_b16 v[84:85], v23 offset:9216\n"
    "s_waitcnt vmcnt(24) lgkmcnt(0)\n"
    "s_barrier\n"
    "s_getpc_b64 s[94:95]\n"
    "RCA25_2_%=:\n"
    "v_lshlrev_b32 v11, 7, v0\n"
    "v_min_u32 v11, .Lcode_end-RCA25_2_%=-4, v11\n"
    "global_load_dword v11, v11, s[94:95]\n"
    "ds_read_b64_tr_b16 v[86:87], v22 offset:16384\n"
    "ds_read_b64_tr_b16 v[94:95], v23 offset:16384\n"
    "ds_read_b64_tr_b16 v[88:89], v22 offset:17408\n"
    "ds_read_b64_tr_b16 v[96:97], v23 offset:17408\n"
    "ds_read_b64_tr_b16 v[90:91], v22 offset:24576\n"
    "ds_read_b64_tr_b16 v[98:99], v23 offset:24576\n"
    "ds_read_b64_tr_b16 v[92:93], v22 offset:25600\n"
    "ds_read_b64_tr_b16 v[100:101], v23 offset:25600\n"
    "ds_write_b128 v107, v[70:73] offset:8192\n"
    "ds_write_b128 v107, v[74:77] offset:9216\n"
    "ds_write_b128 v107, v[78:81] offset:10240\n"
    "ds_write_b128 v107, v[82:85] offset:11264\n"
    "s_waitcnt vmcnt(21) lgkmcnt(0)\n"
    "s_barrier\n"
    "ds_read_b64_tr_b16 a[128:129], v22 offset:32768\n"
    "ds_read_b64_tr_b16 a[144:145], v23 offset:32768\n"
    "ds_read_b64_tr_b16 a[130:131], v22 offset:33792\n"
    "ds_read_b64_tr_b16 a[146:147], v23 offset:33792\n"
    "ds_read_b64_tr_b16 a[132:133], v22 offset:40960\n"
    "ds_read_b64_tr_b16 a[148:149], v23 offset:40960\n"
    "ds_read_b64_tr_b16 a[134:135], v22 offset:41984\n"
    "ds_read_b64_tr_b16 a[150:151], v23 offset:41984\n"
    "ds_write_b128 v107, v[86:89] offset:24576\n"
    "ds_write_b128 v107, v[90:93] offset:25600\n"
    "ds_write_b128 v107, v[94:97] offset:26624\n"
    "ds_write_b128 v107, v[98:101] offset:27648\n"
    "s_waitcnt vmcnt(17) lgkmcnt(0)\n"
    "s_barrier\n"
    "ds_read_b64_tr_b16 a[136:137], v22 offset:49152\n"
    "ds_read_b64_tr_b16 a[152:153], v23 offset:49152\n"
    "ds_read_b64_tr_b16 a[138:139], v22 offset:50176\n"
    "ds_read_b64_tr_b16 a[154:155], v23 offset:50176\n"
    "ds_read_b64_tr_b16 a[140:141], v22 offset:57344\n"
    "ds_read_b64_tr_b16 a[156:157], v23 offset:57344\n"
    "ds_read_b64_tr_b16 a[142:143], v22 offset:58368\n"
    "ds_read_b64_tr_b16 a[158:159], v23 offset:58368\n"
    "ds_write_b128 v107, v[32:35] offset:40960\n"
    "ds_write_b128 v107, v[36:39] offset:41984\n"
    "ds_write_b128 v107, v[40:43] offset:43008\n"
    "ds_write_b128 v107, v[44:47] offset:44032\n"
    "s_waitcnt vmcnt(13) lgkmcnt(0)\n"
    "s_barrier\n"
    "v_add_u32 v22, 65536, v22\n"
    "v_add_u32 v23, 65536, v23\n"
    "v_add_u32 v26, 65536, v26\n"
    "v_add_u32 v27, 65536, v27\n"
    "v_add_u32 v28, 65536, v28\n"
    "v_add_u32 v29, 65536, v29\n"
    "ds_read_b64_tr_b16 v[70:71], v22 offset:0\n"
    "ds_read_b64_tr_b16 v[78:79], v23 offset:0\n"
    "ds_read_b64_tr_b16 v[72:73], v22 offset:1024\n"
    "ds_read_b64_tr_b16 v[80:81], v23 offset:1024\n"
    "ds_read_b64_tr_b16 v[74:75], v22 offset:8192\n"
    "ds_read_b64_tr_b16 v[82:83], v23 offset:8192\n"
    "ds_read_b64_tr_b16 v[76:77], v22 offset:9216\n"
    "ds_read_b64_tr_b16 v[84:85], v23 offset:9216\n"
    "ds_write_b128 v107, v[48:51] offset:57344\n"
    "ds_write_b128 v107, v[52:55] offset:58368\n"
    "ds_write_b128 v107, v[56:59] offset:59392\n"
    "ds_write_b128 v107, v[60:63] offset:60416\n"
    "s_waitcnt vmcnt(9) lgkmcnt(0)\n"
    "s_barrier\n"
    "v_lshrrev_b32 v6, 2, v0\n"
    "v_and_b32 v7, 3, v0\n"
    "v_lshl_add_u32 v6, v6, 3, v7\n"
    "v_lshlrev_b32 v6, 7, v6\n"
    "s_mov_b32 s46, 81793536\n"
    "buffer_load_dword v7, v6, s[20:23], s46 offen sc1\n"
    "s_mov_b32 s46, 81809920\n"
    "buffer_load_dword v8, v6, s[20:23], s46 offen sc1\n"
    "buffer_load_dword v115, off, s[20:23], s41 sc1\n"
    "ds_read_b64_tr_b16 v[86:87], v22 offset:16384\n"
    "ds_read_b64_tr_b16 v[94:95], v23 offset:16384\n"
    "ds_read_b64_tr_b16 v[88:89], v22 offset:17408\n"
    "ds_read_b64_tr_b16 v[96:97], v23 offset:17408\n"
    "ds_read_b64_tr_b16 v[90:91], v22 offset:24576\n"
    "ds_read_b64_tr_b16 v[98:99], v23 offset:24576\n"
    "ds_read_b64_tr_b16 v[92:93], v22 offset:25600\n"
    "ds_read_b64_tr_b16 v[100:101], v23 offset:25600\n"
    "ds_write_b128 v108, v[70:73] offset:8192\n"
    "ds_write_b128 v108, v[74:77] offset:9216\n"
    "ds_write_b128 v108, v[78:81] offset:10240\n"
    "ds_write_b128 v108, v[82:85] offset:11264\n"
    "s_waitcnt vmcnt(8) lgkmcnt(0)\n"
    "s_barrier\n"
    "ds_read_b64_tr_b16 v[70:71], v22 offset:32768\n"
    "ds_read_b64_tr_b16 v[78:79], v23 offset:32768\n"
    "ds_read_b64_tr_b16 v[72:73], v22 offset:33792\n"
    "ds_read_b64_tr_b16 v[80:81], v23 offset:33792\n"
    "ds_read_b64_tr_b16 v[74:75], v22 offset:40960\n"
    "ds_read_b64_tr_b16 v[82:83], v23 offset:40960\n"
    "ds_read_b64_tr_b16 v[76:77], v22 offset:41984\n"
    "ds_read_b64_tr_b16 v[84:85], v23 offset:41984\n"
    "ds_write_b128 v108, v[86:89] offset:24576\n"
    "ds_write_b128 v108, v[90:93] offset:25600\n"
    "ds_write_b128 v108, v[94:97] offset:26624\n"
    "ds_write_b128 v108, v[98:101] offset:27648\n"
    "s_waitcnt vmcnt(4) lgkmcnt(0)\n"
    "s_barrier\n"
    "ds_read_b64_tr_b16 v[86:87], v22 offset:49152\n"
    "ds_read_b64_tr_b16 v[94:95], v23 offset:49152\n"
    "ds_read_b64_tr_b16 v[88:89], v22 offset:50176\n"
    "ds_read_b64_tr_b16 v[96:97], v23 offset:50176\n"
    "ds_read_b64_tr_b16 v[90:91], v22 offset:57344\n"
    "ds_read_b64_tr_b16 v[98:99], v23 offset:57344\n"
    "ds_read_b64_tr_b16 v[92:93], v22 offset:58368\n"
    "ds_read_b64_tr_b16 v[100:101], v23 offset:58368\n"
    "ds_write_b128 v108, v[70:73] offset:40960\n"
    "ds_write_b128 v108, v[74:77] offset:41984\n"
    "ds_write_b128 v108, v[78:81] offset:43008\n"
    "ds_write_b128 v108, v[82:85] offset:44032\n"
    "s_waitcnt lgkmcnt(0)\n"
    "s_barrier\n"
    "ds_write_b128 v108, v[86:89] offset:57344\n"
    "ds_write_b128 v108, v[90:93] offset:58368\n"
    "ds_write_b128 v108, v[94:97] offset:59392\n"
    "ds_write_b128 v108, v[98:101] offset:60416\n"
    "ds_write_b128 v109, v[16:19] offset:8192\n"
    "s_waitcnt vmcnt(1)\n"
    "v_min_u32 v7, v7, v8\n"
    "v_cmp_gt_u32 vcc, 2, v7\n"
    "s_cmp_eq_u64 vcc, 0\n"
    "s_cselect_b32 s68, s68, 0\n"
    "s_waitcnt vmcnt(0)\n"
    "s_cmp_lg_u32 s68, 0\n"
    "s_cbranch_scc1 RF02_%=\n"
    "v_readfirstlane_b32 s46, v67\n"
    "s_cmp_lg_u32 s24, 0\n"
    "s_cselect_b32 s46, %2, s46\n"
    "s_mov_b32 s45, 0\n"
    "RBW2_%=:\n"
    "buffer_load_dwordx2 v[66:67], off, s[20:23], s63 sc1\n"
    "s_add_u32 s45, s45, 1\n"
    "s_cmp_gt_u32 s45, 262144\n"
    "s_cbranch_scc1 REND_%=\n"
    "s_waitcnt vmcnt(0)\n"
    "v_readfirstlane_b32 s62, v66\n"
    "v_cmp_eq_u32 vcc, s46, v67\n"
    "s_cbranch_vccnz RBW2_%=\n"
    "s_bitcmp1_b32 s62, 1\n"
    "s_cbranch_scc0 RBW2_%=\n"
    "s_xor_b32 s62, s62, s24\n"
    "s_and_b32 s62, s62, 1\n"
    "s_lshl_b32 s62, s62, 13\n"
    "v_lshrrev_b32 v4, 2, v0\n"
    "v_and_b32 v5, 3, v0\n"
    "v_lshl_add_u32 v4, v4, 3, v5\n"
    "v_lshlrev_b32 v4, 7, v4\n"
    "s_mov_b32 s45, 0\n"
    "RIW2_%=:\n"
    "s_mov_b32 s46, 81793536\n"
    "buffer_load_dword v5, v4, s[20:23], s46 offen sc1\n"
    "s_mov_b32 s46, 81809920\n"
    "buffer_load_dword v6, v4, s[20:23], s46 offen sc1\n"
    "s_add_u32 s45, s45, 1\n"
    "s_cmp_gt_u32 s45, 262144\n"
    "s_cbranch_scc1 REND_%=\n"
    "s_waitcnt vmcnt(0)\n"
    "v_min_u32 v5, v5, v6\n"
    "v_cmp_gt_u32 vcc, 2, v5\n"
    "s_cbranch_vccnz RIW2_%=\n"
    "s_add_u32 s30, s62, 0\n"
    "s_add_u32 s31, s30, 4096\n"
    "s_lshl_b32 s47, s25, 8\n"
    "s_add_u32 s36, s62, s47\n"
    "s_mov_b32 s50, 0\n"
    "s_mov_b32 s28, s30\n"
    "s_add_u32 s32, s62, s47\n"
    "s_add_u32 s32, s32, 67371008\n"
    "s_add_u32 s33, s32, 65536\n"
    "s_lshl_b32 s47, s25, 14\n"
    "s_add_u32 s34, s62, s47\n"
    "s_add_u32 s34, s34, 67108864\n"
    "s_add_u32 s35, s34, 4096\n"
    "s_add_u32 s44, s34, 4198400\n"
    "s_lshl_b32 s39, s25, 15\n"
    "s_add_u32 s39, s39, s62\n"
    "s_add_u32 s39, s39, 16793600\n"
    "s_xor_b32 s69, s62, 0x2000\n"
    "s_lshr_b32 s41, s25, 3\n"
    "s_lshl_b32 s41, s41, 14\n"
    "s_and_b32 s47, s25, 7\n"
    "s_lshl_b32 s47, s47, 10\n"
    "s_add_u32 s41, s41, s47\n"
    "s_add_u32 s41, s41, s69\n"
    "s_add_u32 s41, s41, 3145984\n"
    "s_add_u32 s69, s69, 3309568\n"
    "s_mov_b32 s37, 0x7f000000\n"
    "s_mov_b32 s42, 0\n"
    "s_cmp_eq_u32 s25, 0\n"
    "s_cselect_b32 s37, s69, s37\n"
    "s_add_u32 s69, s69, 16384\n"
    "s_cmp_eq_u32 s25, 11\n"
    "s_cselect_b32 s37, s69, s37\n"
    "s_add_u32 s69, s69, 16384\n"
    "s_cmp_eq_u32 s25, 22\n"
    "s_cselect_b32 s37, s69, s37\n"
    "s_add_u32 s69, s69, 16384\n"
    "v_mov_b32 v115, 0\n"
    "RF02_%=:\n"
    "v_cmp_gt_u32 vcc, 1, v115\n"
    "s_cbranch_vccz RF32_%=\n"
    "s_mov_b32 s45, 0\n"
    "RPK2_%=:\n"
    "buffer_load_dword v115, off, s[20:23], s41 sc1\n"
    "s_add_u32 s45, s45, 1\n"
    "s_cmp_gt_u32 s45, 0x40000\n"
    "s_cbranch_scc1 REND_%=\n"
    "s_waitcnt vmcnt(0)\n"
    "v_cmp_gt_u32 vcc, 1, v115\n"
    "s_cbranch_vccnz RPK2_%=\n"
    "RF32_%=:\n"
    "buffer_load_dwordx4 v[90:93], v107, s[20:23], s39 offen offset:0 sc1\n"
    "buffer_load_dwordx4 v[94:97], v107, s[20:23], s39 offen offset:1024 sc1\n"
    "buffer_load_dwordx4 v[98:101], v107, s[20:23], s39 offen offset:2048 sc1\n"
    "buffer_load_dwordx4 v[102:105], v107, s[20:23], s39 offen offset:3072 sc1\n"
    "s_mov_b32 s45, 0\n"
    "s_memrealtime s[86:87]\n"
    "s_waitcnt lgkmcnt(0)\n"
    "ds_read_b128 v[32:35], v108 offset:8192\n"
    "ds_read_b128 v[36:39], v108 offset:9216\n"
    "ds_read_b128 v[40:43], v108 offset:10240\n"
    "ds_read_b128 v[44:47], v108 offset:11264\n"
    "ds_read_b128 v[48:51], v108 offset:24576\n"
    "ds_read_b128 v[52:55], v108 offset:25600\n"
    "ds_read_b128 v[56:59], v108 offset:26624\n"
    "ds_read_b128 v[60:63], v108 offset:27648\n"
    "v_mov_b64 v[66:67], 0\n"
    "v_mov_b64 v[68:69], 0\n"
    "v_mov_b64 v[70:71], 0\n"
    "v_mov_b64 v[72:73], 0\n"
    "v_mov_b64 v[74:75], 0\n"
    "v_mov_b64 v[76:77], 0\n"
    "v_mov_b64 v[78:79], 0\n"
    "v_mov_b64 v[80:81], 0\n"
    "v_mov_b64 v[82:83], 0\n"
    "v_mov_b64 v[84:85], 0\n"
    "v_mov_b64 v[86:87], 0\n"
    "v_mov_b64 v[88:89], 0\n"
    "s_waitcnt lgkmcnt(0)\n"
    ".p2align 6\n"
    "RLP2_%=:\n"
    "buffer_load_dwordx4 v[0:3], v107, s[20:23], s31 offen offset:0 sc1\n"
    "s_memrealtime s[90:91]\n"
    "s_add_u32 s42, s42, 1\n"
    "s_add_u32 s43, s42, 1\n"
    "s_and_b32 s46, s42, 1\n"
    "s_lshl_b32 s46, s46, 9\n"
    "s_add_u32 s40, s41, s46\n"
    "s_and_b32 s46, s42, 7\n"
    "s_lshl_b32 s46, s46, 20\n"
    "s_add_u32 s38, s39, s46\n"
    "s_nop 4\n"
    "buffer_load_dwordx4 v[4:7], v107, s[20:23], s31 offen offset:1024 sc1\n"
    "s_lshr_b32 s46, s38, 8\n"
    "s_and_b32 s46, s46, 0x2000\n"
    "s_xor_b32 s38, s38, s46\n"
    "s_cmp_eq_u32 s42, 63\n"
    "s_cselect_b32 s43, 0, s43\n"
    "s_nop 8\n"
    "buffer_load_dwordx4 v[8:11], v107, s[20:23], s31 offen offset:2048 sc1\n"
    "s_nop 13\n"
    "buffer_load_dwordx4 v[12:15], v107, s[20:23], s31 offen offset:3072 sc1\n"
    "s_nop 13\n"
    "buffer_load_dwordx4 v[16:19], v107, s[20:23], s30 offen offset:0 sc1\n"
    "s_waitcnt lgkmcnt(0)\n"
    "s_nop 12\n"
    "buffer_load_dwordx4 v[20:23], v107, s[20:23], s30 offen offset:1024 sc1\n"
    "s_nop 13\n"
    "buffer_load_dwordx4 v[24:27], v107, s[20:23], s30 offen offset:2048 sc1\n"
    "s_nop 13\n"
    "buffer_load_dwordx4 v[28:31], v107, s[20:23], s30 offen offset:3072 sc1\n"
    "RRB2_%=:\n"
    "s_waitcnt vmcnt(7)\n"
    "v_smfmac_f32_16x16x64_bf16 v[66:69], v[0:3], a[0:7], v106\n"
    "v_smfmac_f32_16x16x64_bf16 v[70:73], v[0:3], v[32:39], v106\n"
    "ds_read_b128 v[32:35], v108 offset:40960\n"
    "v_smfmac_f32_16x16x64_bf16 v[74:77], v[0:3], a[64:71], v106\n"
    "ds_read_b128 v[36:39], v108 offset:41984\n"
    "v_smfmac_f32_16x16x64_bf16 v[78:81], v[0:3], v[40:47], v106\n"
    "ds_read_b128 v[40:43], v108 offset:43008\n"
    "s_waitcnt vmcnt(6)\n"
    "v_smfmac_f32_16x16x64_bf16 v[66:69], v[4:7], a[8:15], v106\n"
    "ds_read_b128 v[44:47], v108 offset:44032\n"
    "v_smfmac_f32_16x16x64_bf16 v[70:73], v[4:7], v[48:55], v106\n"
    "ds_read_b128 v[48:51], v108 offset:57344\n"
    "v_max_u32 v116, v0, v2\n"
    "v_smfmac_f32_16x16x64_bf16 v[74:77], v[4:7], a[72:79], v106\n"
    "ds_read_b128 v[52:55], v108 offset:58368\n"
    "v_smfmac_f32_16x16x64_bf16 v[78:81], v[4:7], v[56:63], v106\n"
    "ds_read_b128 v[56:59], v108 offset:59392\n"
    "s_waitcnt vmcnt(5)\n"
    "v_smfmac_f32_16x16x64_bf16 v[66:69], v[8:11], a[16:23], v106\n"
    "ds_read_b128 v[60:63], v108 offset:60416\n"
    "s_waitcnt lgkmcnt(6)\n"
    "v_smfmac_f32_16x16x64_bf16 v[70:73], v[8:11], v[32:39], v106\n"
    "ds_read_b128 v[32:35], v107 offset:8192\n"
    "v_max3_u32 v116, v116, v4, v6\n"
    "v_smfmac_f32_16x16x64_bf16 v[74:77], v[8:11], a[80:87], v106\n"
    "ds_read_b128 v[36:39], v107 offset:9216\n"
    "s_waitcnt lgkmcnt(6)\n"
    "v_smfmac_f32_16x16x64_bf16 v[78:81], v[8:11], v[40:47], v106\n"
    "ds_read_b128 v[40:43], v107 offset:10240\n"
    "s_waitcnt vmcnt(4)\n"
    "v_smfmac_f32_16x16x64_bf16 v[66:69], v[12:15], a[24:31], v106\n"
    "ds_read_b128 v[44:47], v107 offset:11264\n"
    "s_waitcnt lgkmcnt(6)\n"
    "v_smfmac_f32_16x16x64_bf16 v[70:73], v[12:15], v[48:55], v106\n"
    "ds_read_b128 v[48:51], v107 offset:24576\n"
    "v_max3_u32 v116, v116, v8, v10\n"
    "v_smfmac_f32_16x16x64_bf16 v[74:77], v[12:15], a[88:95], v106\n"
    "ds_read_b128 v[52:55], v107 offset:25600\n"
    "s_waitcnt lgkmcnt(6)\n"
    "v_smfmac_f32_16x16x64_bf16 v[78:81], v[12:15], v[56:63], v106\n"
    "ds_read_b128 v[56:59], v107 offset:26624\n"
    "s_waitcnt vmcnt(3)\n"
    "v_smfmac_f32_16x16x64_bf16 v[66:69], v[16:19], a[32:39], v106\n"
    "ds_read_b128 v[60:63], v107 offset:27648\n"
    "s_waitcnt lgkmcnt(6)\n"
    "v_smfmac_f32_16x16x64_bf16 v[70:73], v[16:19], v[32:39], v106\n"
    "ds_read_b128 v[32:35], v107 offset:40960\n"
    "v_max3_u32 v116, v116, v12, v14\n"
    "v_smfmac_f32_16x16x64_bf16 v[74:77], v[16:19], a[96:103], v106\n"
    "ds_read_b128 v[36:39], v107 offset:41984\n"
    "s_waitcnt lgkmcnt(6)\n"
    "v_smfmac_f32_16x16x64_bf16 v[78:81], v[16:19], v[40:47], v106\n"
    "ds_read_b128 v[40:43], v107 offset:43008\n"
    "s_waitcnt vmcnt(2)\n"
    "v_smfmac_f32_16x16x64_bf16 v[66:69], v[20:23], a[40:47], v106\n"
    "ds_read_b128 v[44:47], v107 offset:44032\n"
    "s_waitcnt lgkmcnt(6)\n"
    "v_smfmac_f32_16x16x64_bf16 v[70:73], v[20:23], v[48:55], v106\n"
    "ds_read_b128 v[48:51], v107 offset:57344\n"
    "v_max3_u32 v116, v116, v16, v18\n"
    "v_smfmac_f32_16x16x64_bf16 v[74:77], v[20:23], a[104:111], v106\n"
    "ds_read_b128 v[52:55], v107 offset:58368\n"
    "s_waitcnt lgkmcnt(6)\n"
    "v_smfmac_f32_16x16x64_bf16 v[78:81], v[20:23], v[56:63], v106\n"
    "ds_read_b128 v[56:59], v107 offset:59392\n"
    "s_waitcnt vmcnt(1)\n"
    "v_smfmac_f32_16x16x64_bf16 v[66:69], v[24:27], a[48:55], v106\n"
    "ds_read_b128 v[60:63], v107 offset:60416\n"
    "v_smfmac_f32_16x16x64_bf16 v[70:73], v[24:27], a[128:135], v106\n"
    "v_max3_u32 v116, v116, v20, v22\n"
    "v_smfmac_f32_16x16x64_bf16 v[74:77], v[24:27], a[112:119], v106\n"
    "v_max3_u32 v116, v116, v24, v26\n"
    "v_smfmac_f32_16x16x64_bf16 v[78:81], v[24:27], a[144:151], v106\n"
    "s_waitcnt vmcnt(0)\n"
    "v_smfmac_f32_16x16x64_bf16 v[66:69], v[28:31], a[56:63], v106\n"
    "v_max3_u32 v116, v116, v28, v30\n"
    "v_cmp_eq_u32 vcc, -1, v116\n"
    "v_smfmac_f32_16x16x64_bf16 v[70:73], v[28:31], a[136:143], v106\n"
    "s_cbranch_vccnz RRT2_%=\n"
    "v_smfmac_f32_16x16x64_bf16 v[74:77], v[28:31], a[120:127], v106\n"
    "s_nop 1\n"
    "s_nop 0\n"
    "v_cvt_pk_bf16_f32 v69, v68, v69\n"
    "v_smfmac_f32_16x16x64_bf16 v[78:81], v[28:31], a[152:159], v106\n"
    "s_nop 0\n"
    "v_cvt_pk_bf16_f32 v68, v66, v67\n"
    "v_cvt_pk_bf16_f32 v70, v70, v71\n"
    "v_cvt_pk_bf16_f32 v71, v72, v73\n"
    "v_cvt_pk_bf16_f32 v77, v76, v77\n"
    "v_cvt_pk_bf16_f32 v76, v74, v75\n"
    "v_mfma_f32_16x16x32_bf16 v[68:71], v[68:71], v[120:123], 0\n"
    "s_nop 0\n"
    "v_cvt_pk_bf16_f32 v78, v78, v79\n"
    "v_cvt_pk_bf16_f32 v79, v80, v81\n"
    "s_nop 1\n"
    "v_mfma_f32_16x16x32_bf16 v[76:79], v[76:79], v[120:123], 0\n"
    "s_waitcnt lgkmcnt(6)\n"
    "v_smfmac_f32_16x16x64_bf16 v[82:85], v[0:3], v[32:39], v106\n"
    "v_cvt_pk_bf16_f32 v72, v68, v69\n"
    "v_cvt_pk_bf16_f32 v73, v70, v71\n"
    "s_waitcnt lgkmcnt(4)\n"
    "v_smfmac_f32_16x16x64_bf16 v[86:89], v[0:3], v[40:47], v106\n"
    "s_nop 1\n"
    "v_cvt_pk_bf16_f32 v74, v76, v77\n"
    "v_cvt_pk_bf16_f32 v75, v78, v79\n"
    "s_nop 1\n"
    "v_mfma_f32_16x16x32_bf16 v[66:69], v[90:93], v[72:75], 0\n"
    "v_mfma_f32_16x16x32_bf16 v[78:81], v[94:97], v[72:75], 0\n"
    "s_nop 0\n"
    "v_mfma_f32_16x16x32_bf16 v[72:75], v[72:75], v[72:75], 0\n"
    "s_nop 1\n"
    "s_waitcnt lgkmcnt(2)\n"
    "v_smfmac_f32_16x16x64_bf16 v[82:85], v[4:7], v[48:55], v106\n"
    "s_nop 1\n"
    "v_mov_b32_dpp v66, v78 row_ror:8 row_mask:0xf bank_mask:0xc\n"
    "v_mov_b32_dpp v67, v79 row_ror:8 row_mask:0xf bank_mask:0xc\n"
    "v_mov_b32_dpp v68, v80 row_ror:8 row_mask:0xf bank_mask:0xc\n"
    "v_mov_b32_dpp v69, v81 row_ror:8 row_mask:0xf bank_mask:0xc\n"
    "ds_write_b128 v109, v[66:69] offset:2048\n"
    "v_mov_b32_dpp v72, v74 row_ror:8 row_mask:0xf bank_mask:0xc\n"
    "v_mov_b32_dpp v73, v75 row_ror:8 row_mask:0xf bank_mask:0xc\n"
    "ds_write_b64 v117, v[72:73] offset:1024\n"
    "s_waitcnt lgkmcnt(2)\n"
    "v_smfmac_f32_16x16x64_bf16 v[86:89], v[4:7], v[56:63], v106\n"
    "v_smfmac_f32_16x16x64_bf16 v[82:85], v[8:11], a[160:167], v106\n"
    "v_smfmac_f32_16x16x64_bf16 v[86:89], v[8:11], a[208:215], v106\n"
    "v_smfmac_f32_16x16x64_bf16 v[82:85], v[12:15], a[168:175], v106\n"
    "s_waitcnt lgkmcnt(0)\n"
    "s_barrier\n"
    "ds_read_b128 v[32:35], v109 offset:3072\n"
    "v_smfmac_f32_16x16x64_bf16 v[86:89], v[12:15], a[216:223], v106\n"
    "ds_read_b128 v[36:39], v109 offset:0\n"
    "v_smfmac_f32_16x16x64_bf16 v[82:85], v[16:19], a[176:183], v106\n"
    "ds_read_b128 v[40:43], v109 offset:1024\n"
    "v_smfmac_f32_16x16x64_bf16 v[86:89], v[16:19], a[224:231], v106\n"
    "ds_read_b128 v[44:47], v110 offset:0\n"
    "v_smfmac_f32_16x16x64_bf16 v[82:85], v[20:23], a[184:191], v106\n"
    "ds_read_b128 v[48:51], v110 offset:1024\n"
    "v_smfmac_f32_16x16x64_bf16 v[86:89], v[20:23], a[232:239], v106\n"
    "s_waitcnt lgkmcnt(2)\n"
    "v_pk_add_f32 v[66:67], v[66:67], v[32:33]\n"
    "v_pk_add_f32 v[68:69], v[68:69], v[34:35]\n"
    "v_pk_add_f32 v[66:67], v[66:67], v[36:37]\n"
    "v_pk_add_f32 v[68:69], v[68:69], v[38:39]\n"
    "v_pk_add_f32 v[66:67], v[66:67], v[40:41]\n"
    "v_pk_add_f32 v[68:69], v[68:69], v[42:43]\n"
    "s_waitcnt lgkmcnt(0)\n"
    "v_pk_add_f32 v[70:71], v[44:45], v[46:47]\n"
    "v_pk_add_f32 v[70:71], v[70:71], v[48:49]\n"
    "v_pk_add_f32 v[70:71], v[70:71], v[50:51]\n"
    "v_max3_f32 v72, v66, v67, v68\n"
    "v_max3_f32 v72, v72, v69, v70\n"
    "v_max_f32 v72, v72, v71\n"
    "v_smfmac_f32_16x16x64_bf16 v[82:85], v[24:27], a[192:199], v106\n"
    "s_nop 0\n"
    "v_max_f32_dpp v73, v72, v72 row_ror:8 row_mask:0xf bank_mask:0xf\n"
    "v_max_f32_dpp v72, v72, v72 row_ror:8 row_mask:0xf bank_mask:0xf\n"
    "v_smfmac_f32_16x16x64_bf16 v[86:89], v[24:27], a[240:247], v106\n"
    "s_nop 0\n"
    "v_permlane16_swap_b32 v72, v73\n"
    "v_max_f32 v74, v72, v73\n"
    "v_max_f32 v72, v72, v73\n"
    "v_smfmac_f32_16x16x64_bf16 v[82:85], v[28:31], a[200:207], v106\n"
    "s_nop 0\n"
    "v_permlane32_swap_b32 v72, v74\n"
    "v_max_f32 v72, v72, v74\n"
    "v_mul_f32 v72, s48, v72\n"
    "v_fma_f32 v66, v66, s48, -v72\n"
    "v_fma_f32 v67, v67, s48, -v72\n"
    "v_fma_f32 v68, v68, s48, -v72\n"
    "v_fma_f32 v69, v69, s48, -v72\n"
    "v_exp_f32 v66, v66\n"
    "v_exp_f32 v67, v67\n"
    "v_exp_f32 v68, v68\n"
    "v_cvt_pk_bf16_f32 v62, v66, v67\n"
    "v_exp_f32 v69, v69\n"
    "v_smfmac_f32_16x16x64_bf16 v[86:89], v[28:31], a[248:255], v106\n"
    "v_mov_b32_dpp v60, v62 row_ror:8 row_mask:0xf bank_mask:0xf\n"
    "v_cvt_pk_bf16_f32 v63, v68, v69\n"
    "v_fma_f32 v70, v70, s48, -v72\n"
    "v_fma_f32 v71, v71, s48, -v72\n"
    "v_mov_b32_dpp v61, v63 row_ror:8 row_mask:0xf bank_mask:0xf\n"
    "v_exp_f32 v70, v70\n"
    "v_exp_f32 v71, v71\n"
    "v_mfma_f32_16x16x32_bf16 v[36:39], v[98:101], v[60:63], 0\n"
    "v_cvt_pk_bf16_f32 v64, v70, v71\n"
    "v_cvt_pk_bf16_f32 v82, v82, v83\n"
    "v_mfma_f32_16x16x32_bf16 v[40:43], v[102:105], v[60:63], 0\n"
    "v_cvt_pk_bf16_f32 v83, v84, v85\n"
    "v_cvt_pk_bf16_f32 v84, v88, v89\n"
    "v_mfma_f32_16x16x32_bf16 v[32:35], v[124:127], v[62:65], 0\n"
    "v_mov_b32_dpp v65, v64 row_ror:8 row_mask:0xf bank_mask:0xf\n"
    "v_cvt_pk_bf16_f32 v85, v86, v87\n"
    "s_nop 0\n"
    "v_mfma_f32_16x16x16_bf16 v[36:39], v[82:83], v[64:65], v[36:39]\n"
    "s_nop 0\n"
    "s_nop 1\n"
    "v_mfma_f32_16x16x16_bf16 v[40:43], v[84:85], v[64:65], v[40:43]\n"
    "s_nop 0\n"
    "v_add_f32_dpp v32, v32, v32 row_ror:8 row_mask:0xf bank_mask:0xf\n"
    "v_rcp_f32 v32, v32\n"
    "s_nop 0\n"
    "v_pk_mul_f32 v[36:37], v[36:37], v[32:33] op_sel_hi:[1,0]\n"
    "v_pk_mul_f32 v[38:39], v[38:39], v[32:33] op_sel_hi:[1,0]\n"
    "s_mov_b64 exec, s[98:99]\n"
    "s_nop 0\n"
    "v_pk_mul_f32 v[36:37], v[40:41], v[32:33] op_sel_hi:[1,0]\n"
    "v_pk_mul_f32 v[38:39], v[42:43], v[32:33] op_sel_hi:[1,0]\n"
    "s_mov_b64 exec, -1\n"
    "v_cvt_pk_bf16_f32 v0, v36, v37\n"
    "v_cvt_pk_bf16_f32 v1, v38, v39\n"
    "ds_write_b64 v111, v[0:1] offset:1024\n"
    "v_mov_b32_dpp v2, v0 row_ror:8 row_mask:0xf bank_mask:0xf\n"
    "v_mov_b32_dpp v3, v1 row_ror:8 row_mask:0xf bank_mask:0xf\n"
    "v_mov_b64 v[16:17], 0\n"
    "v_mov_b64 v[18:19], 0\n"
    "v_mov_b64 v[20:21], 0\n"
    "v_mov_b64 v[22:23], 0\n"
    "v_mov_b64 v[24:25], 0\n"
    "v_mov_b64 v[26:27], 0\n"
    "v_mov_b64 v[28:29], 0\n"
    "v_mov_b64 v[30:31], 0\n"
    "v_smfmac_f32_16x16x64_bf16 v[16:19], v[0:3], v[128:135], v106\n"
    "s_waitcnt lgkmcnt(0)\n"
    "s_barrier\n"
    "ds_read_b128 v[4:7], v112 offset:1536\n"
    "ds_read_b128 v[8:11], v112 offset:0\n"
    "ds_read_b128 v[12:15], v112 offset:512\n"
    "v_smfmac_f32_16x16x64_bf16 v[20:23], v[0:3], v[160:167], v106\n"
    "v_smfmac_f32_16x16x64_bf16 v[24:27], v[0:3], v[192:199], v106\n"
    "v_smfmac_f32_16x16x64_bf16 v[28:31], v[0:3], v[224:231], v106\n"
    "s_waitcnt lgkmcnt(2)\n"
    "v_smfmac_f32_16x16x64_bf16 v[16:19], v[4:7], v[136:143], v106\n"
    "v_smfmac_f32_16x16x64_bf16 v[20:23], v[4:7], v[168:175], v106\n"
    "s_waitcnt lgkmcnt(1)\n"
    "v_smfmac_f32_16x16x64_bf16 v[16:19], v[8:11], v[144:151], v106\n"
    "v_smfmac_f32_16x16x64_bf16 v[20:23], v[8:11], v[176:183], v106\n"
    "s_waitcnt lgkmcnt(0)\n"
    "v_smfmac_f32_16x16x64_bf16 v[16:19], v[12:15], v[152:159], v106\n"
    "buffer_load_dword v115, off, s[20:23], s40 sc1\n"
    "v_smfmac_f32_16x16x64_bf16 v[20:23], v[12:15], v[184:191], v106\n"
    "s_nop 2\n"
    "v_smfmac_f32_16x16x64_bf16 v[24:27], v[4:7], v[200:207], v106\n"
    "s_nop 1\n"
    "v_cvt_pk_bf16_f32 v19, v18, v19\n"
    "v_smfmac_f32_16x16x64_bf16 v[24:27], v[8:11], v[208:215], v106\n"
    "v_cvt_pk_bf16_f32 v18, v16, v17\n"
    "v_smfmac_f32_16x16x64_bf16 v[24:27], v[12:15], v[216:223], v106\n"
    "v_or_b32 v18, 1, v18\n"
    "v_smfmac_f32_16x16x64_bf16 v[28:31], v[4:7], v[232:239], v106\n"
    "v_cvt_pk_bf16_f32 v20, v20, v21\n"
    "v_smfmac_f32_16x16x64_bf16 v[28:31], v[8:11], v[240:247], v106\n"
    "v_cvt_pk_bf16_f32 v21, v22, v23\n"
    "v_or_b32 v20, 1, v20\n"
    "buffer_store_dwordx4 v[18:21], v113, s[20:23], s32 offen sc1\n"
    "v_smfmac_f32_16x16x64_bf16 v[28:31], v[12:15], v[248:255], v106\n"
    "s_nop 1\n"
    "v_cvt_pk_bf16_f32 v27, v26, v27\n"
    "v_cvt_pk_bf16_f32 v26, v24, v25\n"
    "v_or_b32 v26, 1, v26\n"
    "s_nop 2\n"
    "v_cvt_pk_bf16_f32 v28, v28, v29\n"
    "v_cvt_pk_bf16_f32 v29, v30, v31\n"
    "v_or_b32 v28, 1, v28\n"
    "buffer_store_dwordx4 v[26:29], v113, s[20:23], s33 offen sc1\n"
    "s_mov_b32 s45, 0\n"
    "s_add_u32 s36, s36, 16384\n"
    "s_bitcmp1_b32 s32, 19\n"
    "s_cselect_b32 s46, 4194304, 0\n"
    "s_bitset1_b32 s46, 19\n"
    "s_xor_b32 s32, s32, s46\n"
    "s_add_u32 s33, s32, 65536\n"
    "ds_read_b128 v[32:35], v108 offset:8192\n"
    "ds_read_b128 v[36:39], v108 offset:9216\n"
    "ds_read_b128 v[40:43], v108 offset:10240\n"
    "ds_read_b128 v[44:47], v108 offset:11264\n"
    "ds_read_b128 v[48:51], v108 offset:24576\n"
    "ds_read_b128 v[52:55], v108 offset:25600\n"
    "ds_read_b128 v[56:59], v108 offset:26624\n"
    "ds_read_b128 v[60:63], v108 offset:27648\n"
    "v_mov_b64 v[66:67], 0\n"
    "v_mov_b64 v[68:69], 0\n"
    "v_mov_b64 v[70:71], 0\n"
    "v_mov_b64 v[72:73], 0\n"
    "v_mov_b64 v[74:75], 0\n"
    "v_mov_b64 v[76:77], 0\n"
    "v_mov_b64 v[78:79], 0\n"
    "v_mov_b64 v[80:81], 0\n"
    "v_mov_b64 v[82:83], 0\n"
    "v_mov_b64 v[84:85], 0\n"
    "v_mov_b64 v[86:87], 0\n"
    "v_mov_b64 v[88:89], 0\n"
    "s_waitcnt vmcnt(2)\n"
    "v_cmp_gt_u32 vcc, s43, v115\n"
    "s_cbranch_vccnz RKW2_%=\n"
    "RKB2_%=:\n"
    "buffer_load_dwordx4 v[90:93], v107, s[20:23], s38 offen offset:0 sc1\n"
    "buffer_load_dwordx4 v[94:97], v107, s[20:23], s38 offen offset:1024 sc1\n"
    "buffer_load_dwordx4 v[98:101], v107, s[20:23], s38 offen offset:2048 sc1\n"
    "buffer_load_dwordx4 v[102:105], v107, s[20:23], s38 offen offset:3072 sc1\n"
    "s_add_u32 s51, s36, 32768\n"
    "s_cmp_gt_u32 s42, 61\n"
    "s_cselect_b32 s51, 0x7f000000, s51\n"
    "v_mov_b64 v[0:1], -1\n"
    "buffer_store_dwordx2 v[0:1], v114, s[20:23], s51 offen sc1\n"
    "s_mov_b32 s45, 0\n"
    "s_add_u32 s37, s37, 128\n"
    "s_barrier\n"
    "ds_read_b64 v[118:119], v109 offset:9216\n"
    "s_waitcnt lgkmcnt(0)\n"
    "v_readfirstlane_b32 s30, v119\n"
    "s_add_u32 s31, s30, 4096\n"
    "s_cmp_lt_u32 s42, 63\n"
    "s_barrier\n"
    "s_cbranch_scc1 RLP2_%=\n"
    "s_branch REND_%=\n"
    "RRT2_%=:\n"
    "s_add_u32 s85, s90, 52\n"
    "s_add_u32 s45, s45, 1\n"
    "s_cmp_gt_u32 s45, 0x40000\n"
    "s_cbranch_scc1 REND_%=\n"
    "s_mov_b32 s50, 56\n"
    "s_or_b32 s86, s30, 1048576\n"
    "s_or_b32 s87, s31, 1048576\n"
    "RT2_%=:\n"
    "s_memrealtime s[90:91]\n"
    "s_waitcnt lgkmcnt(0)\n"
    "s_sub_u32 s46, s90, s85\n"
    "s_cmp_lt_i32 s46, 0\n"
    "s_cbranch_scc1 RT2_%=\n"
    "buffer_load_dwordx4 v[0:3], v107, s[20:23], s87 offen offset:0 sc1\n"
    "ds_read_b128 v[32:35], v108 offset:8192\n"
    "ds_read_b128 v[36:39], v108 offset:9216\n"
    "ds_read_b128 v[40:43], v108 offset:10240\n"
    "ds_read_b128 v[44:47], v108 offset:11264\n"
    "s_nop 7\n"
    "buffer_load_dwordx4 v[4:7], v107, s[20:23], s87 offen offset:1024 sc1\n"
    "ds_read_b128 v[48:51], v108 offset:24576\n"
    "ds_read_b128 v[52:55], v108 offset:25600\n"
    "ds_read_b128 v[56:59], v108 offset:26624\n"
    "ds_read_b128 v[60:63], v108 offset:27648\n"
    "s_nop 7\n"
    "buffer_load_dwordx4 v[8:11], v107, s[20:23], s87 offen offset:2048 sc1\n"
    "v_mov_b64 v[66:67], 0\n"
    "v_mov_b64 v[68:69], 0\n"
    "v_mov_b64 v[70:71], 0\n"
    "v_mov_b64 v[72:73], 0\n"
    "s_nop 7\n"
    "buffer_load_dwordx4 v[12:15], v107, s[20:23], s87 offen offset:3072 sc1\n"
    "v_mov_b64 v[74:75], 0\n"
    "v_mov_b64 v[76:77], 0\n"
    "v_mov_b64 v[78:79], 0\n"
    "v_mov_b64 v[80:81], 0\n"
    "s_nop 7\n"
    "buffer_load_dwordx4 v[16:19], v107, s[20:23], s86 offen offset:0 sc1\n"
    "s_nop 13\n"
    "buffer_load_dwordx4 v[20:23], v107, s[20:23], s86 offen offset:1024 sc1\n"
    "s_nop 13\n"
    "buffer_load_dwordx4 v[24:27], v107, s[20:23], s86 offen offset:2048 sc1\n"
    "s_nop 13\n"
    "buffer_load_dwordx4 v[28:31], v107, s[20:23], s86 offen offset:3072 sc1\n"
    "s_waitcnt lgkmcnt(0)\n"
    "s_branch RRB2_%=\n"
    "RKW2_%=:\n"
    "s_mov_b32 s45, 0\n"
    "RK22_%=:\n"
    "buffer_load_dword v115, off, s[20:23], s40 sc1\n"
    "s_add_u32 s45, s45, 1\n"
    "s_cmp_gt_u32 s45, 0x40000\n"
    "s_cbranch_scc1 REND_%=\n"
    "s_waitcnt vmcnt(0)\n"
    "v_cmp_gt_u32 vcc, s43, v115\n"
    "s_cbranch_vccnz RK22_%=\n"
    "s_branch RKB2_%=\n"
    "RW3_%=:\n"
    "s_mov_b64 s[76:77], s[10:11]\n"
    "s_and_b32 s77, s77, 0xffff\n"
    "s_mov_b32 s78, 4194304\n"
    "s_mov_b32 s79, 0x20000\n"
    "s_mov_b32 m0, 12288\n"
    "s_add_u32 s80, s61, 98304\n"
    "buffer_load_dwordx4 v24, s[76:79], s80 offen offset:0  lds\n"
    "s_add_u32 s80, s61, 101376\n"
    "buffer_load_dwordx4 v24, s[76:79], s80 offen offset:1024  lds\n"
    "s_add_u32 s80, s61, 112640\n"
    "buffer_load_dwordx4 v25, s[76:79], s80 offen offset:2048  lds\n"
    "s_add_u32 s80, s61, 115712\n"
    "buffer_load_dwordx4 v25, s[76:79], s80 offen offset:3072  lds\n"
    "s_mov_b32 m0, 28672\n"
    "s_add_u32 s80, s61, 106496\n"
    "buffer_load_dwordx4 v24, s[76:79], s80 offen offset:0  lds\n"
    "s_add_u32 s80, s61, 109568\n"
    "buffer_load_dwordx4 v24, s[76:79], s80 offen offset:1024  lds\n"
    "s_add_u32 s80, s61, 120832\n"
    "buffer_load_dwordx4 v25, s[76:79], s80 offen offset:2048  lds\n"
    "s_add_u32 s80, s61, 123904\n"
    "buffer_load_dwordx4 v25, s[76:79], s80 offen offset:3072  lds\n"
    "s_mov_b32 m0, 45056\n"
    "s_add_u32 s80, s61, 0\n"
    "buffer_load_dwordx4 v24, s[76:79], s80 offen offset:0  lds\n"
    "s_add_u32 s80, s61, 3072\n"
    "buffer_load_dwordx4 v24, s[76:79], s80 offen offset:1024  lds\n"
    "s_add_u32 s80, s61, 14336\n"
    "buffer_load_dwordx4 v25, s[76:79], s80 offen offset:2048  lds\n"
    "s_add_u32 s80, s61, 17408\n"
    "buffer_load_dwordx4 v25, s[76:79], s80 offen offset:3072  lds\n"
    "s_mov_b32 m0, 61440\n"
    "s_add_u32 s80, s61, 8192\n"
    "buffer_load_dwordx4 v24, s[76:79], s80 offen offset:0  lds\n"
    "s_add_u32 s80, s61, 11264\n"
    "buffer_load_dwordx4 v24, s[76:79], s80 offen offset:1024  lds\n"
    "s_add_u32 s80, s61, 22528\n"
    "buffer_load_dwordx4 v25, s[76:79], s80 offen offset:2048  lds\n"
    "s_add_u32 s80, s61, 25600\n"
    "buffer_load_dwordx4 v25, s[76:79], s80 offen offset:3072  lds\n"
    "s_mov_b32 m0, 77824\n"
    "s_add_u32 s80, s61, 32768\n"
    "buffer_load_dwordx4 v24, s[76:79], s80 offen offset:0  lds\n"
    "s_add_u32 s80, s61, 35840\n"
    "buffer_load_dwordx4 v24, s[76:79], s80 offen offset:1024  lds\n"
    "s_add_u32 s80, s61, 47104\n"
    "buffer_load_dwordx4 v25, s[76:79], s80 offen offset:2048  lds\n"
    "s_add_u32 s80, s61, 50176\n"
    "buffer_load_dwordx4 v25, s[76:79], s80 offen offset:3072  lds\n"
    "s_mov_b32 m0, 94208\n"
    "s_add_u32 s80, s61, 40960\n"
    "buffer_load_dwordx4 v24, s[76:79], s80 offen offset:0  lds\n"
    "s_add_u32 s80, s61, 44032\n"
    "buffer_load_dwordx4 v24, s[76:79], s80 offen offset:1024  lds\n"
    "s_add_u32 s80, s61, 55296\n"
    "buffer_load_dwordx4 v25, s[76:79], s80 offen offset:2048  lds\n"
    "s_add_u32 s80, s61, 58368\n"
    "buffer_load_dwordx4 v25, s[76:79], s80 offen offset:3072  lds\n"
    "s_mov_b32 m0, 110592\n"
    "s_add_u32 s80, s61, 65536\n"
    "buffer_load_dwordx4 v24, s[76:79], s80 offen offset:0  lds\n"
    "s_add_u32 s80, s61, 68608\n"
    "buffer_load_dwordx4 v24, s[76:79], s80 offen offset:1024  lds\n"
    "s_add_u32 s80, s61, 79872\n"
    "buffer_load_dwordx4 v25, s[76:79], s80 offen offset:2048  lds\n"
    "s_add_u32 s80, s61, 82944\n"
    "buffer_load_dwordx4 v25, s[76:79], s80 offen offset:3072  lds\n"
    "s_mov_b32 m0, 126976\n"
    "s_add_u32 s80, s61, 73728\n"
    "buffer_load_dwordx4 v24, s[76:79], s80 offen offset:0  lds\n"
    "s_add_u32 s80, s61, 76800\n"
    "buffer_load_dwordx4 v24, s[76:79], s80 offen offset:1024  lds\n"
    "s_add_u32 s80, s61, 88064\n"
    "buffer_load_dwordx4 v25, s[76:79], s80 offen offset:2048  lds\n"
    "s_add_u32 s80, s61, 91136\n"
    "buffer_load_dwordx4 v25, s[76:79], s80 offen offset:3072  lds\n"
    "s_bfe_u32 s24, %1, 0x10000\n"
    "s_and_b32 s26, s16, 0xffff\n"
    "s_and_b32 s27, s17, 0xffff\n"
    "s_mov_b32 s48, 0x3e0293ee\n"
    "s_mov_b32 s96, 0x00ff00ff\n"
    "s_mov_b32 s97, s96\n"
    "s_not_b64 s[98:99], s[96:97]\n"
    "v_lshlrev_b32 v107, 4, v0\n"
    "v_mov_b64 v[64:65], 0\n"
    "v_lshlrev_b32 v5, 4, v0\n"
    "v_add_u32 v117, 135168, v5\n"
    "v_and_b32 v3, 7, v0\n"
    "v_bfe_u32 v4, v0, 3, 1\n"
    "v_mov_b32 v5, 0x4444\n"
    "v_mov_b32 v6, 0xeeee\n"
    "v_cmp_eq_u32 vcc, 1, v4\n"
    "s_nop 1\n"
    "v_cndmask_b32 v106, v5, v6, vcc\n"
    "v_add_u32 v108, 0x10000, v107\n"
    "v_add_u32 v109, 131072, v107\n"
    "v_and_b32 v5, 31, v0\n"
    "v_lshlrev_b32 v5, 4, v5\n"
    "v_add_u32 v110, 135680, v5\n"
    "v_lshl_add_u32 v5, v2, 3, v3\n"
    "v_lshlrev_b32 v5, 4, v5\n"
    "v_add_u32 v112, 137216, v5\n"
    "v_lshl_add_u32 v111, v4, 3, v112\n"
    "v_lshrrev_b32 v5, 1, v2\n"
    "v_lshl_add_u32 v6, v5, 1, v4\n"
    "v_lshlrev_b32 v6, 14, v6\n"
    "v_and_b32 v7, 1, v2\n"
    "v_lshl_add_u32 v8, v7, 3, v3\n"
    "v_lshl_add_u32 v113, v8, 4, v6\n"
    "v_mul_u32_u24 v8, 2176, v7\n"
    "v_lshl_add_u32 v8, v5, 10, v8\n"
    "v_lshrrev_b32 v9, 1, v1\n"
    "v_lshl_add_u32 v26, v9, 3, v8\n"
    "v_xor_b32 v27, 128, v26\n"
    "v_xor_b32 v28, 2176, v26\n"
    "v_xor_b32 v29, 2048, v26\n"
    "v_and_b32 v10, 1, v0\n"
    "v_mul_u32_u24 v8, 0x020202, v10\n"
    "v_lshl_add_u32 v8, v10, 25, v8\n"
    "v_add_u32 v68, 0x05040100, v8\n"
    "v_and_b32 v8, 3, v0\n"
    "v_lshl_add_u32 v8, v5, 2, v8\n"
    "v_bfe_u32 v9, v0, 2, 1\n"
    "v_lshl_add_u32 v8, v9, 3, v8\n"
    "v_lshlrev_b32 v8, 4, v8\n"
    "v_lshl_add_u32 v8, v7, 3, v8\n"
    "v_lshl_add_u32 v114, v4, 20, v8\n"
    "v_lshlrev_b32 v10, 4, v10\n"
    "v_mov_b32 v11, 0x3f80\n"
    "v_lshlrev_b32 v11, v10, v11\n"
    "v_cmp_eq_u32 vcc, v9, v7\n"
    "s_nop 1\n"
    "v_cndmask_b32 v12, 0, v11, vcc\n"
    "v_bfe_u32 v13, v0, 1, 1\n"
    "v_lshl_add_u32 v13, v4, 1, v13\n"
    "v_cmp_eq_u32 vcc, 0, v13\n"
    "s_nop 1\n"
    "v_cndmask_b32 v120, 0, v12, vcc\n"
    "v_cmp_eq_u32 vcc, 1, v13\n"
    "s_nop 1\n"
    "v_cndmask_b32 v121, 0, v12, vcc\n"
    "v_cmp_eq_u32 vcc, 2, v13\n"
    "s_nop 1\n"
    "v_cndmask_b32 v122, 0, v12, vcc\n"
    "v_cmp_eq_u32 vcc, 3, v13\n"
    "s_nop 1\n"
    "v_cndmask_b32 v123, 0, v12, vcc\n"
    "v_bfe_u32 v13, v0, 1, 2\n"
    "v_cmp_eq_u32 vcc, 0, v13\n"
    "s_nop 1\n"
    "v_cndmask_b32 v16, 0, v11, vcc\n"
    "v_cmp_eq_u32 vcc, 1, v13\n"
    "s_nop 1\n"
    "v_cndmask_b32 v17, 0, v11, vcc\n"
    "v_cmp_eq_u32 vcc, 2, v13\n"
    "s_nop 1\n"
    "v_cndmask_b32 v18, 0, v11, vcc\n"
    "v_cmp_eq_u32 vcc, 3, v13\n"
    "s_nop 1\n"
    "v_cndmask_b32 v19, 0, v11, vcc\n"
    "v_mov_b32 v124, 0x3f803f80\n"
    "v_mov_b32 v125, 0x3f803f80\n"
    "v_mov_b32 v126, 0x3f003f00\n"
    "v_mov_b32 v127, 0\n"
    "v_mov_b64 v[118:119], 0\n"
    "s_waitcnt vmcnt(28) lgkmcnt(0)\n"
    "s_barrier\n"
    "ds_read_b64 v[70:71], v26 offset:12288\n"
    "ds_read_b64 v[72:73], v26 offset:12544\n"
    "ds_read_b64 v[74:75], v26 offset:12800\n"
    "ds_read_b64 v[76:77], v26 offset:13056\n"
    "ds_read_b64 v[78:79], v28 offset:12352\n"
    "ds_read_b64 v[80:81], v28 offset:12608\n"
    "ds_read_b64 v[82:83], v28 offset:12864\n"
    "ds_read_b64 v[84:85], v28 offset:13120\n"
    "ds_read_b64 v[86:87], v27 offset:12288\n"
    "ds_read_b64 v[88:89], v27 offset:12544\n"
    "ds_read_b64 v[90:91], v27 offset:12800\n"
    "ds_read_b64 v[92:93], v27 offset:13056\n"
    "ds_read_b64 v[94:95], v29 offset:12352\n"
    "ds_read_b64 v[96:97], v29 offset:12608\n"
    "ds_read_b64 v[98:99], v29 offset:12864\n"
    "ds_read_b64 v[100:101], v29 offset:13120\n"
    "s_waitcnt lgkmcnt(14)\n"
    "v_perm_b32 v128, v72, v70, v68\n"
    "v_perm_b32 v160, v73, v71, v68\n"
    "s_waitcnt lgkmcnt(12)\n"
    "v_perm_b32 v130, v76, v74, v68\n"
    "v_perm_b32 v162, v77, v75, v68\n"
    "s_waitcnt lgkmcnt(10)\n"
    "v_perm_b32 v129, v80, v78, v68\n"
    "v_perm_b32 v161, v81, v79, v68\n"
    "s_waitcnt lgkmcnt(8)\n"
    "v_perm_b32 v131, v84, v82, v68\n"
    "v_perm_b32 v163, v85, v83, v68\n"
    "s_waitcnt lgkmcnt(6)\n"
    "v_perm_b32 v192, v88, v86, v68\n"
    "v_perm_b32 v224, v89, v87, v68\n"
    "s_waitcnt lgkmcnt(4)\n"
    "v_perm_b32 v194, v92, v90, v68\n"
    "v_perm_b32 v226, v93, v91, v68\n"
    "s_waitcnt lgkmcnt(2)\n"
    "v_perm_b32 v193, v96, v94, v68\n"
    "v_perm_b32 v225, v97, v95, v68\n"
    "s_waitcnt lgkmcnt(0)\n"
    "v_perm_b32 v195, v100, v98, v68\n"
    "v_perm_b32 v227, v101, v99, v68\n"
    "s_waitcnt vmcnt(24) lgkmcnt(0)\n"
    "s_barrier\n"
    "s_mov_b64 s[76:77], s[4:5]\n"
    "s_and_b32 s77, s77, 0xffff\n"
    "s_mov_b32 s78, 4194304\n"
    "s_mov_b32 s79, 0x20000\n"
    "s_mov_b32 m0, 12288\n"
    "s_add_u32 s80, s60, 0\n"
    "buffer_load_dwordx4 v20, s[76:79], s80 offen offset:0  lds\n"
    "s_add_u32 s80, s60, 31744\n"
    "buffer_load_dwordx4 v20, s[76:79], s80 offen offset:1024  lds\n"
    "s_add_u32 s80, s60, 63488\n"
    "buffer_load_dwordx4 v21, s[76:79], s80 offen offset:2048  lds\n"
    "s_add_u32 s80, s60, 95232\n"
    "buffer_load_dwordx4 v21, s[76:79], s80 offen offset:3072  lds\n"
    "ds_read_b64 v[70:71], v26 offset:28672\n"
    "ds_read_b64 v[72:73], v26 offset:28928\n"
    "ds_read_b64 v[74:75], v26 offset:29184\n"
    "ds_read_b64 v[76:77], v26 offset:29440\n"
    "ds_read_b64 v[78:79], v28 offset:28736\n"
    "ds_read_b64 v[80:81], v28 offset:28992\n"
    "ds_read_b64 v[82:83], v28 offset:29248\n"
    "ds_read_b64 v[84:85], v28 offset:29504\n"
    "ds_read_b64 v[86:87], v27 offset:28672\n"
    "ds_read_b64 v[88:89], v27 offset:28928\n"
    "ds_read_b64 v[90:91], v27 offset:29184\n"
    "ds_read_b64 v[92:93], v27 offset:29440\n"
    "ds_read_b64 v[94:95], v29 offset:28736\n"
    "ds_read_b64 v[96:97], v29 offset:28992\n"
    "ds_read_b64 v[98:99], v29 offset:29248\n"
    "ds_read_b64 v[100:101], v29 offset:29504\n"
    "s_waitcnt lgkmcnt(14)\n"
    "v_perm_b32 v132, v72, v70, v68\n"
    "v_perm_b32 v164, v73, v71, v68\n"
    "s_waitcnt lgkmcnt(12)\n"
    "v_perm_b32 v134, v76, v74, v68\n"
    "v_perm_b32 v166, v77, v75, v68\n"
    "s_waitcnt lgkmcnt(10)\n"
    "v_perm_b32 v133, v80, v78, v68\n"
    "v_perm_b32 v165, v81, v79, v68\n"
    "s_waitcnt lgkmcnt(8)\n"
    "v_perm_b32 v135, v84, v82, v68\n"
    "v_perm_b32 v167, v85, v83, v68\n"
    "s_waitcnt lgkmcnt(6)\n"
    "v_perm_b32 v196, v88, v86, v68\n"
    "v_perm_b32 v228, v89, v87, v68\n"
    "s_waitcnt lgkmcnt(4)\n"
    "v_perm_b32 v198, v92, v90, v68\n"
    "v_perm_b32 v230, v93, v91, v68\n"
    "s_waitcnt lgkmcnt(2)\n"
    "v_perm_b32 v197, v96, v94, v68\n"
    "v_perm_b32 v229, v97, v95, v68\n"
    "s_waitcnt lgkmcnt(0)\n"
    "v_perm_b32 v199, v100, v98, v68\n"
    "v_perm_b32 v231, v101, v99, v68\n"
    "s_waitcnt vmcnt(24) lgkmcnt(0)\n"
    "s_barrier\n"
    "s_mov_b32 m0, 28672\n"
    "s_add_u32 s80, s60, 524288\n"
    "buffer_load_dwordx4 v20, s[76:79], s80 offen offset:0  lds\n"
    "s_add_u32 s80, s60, 556032\n"
    "buffer_load_dwordx4 v20, s[76:79], s80 offen offset:1024  lds\n"
    "s_add_u32 s80, s60, 587776\n"
    "buffer_load_dwordx4 v21, s[76:79], s80 offen offset:2048  lds\n"
    "s_add_u32 s80, s60, 619520\n"
    "buffer_load_dwordx4 v21, s[76:79], s80 offen offset:3072  lds\n"
    "ds_read_b64 v[70:71], v26 offset:45056\n"
    "ds_read_b64 v[72:73], v26 offset:45312\n"
    "ds_read_b64 v[74:75], v26 offset:45568\n"
    "ds_read_b64 v[76:77], v26 offset:45824\n"
    "ds_read_b64 v[78:79], v26 offset:45120\n"
    "ds_read_b64 v[80:81], v26 offset:45376\n"
    "ds_read_b64 v[82:83], v26 offset:45632\n"
    "ds_read_b64 v[84:85], v26 offset:45888\n"
    "ds_read_b64 v[86:87], v27 offset:45056\n"
    "ds_read_b64 v[88:89], v27 offset:45312\n"
    "ds_read_b64 v[90:91], v27 offset:45568\n"
    "ds_read_b64 v[92:93], v27 offset:45824\n"
    "ds_read_b64 v[94:95], v27 offset:45120\n"
    "ds_read_b64 v[96:97], v27 offset:45376\n"
    "ds_read_b64 v[98:99], v27 offset:45632\n"
    "ds_read_b64 v[100:101], v27 offset:45888\n"
    "s_waitcnt lgkmcnt(14)\n"
    "v_perm_b32 v136, v72, v70, v68\n"
    "v_perm_b32 v168, v73, v71, v68\n"
    "s_waitcnt lgkmcnt(12)\n"
    "v_perm_b32 v138, v76, v74, v68\n"
    "v_perm_b32 v170, v77, v75, v68\n"
    "s_waitcnt lgkmcnt(10)\n"
    "v_perm_b32 v137, v80, v78, v68\n"
    "v_perm_b32 v169, v81, v79, v68\n"
    "s_waitcnt lgkmcnt(8)\n"
    "v_perm_b32 v139, v84, v82, v68\n"
    "v_perm_b32 v171, v85, v83, v68\n"
    "s_waitcnt lgkmcnt(6)\n"
    "v_perm_b32 v200, v88, v86, v68\n"
    "v_perm_b32 v232, v89, v87, v68\n"
    "s_waitcnt lgkmcnt(4)\n"
    "v_perm_b32 v202, v92, v90, v68\n"
    "v_perm_b32 v234, v93, v91, v68\n"
    "s_waitcnt lgkmcnt(2)\n"
    "v_perm_b32 v201, v96, v94, v68\n"
    "v_perm_b32 v233, v97, v95, v68\n"
    "s_waitcnt lgkmcnt(0)\n"
    "v_perm_b32 v203, v100, v98, v68\n"
    "v_perm_b32 v235, v101, v99, v68\n"
    "s_waitcnt vmcnt(24) lgkmcnt(0)\n"
    "s_barrier\n"
    "s_mov_b32 m0, 45056\n"
    "s_add_u32 s80, s60, 1048576\n"
    "buffer_load_dwordx4 v20, s[76:79], s80 offen offset:0  lds\n"
    "s_add_u32 s80, s60, 1080320\n"
    "buffer_load_dwordx4 v20, s[76:79], s80 offen offset:1024  lds\n"
    "s_add_u32 s80, s60, 1112064\n"
    "buffer_load_dwordx4 v21, s[76:79], s80 offen offset:2048  lds\n"
    "s_add_u32 s80, s60, 1143808\n"
    "buffer_load_dwordx4 v21, s[76:79], s80 offen offset:3072  lds\n"
    "ds_read_b64 v[70:71], v26 offset:61440\n"
    "ds_read_b64 v[72:73], v26 offset:61696\n"
    "ds_read_b64 v[74:75], v26 offset:61952\n"
    "ds_read_b64 v[76:77], v26 offset:62208\n"
    "ds_read_b64 v[78:79], v26 offset:61504\n"
    "ds_read_b64 v[80:81], v26 offset:61760\n"
    "ds_read_b64 v[82:83], v26 offset:62016\n"
    "ds_read_b64 v[84:85], v26 offset:62272\n"
    "ds_read_b64 v[86:87], v27 offset:61440\n"
    "ds_read_b64 v[88:89], v27 offset:61696\n"
    "ds_read_b64 v[90:91], v27 offset:61952\n"
    "ds_read_b64 v[92:93], v27 offset:62208\n"
    "ds_read_b64 v[94:95], v27 offset:61504\n"
    "ds_read_b64 v[96:97], v27 offset:61760\n"
    "ds_read_b64 v[98:99], v27 offset:62016\n"
    "ds_read_b64 v[100:101], v27 offset:62272\n"
    "s_waitcnt lgkmcnt(14)\n"
    "v_perm_b32 v140, v72, v70, v68\n"
    "v_perm_b32 v172, v73, v71, v68\n"
    "s_waitcnt lgkmcnt(12)\n"
    "v_perm_b32 v142, v76, v74, v68\n"
    "v_perm_b32 v174, v77, v75, v68\n"
    "s_waitcnt lgkmcnt(10)\n"
    "v_perm_b32 v141, v80, v78, v68\n"
    "v_perm_b32 v173, v81, v79, v68\n"
    "s_waitcnt lgkmcnt(8)\n"
    "v_perm_b32 v143, v84, v82, v68\n"
    "v_perm_b32 v175, v85, v83, v68\n"
    "s_waitcnt lgkmcnt(6)\n"
    "v_perm_b32 v204, v88, v86, v68\n"
    "v_perm_b32 v236, v89, v87, v68\n"
    "s_waitcnt lgkmcnt(4)\n"
    "v_perm_b32 v206, v92, v90, v68\n"
    "v_perm_b32 v238, v93, v91, v68\n"
    "s_waitcnt lgkmcnt(2)\n"
    "v_perm_b32 v205, v96, v94, v68\n"
    "v_perm_b32 v237, v97, v95, v68\n"
    "s_waitcnt lgkmcnt(0)\n"
    "v_perm_b32 v207, v100, v98, v68\n"
    "v_perm_b32 v239, v101, v99, v68\n"
    "s_waitcnt vmcnt(24) lgkmcnt(0)\n"
    "s_barrier\n"
    "s_mov_b32 m0, 61440\n"
    "s_add_u32 s80, s60, 1572864\n"
    "buffer_load_dwordx4 v20, s[76:79], s80 offen offset:0  lds\n"
    "s_add_u32 s80, s60, 1604608\n"
    "buffer_load_dwordx4 v20, s[76:79], s80 offen offset:1024  lds\n"
    "s_add_u32 s80, s60, 1636352\n"
    "buffer_load_dwordx4 v21, s[76:79], s80 offen offset:2048  lds\n"
    "s_add_u32 s80, s60, 1668096\n"
    "buffer_load_dwordx4 v21, s[76:79], s80 offen offset:3072  lds\n"
    "v_add_u32 v22, 65536, v22\n"
    "v_add_u32 v23, 65536, v23\n"
    "v_add_u32 v26, 65536, v26\n"
    "v_add_u32 v27, 65536, v27\n"
    "v_add_u32 v28, 65536, v28\n"
    "v_add_u32 v29, 65536, v29\n"
    "ds_read_b64 v[70:71], v26 offset:12288\n"
    "ds_read_b64 v[72:73], v26 offset:12544\n"
    "ds_read_b64 v[74:75], v26 offset:12800\n"
    "ds_read_b64 v[76:77], v26 offset:13056\n"
    "ds_read_b64 v[78:79], v26 offset:12352\n"
    "ds_read_b64 v[80:81], v26 offset:12608\n"
    "ds_read_b64 v[82:83], v26 offset:12864\n"
    "ds_read_b64 v[84:85], v26 offset:13120\n"
    "ds_read_b64 v[86:87], v27 offset:12288\n"
    "ds_read_b64 v[88:89], v27 offset:12544\n"
    "ds_read_b64 v[90:91], v27 offset:12800\n"
    "ds_read_b64 v[92:93], v27 offset:13056\n"
    "ds_read_b64 v[94:95], v27 offset:12352\n"
    "ds_read_b64 v[96:97], v27 offset:12608\n"
    "ds_read_b64 v[98:99], v27 offset:12864\n"
    "ds_read_b64 v[100:101], v27 offset:13120\n"
    "s_waitcnt lgkmcnt(14)\n"
    "v_perm_b32 v144, v72, v70, v68\n"
    "v_perm_b32 v176, v73, v71, v68\n"
    "s_waitcnt lgkmcnt(12)\n"
    "v_perm_b32 v146, v76, v74, v68\n"
    "v_perm_b32 v178, v77, v75, v68\n"
    "s_waitcnt lgkmcnt(10)\n"
    "v_perm_b32 v145, v80, v78, v68\n"
    "v_perm_b32 v177, v81, v79, v68\n"
    "s_waitcnt lgkmcnt(8)\n"
    "v_perm_b32 v147, v84, v82, v68\n"
    "v_perm_b32 v179, v85, v83, v68\n"
    "s_waitcnt lgkmcnt(6)\n"
    "v_perm_b32 v208, v88, v86, v68\n"
    "v_perm_b32 v240, v89, v87, v68\n"
    "s_waitcnt lgkmcnt(4)\n"
    "v_perm_b32 v210, v92, v90, v68\n"
    "v_perm_b32 v242, v93, v91, v68\n"
    "s_waitcnt lgkmcnt(2)\n"
    "v_perm_b32 v209, v96, v94, v68\n"
    "v_perm_b32 v241, v97, v95, v68\n"
    "s_waitcnt lgkmcnt(0)\n"
    "v_perm_b32 v211, v100, v98, v68\n"
    "v_perm_b32 v243, v101, v99, v68\n"
    "s_waitcnt vmcnt(24) lgkmcnt(0)\n"
    "s_barrier\n"
    "s_mov_b32 m0, 77824\n"
    "s_add_u32 s80, s60, 2097152\n"
    "buffer_load_dwordx4 v20, s[76:79], s80 offen offset:0  lds\n"
    "s_add_u32 s80, s60, 2128896\n"
    "buffer_load_dwordx4 v20, s[76:79], s80 offen offset:1024  lds\n"
    "s_add_u32 s80, s60, 2160640\n"
    "buffer_load_dwordx4 v21, s[76:79], s80 offen offset:2048  lds\n"
    "s_add_u32 s80, s60, 2192384\n"
    "buffer_load_dwordx4 v21, s[76:79], s80 offen offset:3072  lds\n"
    "ds_read_b64 v[70:71], v26 offset:28672\n"
    "ds_read_b64 v[72:73], v26 offset:28928\n"
    "ds_read_b64 v[74:75], v26 offset:29184\n"
    "ds_read_b64 v[76:77], v26 offset:29440\n"
    "ds_read_b64 v[78:79], v26 offset:28736\n"
    "ds_read_b64 v[80:81], v26 offset:28992\n"
    "ds_read_b64 v[82:83], v26 offset:29248\n"
    "ds_read_b64 v[84:85], v26 offset:29504\n"
    "ds_read_b64 v[86:87], v27 offset:28672\n"
    "ds_read_b64 v[88:89], v27 offset:28928\n"
    "ds_read_b64 v[90:91], v27 offset:29184\n"
    "ds_read_b64 v[92:93], v27 offset:29440\n"
    "ds_read_b64 v[94:95], v27 offset:28736\n"
    "ds_read_b64 v[96:97], v27 offset:28992\n"
    "ds_read_b64 v[98:99], v27 offset:29248\n"
    "ds_read_b64 v[100:101], v27 offset:29504\n"
    "s_waitcnt lgkmcnt(14)\n"
    "v_perm_b32 v148, v72, v70, v68\n"
    "v_perm_b32 v180, v73, v71, v68\n"
    "s_waitcnt lgkmcnt(12)\n"
    "v_perm_b32 v150, v76, v74, v68\n"
    "v_perm_b32 v182, v77, v75, v68\n"
    "s_waitcnt lgkmcnt(10)\n"
    "v_perm_b32 v149, v80, v78, v68\n"
    "v_perm_b32 v181, v81, v79, v68\n"
    "s_waitcnt lgkmcnt(8)\n"
    "v_perm_b32 v151, v84, v82, v68\n"
    "v_perm_b32 v183, v85, v83, v68\n"
    "s_waitcnt lgkmcnt(6)\n"
    "v_perm_b32 v212, v88, v86, v68\n"
    "v_perm_b32 v244, v89, v87, v68\n"
    "s_waitcnt lgkmcnt(4)\n"
    "v_perm_b32 v214, v92, v90, v68\n"
    "v_perm_b32 v246, v93, v91, v68\n"
    "s_waitcnt lgkmcnt(2)\n"
    "v_perm_b32 v213, v96, v94, v68\n"
    "v_perm_b32 v245, v97, v95, v68\n"
    "s_waitcnt lgkmcnt(0)\n"
    "v_perm_b32 v215, v100, v98, v68\n"
    "v_perm_b32 v247, v101, v99, v68\n"
    "s_waitcnt vmcnt(24) lgkmcnt(0)\n"
    "s_barrier\n"
    "s_mov_b32 m0, 94208\n"
    "s_add_u32 s80, s60, 2621440\n"
    "buffer_load_dwordx4 v20, s[76:79], s80 offen offset:0  lds\n"
    "s_add_u32 s80, s60, 2653184\n"
    "buffer_load_dwordx4 v20, s[76:79], s80 offen offset:1024  lds\n"
    "s_add_u32 s80, s60, 2684928\n"
    "buffer_load_dwordx4 v21, s[76:79], s80 offen offset:2048  lds\n"
    "s_add_u32 s80, s60, 2716672\n"
    "buffer_load_dwordx4 v21, s[76:79], s80 offen offset:3072  lds\n"
    "ds_read_b64 v[70:71], v26 offset:45056\n"
    "ds_read_b64 v[72:73], v26 offset:45312\n"
    "ds_read_b64 v[74:75], v26 offset:45568\n"
    "ds_read_b64 v[76:77], v26 offset:45824\n"
    "ds_read_b64 v[78:79], v26 offset:45120\n"
    "ds_read_b64 v[80:81], v26 offset:45376\n"
    "ds_read_b64 v[82:83], v26 offset:45632\n"
    "ds_read_b64 v[84:85], v26 offset:45888\n"
    "ds_read_b64 v[86:87], v27 offset:45056\n"
    "ds_read_b64 v[88:89], v27 offset:45312\n"
    "ds_read_b64 v[90:91], v27 offset:45568\n"
    "ds_read_b64 v[92:93], v27 offset:45824\n"
    "ds_read_b64 v[94:95], v27 offset:45120\n"
    "ds_read_b64 v[96:97], v27 offset:45376\n"
    "ds_read_b64 v[98:99], v27 offset:45632\n"
    "ds_read_b64 v[100:101], v27 offset:45888\n"
    "s_waitcnt lgkmcnt(14)\n"
    "v_perm_b32 v152, v72, v70, v68\n"
    "v_perm_b32 v184, v73, v71, v68\n"
    "s_waitcnt lgkmcnt(12)\n"
    "v_perm_b32 v154, v76, v74, v68\n"
    "v_perm_b32 v186, v77, v75, v68\n"
    "s_waitcnt lgkmcnt(10)\n"
    "v_perm_b32 v153, v80, v78, v68\n"
    "v_perm_b32 v185, v81, v79, v68\n"
    "s_waitcnt lgkmcnt(8)\n"
    "v_perm_b32 v155, v84, v82, v68\n"
    "v_perm_b32 v187, v85, v83, v68\n"
    "s_waitcnt lgkmcnt(6)\n"
    "v_perm_b32 v216, v88, v86, v68\n"
    "v_perm_b32 v248, v89, v87, v68\n"
    "s_waitcnt lgkmcnt(4)\n"
    "v_perm_b32 v218, v92, v90, v68\n"
    "v_perm_b32 v250, v93, v91, v68\n"
    "s_waitcnt lgkmcnt(2)\n"
    "v_perm_b32 v217, v96, v94, v68\n"
    "v_perm_b32 v249, v97, v95, v68\n"
    "s_waitcnt lgkmcnt(0)\n"
    "v_perm_b32 v219, v100, v98, v68\n"
    "v_perm_b32 v251, v101, v99, v68\n"
    "s_waitcnt vmcnt(24) lgkmcnt(0)\n"
    "s_barrier\n"
    "s_mov_b32 m0, 110592\n"
    "s_add_u32 s80, s60, 3145728\n"
    "buffer_load_dwordx4 v20, s[76:79], s80 offen offset:0  lds\n"
    "s_add_u32 s80, s60, 3177472\n"
    "buffer_load_dwordx4 v20, s[76:79], s80 offen offset:1024  lds\n"
    "s_add_u32 s80, s60, 3209216\n"
    "buffer_load_dwordx4 v21, s[76:79], s80 offen offset:2048  lds\n"
    "s_add_u32 s80, s60, 3240960\n"
    "buffer_load_dwordx4 v21, s[76:79], s80 offen offset:3072  lds\n"
    "ds_read_b64 v[70:71], v26 offset:61440\n"
    "ds_read_b64 v[72:73], v26 offset:61696\n"
    "ds_read_b64 v[74:75], v26 offset:61952\n"
    "ds_read_b64 v[76:77], v26 offset:62208\n"
    "ds_read_b64 v[78:79], v26 offset:61504\n"
    "ds_read_b64 v[80:81], v26 offset:61760\n"
    "ds_read_b64 v[82:83], v26 offset:62016\n"
    "ds_read_b64 v[84:85], v26 offset:62272\n"
    "ds_read_b64 v[86:87], v27 offset:61440\n"
    "ds_read_b64 v[88:89], v27 offset:61696\n"
    "ds_read_b64 v[90:91], v27 offset:61952\n"
    "ds_read_b64 v[92:93], v27 offset:62208\n"
    "ds_read_b64 v[94:95], v27 offset:61504\n"
    "ds_read_b64 v[96:97], v27 offset:61760\n"
    "ds_read_b64 v[98:99], v27 offset:62016\n"
    "ds_read_b64 v[100:101], v27 offset:62272\n"
    "s_waitcnt lgkmcnt(14)\n"
    "v_perm_b32 v156, v72, v70, v68\n"
    "v_perm_b32 v188, v73, v71, v68\n"
    "s_waitcnt lgkmcnt(12)\n"
    "v_perm_b32 v158, v76, v74, v68\n"
    "v_perm_b32 v190, v77, v75, v68\n"
    "s_waitcnt lgkmcnt(10)\n"
    "v_perm_b32 v157, v80, v78, v68\n"
    "v_perm_b32 v189, v81, v79, v68\n"
    "s_waitcnt lgkmcnt(8)\n"
    "v_perm_b32 v159, v84, v82, v68\n"
    "v_perm_b32 v191, v85, v83, v68\n"
    "s_waitcnt lgkmcnt(6)\n"
    "v_perm_b32 v220, v88, v86, v68\n"
    "v_perm_b32 v252, v89, v87, v68\n"
    "s_waitcnt lgkmcnt(4)\n"
    "v_perm_b32 v222, v92, v90, v68\n"
    "v_perm_b32 v254, v93, v91, v68\n"
    "s_waitcnt lgkmcnt(2)\n"
    "v_perm_b32 v221, v96, v94, v68\n"
    "v_perm_b32 v253, v97, v95, v68\n"
    "s_waitcnt lgkmcnt(0)\n"
    "v_perm_b32 v223, v100, v98, v68\n"
    "v_perm_b32 v255, v101, v99, v68\n"
    "s_waitcnt vmcnt(24) lgkmcnt(0)\n"
    "s_barrier\n"
    "s_mov_b32 m0, 126976\n"
    "s_add_u32 s80, s60, 3670016\n"
    "buffer_load_dwordx4 v20, s[76:79], s80 offen offset:0  lds\n"
    "s_add_u32 s80, s60, 3701760\n"
    "buffer_load_dwordx4 v20, s[76:79], s80 offen offset:1024  lds\n"
    "s_add_u32 s80, s60, 3733504\n"
    "buffer_load_dwordx4 v21, s[76:79], s80 offen offset:2048  lds\n"
    "s_add_u32 s80, s60, 3765248\n"
    "buffer_load_dwordx4 v21, s[76:79], s80 offen offset:3072  lds\n"
    "buffer_load_dwordx2 v[4:5], off, s[20:23], s63 sc1\n"
    "v_add_u32 v22, 4294901760, v22\n"
    "v_add_u32 v23, 4294901760, v23\n"
    "v_add_u32 v26, 4294901760, v26\n"
    "v_add_u32 v27, 4294901760, v27\n"
    "v_add_u32 v28, 4294901760, v28\n"
    "v_add_u32 v29, 4294901760, v29\n"
    "ds_read_b64_tr_b16 a[16:17], v22 offset:0\n"
    "ds_read_b64_tr_b16 a[80:81], v23 offset:0\n"
    "ds_read_b64_tr_b16 a[18:19], v22 offset:1024\n"
    "ds_read_b64_tr_b16 a[82:83], v23 offset:1024\n"
    "ds_read_b64_tr_b16 a[20:21], v22 offset:8192\n"
    "ds_read_b64_tr_b16 a[84:85], v23 offset:8192\n"
    "ds_read_b64_tr_b16 a[22:23], v22 offset:9216\n"
    "ds_read_b64_tr_b16 a[86:87], v23 offset:9216\n"
    "s_waitcnt vmcnt(25) lgkmcnt(0)\n"
    "s_barrier\n"
    "s_mov_b64 s[76:77], s[8:9]\n"
    "s_and_b32 s77, s77, 0xffff\n"
    "s_mov_b32 s78, 4194304\n"
    "s_mov_b32 s79, 0x20000\n"
    "s_mov_b32 m0, 12288\n"
    "s_add_u32 s80, s60, 0\n"
    "buffer_load_dwordx4 v20, s[76:79], s80 offen offset:0  lds\n"
    "s_add_u32 s80, s60, 31744\n"
    "buffer_load_dwordx4 v20, s[76:79], s80 offen offset:1024  lds\n"
    "s_add_u32 s80, s60, 63488\n"
    "buffer_load_dwordx4 v21, s[76:79], s80 offen offset:2048  lds\n"
    "s_add_u32 s80, s60, 95232\n"
    "buffer_load_dwordx4 v21, s[76:79], s80 offen offset:3072  lds\n"
    "ds_read_b64_tr_b16 a[24:25], v22 offset:16384\n"
    "ds_read_b64_tr_b16 a[88:89], v23 offset:16384\n"
    "ds_read_b64_tr_b16 a[26:27], v22 offset:17408\n"
    "ds_read_b64_tr_b16 a[90:91], v23 offset:17408\n"
    "ds_read_b64_tr_b16 a[28:29], v22 offset:24576\n"
    "ds_read_b64_tr_b16 a[92:93], v23 offset:24576\n"
    "ds_read_b64_tr_b16 a[30:31], v22 offset:25600\n"
    "ds_read_b64_tr_b16 a[94:95], v23 offset:25600\n"
    "s_waitcnt vmcnt(25) lgkmcnt(0)\n"
    "s_barrier\n"
    "s_mov_b32 m0, 28672\n"
    "s_add_u32 s80, s60, 524288\n"
    "buffer_load_dwordx4 v20, s[76:79], s80 offen offset:0  lds\n"
    "s_add_u32 s80, s60, 556032\n"
    "buffer_load_dwordx4 v20, s[76:79], s80 offen offset:1024  lds\n"
    "s_add_u32 s80, s60, 587776\n"
    "buffer_load_dwordx4 v21, s[76:79], s80 offen offset:2048  lds\n"
    "s_add_u32 s80, s60, 619520\n"
    "buffer_load_dwordx4 v21, s[76:79], s80 offen offset:3072  lds\n"
    "ds_read_b64_tr_b16 a[32:33], v22 offset:32768\n"
    "ds_read_b64_tr_b16 a[96:97], v23 offset:32768\n"
    "ds_read_b64_tr_b16 a[34:35], v22 offset:33792\n"
    "ds_read_b64_tr_b16 a[98:99], v23 offset:33792\n"
    "ds_read_b64_tr_b16 a[36:37], v22 offset:40960\n"
    "ds_read_b64_tr_b16 a[100:101], v23 offset:40960\n"
    "ds_read_b64_tr_b16 a[38:39], v22 offset:41984\n"
    "ds_read_b64_tr_b16 a[102:103], v23 offset:41984\n"
    "s_waitcnt vmcnt(25) lgkmcnt(0)\n"
    "s_barrier\n"
    "s_mov_b32 m0, 45056\n"
    "s_add_u32 s80, s60, 1048576\n"
    "buffer_load_dwordx4 v20, s[76:79], s80 offen offset:0  lds\n"
    "s_add_u32 s80, s60, 1080320\n"
    "buffer_load_dwordx4 v20, s[76:79], s80 offen offset:1024  lds\n"
    "s_add_u32 s80, s60, 1112064\n"
    "buffer_load_dwordx4 v21, s[76:79], s80 offen offset:2048  lds\n"
    "s_add_u32 s80, s60, 1143808\n"
    "buffer_load_dwordx4 v21, s[76:79], s80 offen offset:3072  lds\n"
    "ds_read_b64_tr_b16 a[40:41], v22 offset:49152\n"
    "ds_read_b64_tr_b16 a[104:105], v23 offset:49152\n"
    "ds_read_b64_tr_b16 a[42:43], v22 offset:50176\n"
    "ds_read_b64_tr_b16 a[106:107], v23 offset:50176\n"
    "ds_read_b64_tr_b16 a[44:45], v22 offset:57344\n"
    "ds_read_b64_tr_b16 a[108:109], v23 offset:57344\n"
    "ds_read_b64_tr_b16 a[46:47], v22 offset:58368\n"
    "ds_read_b64_tr_b16 a[110:111], v23 offset:58368\n"
    "s_waitcnt vmcnt(25) lgkmcnt(0)\n"
    "s_barrier\n"
    "s_mov_b32 m0, 61440\n"
    "s_add_u32 s80, s60, 1572864\n"
    "buffer_load_dwordx4 v20, s[76:79], s80 offen offset:0  lds\n"
    "s_add_u32 s80, s60, 1604608\n"
    "buffer_load_dwordx4 v20, s[76:79], s80 offen offset:1024  lds\n"
    "s_add_u32 s80, s60, 1636352\n"
    "buffer_load_dwordx4 v21, s[76:79], s80 offen offset:2048  lds\n"
    "s_add_u32 s80, s60, 1668096\n"
    "buffer_load_dwordx4 v21, s[76:79], s80 offen offset:3072  lds\n"
    "s_getpc_b64 s[94:95]\n"
    "RCA12_3_%=:\n"
    "v_lshlrev_b32 v10, 7, v0\n"
    "v_min_u32 v10, .Lcode_end-RCA12_3_%=-4, v10\n"
    "global_load_dword v10, v10, s[94:95]\n"
    "v_add_u32 v22, 65536, v22\n"
    "v_add_u32 v23, 65536, v23\n"
    "v_add_u32 v26, 65536, v26\n"
    "v_add_u32 v27, 65536, v27\n"
    "v_add_u32 v28, 65536, v28\n"
    "v_add_u32 v29, 65536, v29\n"
    "ds_read_b64_tr_b16 a[48:49], v22 offset:0\n"
    "ds_read_b64_tr_b16 a[112:113], v23 offset:0\n"
    "ds_read_b64_tr_b16 a[50:51], v22 offset:1024\n"
    "ds_read_b64_tr_b16 a[114:115], v23 offset:1024\n"
    "ds_read_b64_tr_b16 a[52:53], v22 offset:8192\n"
    "ds_read_b64_tr_b16 a[116:117], v23 offset:8192\n"
    "ds_read_b64_tr_b16 a[54:55], v22 offset:9216\n"
    "ds_read_b64_tr_b16 a[118:119], v23 offset:9216\n"
    "s_waitcnt vmcnt(26) lgkmcnt(0)\n"
    "s_barrier\n"
    "s_mov_b32 m0, 77824\n"
    "s_add_u32 s80, s60, 2097152\n"
    "buffer_load_dwordx4 v20, s[76:79], s80 offen offset:0  lds\n"
    "s_add_u32 s80, s60, 2128896\n"
    "buffer_load_dwordx4 v20, s[76:79], s80 offen offset:1024  lds\n"
    "s_add_u32 s80, s60, 2160640\n"
    "buffer_load_dwordx4 v21, s[76:79], s80 offen offset:2048  lds\n"
    "s_add_u32 s80, s60, 2192384\n"
    "buffer_load_dwordx4 v21, s[76:79], s80 offen offset:3072  lds\n"
    "ds_read_b64_tr_b16 a[56:57], v22 offset:16384\n"
    "ds_read_b64_tr_b16 a[120:121], v23 offset:16384\n"
    "ds_read_b64_tr_b16 a[58:59], v22 offset:17408\n"
    "ds_read_b64_tr_b16 a[122:123], v23 offset:17408\n"
    "ds_read_b64_tr_b16 a[60:61], v22 offset:24576\n"
    "ds_read_b64_tr_b16 a[124:125], v23 offset:24576\n"
    "ds_read_b64_tr_b16 a[62:63], v22 offset:25600\n"
    "ds_read_b64_tr_b16 a[126:127], v23 offset:25600\n"
    "s_waitcnt vmcnt(26) lgkmcnt(0)\n"
    "s_barrier\n"
    "s_mov_b32 m0, 94208\n"
    "s_add_u32 s80, s60, 2621440\n"
    "buffer_load_dwordx4 v20, s[76:79], s80 offen offset:0  lds\n"
    "s_add_u32 s80, s60, 2653184\n"
    "buffer_load_dwordx4 v20, s[76:79], s80 offen offset:1024  lds\n"
    "s_add_u32 s80, s60, 2684928\n"
    "buffer_load_dwordx4 v21, s[76:79], s80 offen offset:2048  lds\n"
    "s_add_u32 s80, s60, 2716672\n"
    "buffer_load_dwordx4 v21, s[76:79], s80 offen offset:3072  lds\n"
    "ds_read_b64_tr_b16 a[0:1], v22 offset:32768\n"
    "ds_read_b64_tr_b16 a[64:65], v23 offset:32768\n"
    "ds_read_b64_tr_b16 a[2:3], v22 offset:33792\n"
    "ds_read_b64_tr_b16 a[66:67], v23 offset:33792\n"
    "ds_read_b64_tr_b16 a[4:5], v22 offset:40960\n"
    "ds_read_b64_tr_b16 a[68:69], v23 offset:40960\n"
    "ds_read_b64_tr_b16 a[6:7], v22 offset:41984\n"
    "ds_read_b64_tr_b16 a[70:71], v23 offset:41984\n"
    "s_waitcnt vmcnt(26) lgkmcnt(0)\n"
    "s_barrier\n"
    "s_mov_b32 m0, 110592\n"
    "s_add_u32 s80, s60, 3145728\n"
    "buffer_load_dwordx4 v20, s[76:79], s80 offen offset:0  lds\n"
    "s_add_u32 s80, s60, 3177472\n"
    "buffer_load_dwordx4 v20, s[76:79], s80 offen offset:1024  lds\n"
    "s_add_u32 s80, s60, 3209216\n"
    "buffer_load_dwordx4 v21, s[76:79], s80 offen offset:2048  lds\n"
    "s_add_u32 s80, s60, 3240960\n"
    "buffer_load_dwordx4 v21, s[76:79], s80 offen offset:3072  lds\n"
    "ds_read_b64_tr_b16 a[8:9], v22 offset:49152\n"
    "ds_read_b64_tr_b16 a[72:73], v23 offset:49152\n"
    "ds_read_b64_tr_b16 a[10:11], v22 offset:50176\n"
    "ds_read_b64_tr_b16 a[74:75], v23 offset:50176\n"
    "ds_read_b64_tr_b16 a[12:13], v22 offset:57344\n"
    "ds_read_b64_tr_b16 a[76:77], v23 offset:57344\n"
    "ds_read_b64_tr_b16 a[14:15], v22 offset:58368\n"
    "ds_read_b64_tr_b16 a[78:79], v23 offset:58368\n"
    "s_waitcnt vmcnt(25) lgkmcnt(0)\n"
    "s_barrier\n"
    "s_mov_b32 m0, 126976\n"
    "s_add_u32 s80, s60, 3670016\n"
    "buffer_load_dwordx4 v20, s[76:79], s80 offen offset:0  lds\n"
    "s_add_u32 s80, s60, 3701760\n"
    "buffer_load_dwordx4 v20, s[76:79], s80 offen offset:1024  lds\n"
    "s_add_u32 s80, s60, 3733504\n"
    "buffer_load_dwordx4 v21, s[76:79], s80 offen offset:2048  lds\n"
    "s_add_u32 s80, s60, 3765248\n"
    "buffer_load_dwordx4 v21, s[76:79], s80 offen offset:3072  lds\n"
    "v_readfirstlane_b32 s46, v67\n"
    "v_readfirstlane_b32 s68, v5\n"
    "s_cmp_lg_u32 s24, 0\n"
    "s_cselect_b32 s46, %2, s46\n"
    "s_cmp_lg_u32 s46, s68\n"
    "s_cselect_b32 s68, -1, 0\n"
    "buffer_load_dwordx2 v[4:5], off, s[20:23], s63 sc1\n"
    "v_lshrrev_b32 v6, 2, v0\n"
    "v_and_b32 v7, 3, v0\n"
    "v_lshl_add_u32 v6, v6, 3, v7\n"
    "v_lshlrev_b32 v6, 7, v6\n"
    "s_mov_b32 s46, 81793536\n"
    "buffer_load_dword v7, v6, s[20:23], s46 offen sc1\n"
    "s_mov_b32 s46, 81809920\n"
    "buffer_load_dword v8, v6, s[20:23], s46 offen sc1\n"
    "v_add_u32 v22, 4294901760, v22\n"
    "v_add_u32 v23, 4294901760, v23\n"
    "v_add_u32 v26, 4294901760, v26\n"
    "v_add_u32 v27, 4294901760, v27\n"
    "v_add_u32 v28, 4294901760, v28\n"
    "v_add_u32 v29, 4294901760, v29\n"
    "ds_read_b64_tr_b16 a[160:161], v22 offset:0\n"
    "ds_read_b64_tr_b16 a[208:209], v23 offset:0\n"
    "ds_read_b64_tr_b16 a[162:163], v22 offset:1024\n"
    "ds_read_b64_tr_b16 a[210:211], v23 offset:1024\n"
    "ds_read_b64_tr_b16 a[164:165], v22 offset:8192\n"
    "ds_read_b64_tr_b16 a[212:213], v23 offset:8192\n"
    "ds_read_b64_tr_b16 a[166:167], v22 offset:9216\n"
    "ds_read_b64_tr_b16 a[214:215], v23 offset:9216\n"
    "s_waitcnt vmcnt(28) lgkmcnt(0)\n"
    "s_barrier\n"
    "s_mov_b64 s[76:77], s[6:7]\n"
    "s_and_b32 s77, s77, 0xffff\n"
    "s_mov_b32 s78, 4194304\n"
    "s_mov_b32 s79, 0x20000\n"
    "s_mov_b32 m0, 12288\n"
    "s_add_u32 s80, s60, 0\n"
    "buffer_load_dwordx4 v20, s[76:79], s80 offen offset:0  lds\n"
    "s_add_u32 s80, s60, 31744\n"
    "buffer_load_dwordx4 v20, s[76:79], s80 offen offset:1024  lds\n"
    "s_add_u32 s80, s60, 63488\n"
    "buffer_load_dwordx4 v21, s[76:79], s80 offen offset:2048  lds\n"
    "s_add_u32 s80, s60, 95232\n"
    "buffer_load_dwordx4 v21, s[76:79], s80 offen offset:3072  lds\n"
    "ds_read_b64_tr_b16 a[168:169], v22 offset:16384\n"
    "ds_read_b64_tr_b16 a[216:217], v23 offset:16384\n"
    "ds_read_b64_tr_b16 a[170:171], v22 offset:17408\n"
    "ds_read_b64_tr_b16 a[218:219], v23 offset:17408\n"
    "ds_read_b64_tr_b16 a[172:173], v22 offset:24576\n"
    "ds_read_b64_tr_b16 a[220:221], v23 offset:24576\n"
    "ds_read_b64_tr_b16 a[174:175], v22 offset:25600\n"
    "ds_read_b64_tr_b16 a[222:223], v23 offset:25600\n"
    "s_waitcnt vmcnt(28) lgkmcnt(0)\n"
    "s_barrier\n"
    "s_mov_b32 m0, 28672\n"
    "s_add_u32 s80, s60, 524288\n"
    "buffer_load_dwordx4 v20, s[76:79], s80 offen offset:0  lds\n"
    "s_add_u32 s80, s60, 556032\n"
    "buffer_load_dwordx4 v20, s[76:79], s80 offen offset:1024  lds\n"
    "s_add_u32 s80, s60, 587776\n"
    "buffer_load_dwordx4 v21, s[76:79], s80 offen offset:2048  lds\n"
    "s_add_u32 s80, s60, 619520\n"
    "buffer_load_dwordx4 v21, s[76:79], s80 offen offset:3072  lds\n"
    "ds_read_b64_tr_b16 a[176:177], v22 offset:32768\n"
    "ds_read_b64_tr_b16 a[224:225], v23 offset:32768\n"
    "ds_read_b64_tr_b16 a[178:179], v22 offset:33792\n"
    "ds_read_b64_tr_b16 a[226:227], v23 offset:33792\n"
    "ds_read_b64_tr_b16 a[180:181], v22 offset:40960\n"
    "ds_read_b64_tr_b16 a[228:229], v23 offset:40960\n"
    "ds_read_b64_tr_b16 a[182:183], v22 offset:41984\n"
    "ds_read_b64_tr_b16 a[230:231], v23 offset:41984\n"
    "s_waitcnt vmcnt(28) lgkmcnt(0)\n"
    "s_barrier\n"
    "s_mov_b32 m0, 45056\n"
    "s_add_u32 s80, s60, 1048576\n"
    "buffer_load_dwordx4 v20, s[76:79], s80 offen offset:0  lds\n"
    "s_add_u32 s80, s60, 1080320\n"
    "buffer_load_dwordx4 v20, s[76:79], s80 offen offset:1024  lds\n"
    "s_add_u32 s80, s60, 1112064\n"
    "buffer_load_dwordx4 v21, s[76:79], s80 offen offset:2048  lds\n"
    "s_add_u32 s80, s60, 1143808\n"
    "buffer_load_dwordx4 v21, s[76:79], s80 offen offset:3072  lds\n"
    "ds_read_b64_tr_b16 a[184:185], v22 offset:49152\n"
    "ds_read_b64_tr_b16 a[232:233], v23 offset:49152\n"
    "ds_read_b64_tr_b16 a[186:187], v22 offset:50176\n"
    "ds_read_b64_tr_b16 a[234:235], v23 offset:50176\n"
    "ds_read_b64_tr_b16 a[188:189], v22 offset:57344\n"
    "ds_read_b64_tr_b16 a[236:237], v23 offset:57344\n"
    "ds_read_b64_tr_b16 a[190:191], v22 offset:58368\n"
    "ds_read_b64_tr_b16 a[238:239], v23 offset:58368\n"
    "s_waitcnt vmcnt(27) lgkmcnt(0)\n"
    "s_barrier\n"
    "s_mov_b32 m0, 61440\n"
    "s_add_u32 s80, s60, 1572864\n"
    "buffer_load_dwordx4 v20, s[76:79], s80 offen offset:0  lds\n"
    "s_add_u32 s80, s60, 1604608\n"
    "buffer_load_dwordx4 v20, s[76:79], s80 offen offset:1024  lds\n"
    "s_add_u32 s80, s60, 1636352\n"
    "buffer_load_dwordx4 v21, s[76:79], s80 offen offset:2048  lds\n"
    "s_add_u32 s80, s60, 1668096\n"
    "buffer_load_dwordx4 v21, s[76:79], s80 offen offset:3072  lds\n"
    "v_add_u32 v22, 65536, v22\n"
    "v_add_u32 v23, 65536, v23\n"
    "v_add_u32 v26, 65536, v26\n"
    "v_add_u32 v27, 65536, v27\n"
    "v_add_u32 v28, 65536, v28\n"
    "v_add_u32 v29, 65536, v29\n"
    "ds_read_b64_tr_b16 a[192:193], v22 offset:0\n"
    "ds_read_b64_tr_b16 a[240:241], v23 offset:0\n"
    "ds_read_b64_tr_b16 a[194:195], v22 offset:1024\n"
    "ds_read_b64_tr_b16 a[242:243], v23 offset:1024\n"
    "ds_read_b64_tr_b16 a[196:197], v22 offset:8192\n"
    "ds_read_b64_tr_b16 a[244:245], v23 offset:8192\n"
    "ds_read_b64_tr_b16 a[198:199], v22 offset:9216\n"
    "ds_read_b64_tr_b16 a[246:247], v23 offset:9216\n"
    "s_waitcnt vmcnt(27) lgkmcnt(0)\n"
    "s_barrier\n"
    "s_mov_b32 m0, 77824\n"
    "s_add_u32 s80, s60, 2097152\n"
    "buffer_load_dwordx4 v20, s[76:79], s80 offen offset:0  lds\n"
    "s_add_u32 s80, s60, 2128896\n"
    "buffer_load_dwordx4 v20, s[76:79], s80 offen offset:1024  lds\n"
    "s_add_u32 s80, s60, 2160640\n"
    "buffer_load_dwordx4 v21, s[76:79], s80 offen offset:2048  lds\n"
    "s_add_u32 s80, s60, 2192384\n"
    "buffer_load_dwordx4 v21, s[76:79], s80 offen offset:3072  lds\n"
    "ds_read_b64_tr_b16 a[200:201], v22 offset:16384\n"
    "ds_read_b64_tr_b16 a[248:249], v23 offset:16384\n"
    "ds_read_b64_tr_b16 a[202:203], v22 offset:17408\n"
    "ds_read_b64_tr_b16 a[250:251], v23 offset:17408\n"
    "ds_read_b64_tr_b16 a[204:205], v22 offset:24576\n"
    "ds_read_b64_tr_b16 a[252:253], v23 offset:24576\n"
    "ds_read_b64_tr_b16 a[206:207], v22 offset:25600\n"
    "ds_read_b64_tr_b16 a[254:255], v23 offset:25600\n"
    "s_waitcnt vmcnt(27) lgkmcnt(0)\n"
    "s_barrier\n"
    "s_mov_b32 m0, 94208\n"
    "s_add_u32 s80, s60, 2621440\n"
    "buffer_load_dwordx4 v20, s[76:79], s80 offen offset:0  lds\n"
    "s_add_u32 s80, s60, 2653184\n"
    "buffer_load_dwordx4 v20, s[76:79], s80 offen offset:1024  lds\n"
    "s_add_u32 s80, s60, 2684928\n"
    "buffer_load_dwordx4 v21, s[76:79], s80 offen offset:2048  lds\n"
    "s_add_u32 s80, s60, 2716672\n"
    "buffer_load_dwordx4 v21, s[76:79], s80 offen offset:3072  lds\n"
    "ds_read_b64_tr_b16 v[32:33], v22 offset:32768\n"
    "ds_read_b64_tr_b16 v[40:41], v23 offset:32768\n"
    "ds_read_b64_tr_b16 v[34:35], v22 offset:33792\n"
    "ds_read_b64_tr_b16 v[42:43], v23 offset:33792\n"
    "ds_read_b64_tr_b16 v[36:37], v22 offset:40960\n"
    "ds_read_b64_tr_b16 v[44:45], v23 offset:40960\n"
    "ds_read_b64_tr_b16 v[38:39], v22 offset:41984\n"
    "ds_read_b64_tr_b16 v[46:47], v23 offset:41984\n"
    "s_waitcnt vmcnt(27) lgkmcnt(0)\n"
    "s_barrier\n"
    "s_mov_b32 m0, 110592\n"
    "s_add_u32 s80, s60, 3145728\n"
    "buffer_load_dwordx4 v20, s[76:79], s80 offen offset:0  lds\n"
    "s_add_u32 s80, s60, 3177472\n"
    "buffer_load_dwordx4 v20, s[76:79], s80 offen offset:1024  lds\n"
    "s_add_u32 s80, s60, 3209216\n"
    "buffer_load_dwordx4 v21, s[76:79], s80 offen offset:2048  lds\n"
    "s_add_u32 s80, s60, 3240960\n"
    "buffer_load_dwordx4 v21, s[76:79], s80 offen offset:3072  lds\n"
    "ds_read_b64_tr_b16 v[48:49], v22 offset:49152\n"
    "ds_read_b64_tr_b16 v[56:57], v23 offset:49152\n"
    "ds_read_b64_tr_b16 v[50:51], v22 offset:50176\n"
    "ds_read_b64_tr_b16 v[58:59], v23 offset:50176\n"
    "ds_read_b64_tr_b16 v[52:53], v22 offset:57344\n"
    "ds_read_b64_tr_b16 v[60:61], v23 offset:57344\n"
    "ds_read_b64_tr_b16 v[54:55], v22 offset:58368\n"
    "ds_read_b64_tr_b16 v[62:63], v23 offset:58368\n"
    "s_waitcnt vmcnt(24) lgkmcnt(0)\n"
    "s_barrier\n"
    "s_mov_b32 m0, 126976\n"
    "s_add_u32 s80, s60, 3670016\n"
    "buffer_load_dwordx4 v20, s[76:79], s80 offen offset:0  lds\n"
    "s_add_u32 s80, s60, 3701760\n"
    "buffer_load_dwordx4 v20, s[76:79], s80 offen offset:1024  lds\n"
    "s_add_u32 s80, s60, 3733504\n"
    "buffer_load_dwordx4 v21, s[76:79], s80 offen offset:2048  lds\n"
    "s_add_u32 s80, s60, 3765248\n"
    "buffer_load_dwordx4 v21, s[76:79], s80 offen offset:3072  lds\n"
    "v_readfirstlane_b32 s62, v4\n"
    "s_bitcmp1_b32 s62, 1\n"
    "s_cselect_b32 s68, s68, 0\n"
    "s_xor_b32 s62, s62, s24\n"
    "s_and_b32 s62, s62, 1\n"
    "s_lshl_b32 s62, s62, 13\n"
    "v_min_u32 v7, v7, v8\n"
    "v_cmp_gt_u32 vcc, 1, v7\n"
    "s_cmp_eq_u64 vcc, 0\n"
    "s_cselect_b32 s68, s68, 0\n"
    "s_add_u32 s30, s62, 0\n"
    "s_add_u32 s31, s30, 4096\n"
    "s_lshl_b32 s47, s25, 8\n"
    "s_add_u32 s36, s62, s47\n"
    "s_mov_b32 s50, 0\n"
    "s_mov_b32 s28, s30\n"
    "s_add_u32 s32, s62, s47\n"
    "s_add_u32 s32, s32, 67502080\n"
    "s_add_u32 s33, s32, 65536\n"
    "s_lshl_b32 s47, s25, 14\n"
    "s_add_u32 s34, s62, s47\n"
    "s_add_u32 s34, s34, 67108864\n"
    "s_add_u32 s35, s34, 4096\n"
    "s_add_u32 s44, s34, 4200448\n"
    "s_lshl_b32 s39, s25, 15\n"
    "s_add_u32 s39, s39, s62\n"
    "s_add_u32 s39, s39, 16797696\n"
    "s_xor_b32 s69, s62, 0x2000\n"
    "s_lshr_b32 s41, s25, 3\n"
    "s_lshl_b32 s41, s41, 14\n"
    "s_and_b32 s47, s25, 7\n"
    "s_lshl_b32 s47, s47, 10\n"
    "s_add_u32 s41, s41, s47\n"
    "s_add_u32 s41, s41, s69\n"
    "s_add_u32 s41, s41, 3146112\n"
    "s_add_u32 s69, s69, 3309568\n"
    "s_mov_b32 s37, 0x7f000000\n"
    "s_mov_b32 s42, 0\n"
    "s_cmp_eq_u32 s25, 0\n"
    "s_cselect_b32 s37, s69, s37\n"
    "s_add_u32 s69, s69, 16384\n"
    "s_cmp_eq_u32 s25, 11\n"
    "s_cselect_b32 s37, s69, s37\n"
    "s_add_u32 s69, s69, 16384\n"
    "s_cmp_eq_u32 s25, 22\n"
    "s_cselect_b32 s37, s69, s37\n"
    "s_add_u32 s69, s69, 16384\n"
    "v_add_u32 v22, 4294901760, v22\n"
    "v_add_u32 v23, 4294901760, v23\n"
    "v_add_u32 v26, 4294901760, v26\n"
    "v_add_u32 v27, 4294901760, v27\n"
    "v_add_u32 v28, 4294901760, v28\n"
    "v_add_u32 v29, 4294901760, v29\n"
    "ds_read_b64_tr_b16 v[70:71], v22 offset:0\n"
    "ds_read_b64_tr_b16 v[78:79], v23 offset:0\n"
    "ds_read_b64_tr_b16 v[72:73], v22 offset:1024\n"
    "ds_read_b64_tr_b16 v[80:81], v23 offset:1024\n"
    "ds_read_b64_tr_b16 v[74:75], v22 offset:8192\n"
    "ds_read_b64_tr_b16 v[82:83], v23 offset:8192\n"
    "ds_read_b64_tr_b16 v[76:77], v22 offset:9216\n"
    "ds_read_b64_tr_b16 v[84:85], v23 offset:9216\n"
    "s_waitcnt vmcnt(24) lgkmcnt(0)\n"
    "s_barrier\n"
    "s_getpc_b64 s[94:95]\n"
    "RCA25_3_%=:\n"
    "v_lshlrev_b32 v11, 7, v0\n"
    "v_min_u32 v11, .Lcode_end-RCA25_3_%=-4, v11\n"
    "global_load_dword v11, v11, s[94:95]\n"
    "ds_read_b64_tr_b16 v[86:87], v22 offset:16384\n"
    "ds_read_b64_tr_b16 v[94:95], v23 offset:16384\n"
    "ds_read_b64_tr_b16 v[88:89], v22 offset:17408\n"
    "ds_read_b64_tr_b16 v[96:97], v23 offset:17408\n"
    "ds_read_b64_tr_b16 v[90:91], v22 offset:24576\n"
    "ds_read_b64_tr_b16 v[98:99], v23 offset:24576\n"
    "ds_read_b64_tr_b16 v[92:93], v22 offset:25600\n"
    "ds_read_b64_tr_b16 v[100:101], v23 offset:25600\n"
    "ds_write_b128 v107, v[70:73] offset:12288\n"
    "ds_write_b128 v107, v[74:77] offset:13312\n"
    "ds_write_b128 v107, v[78:81] offset:14336\n"
    "ds_write_b128 v107, v[82:85] offset:15360\n"
    "s_waitcnt vmcnt(21) lgkmcnt(0)\n"
    "s_barrier\n"
    "ds_read_b64_tr_b16 v[70:71], v22 offset:32768\n"
    "ds_read_b64_tr_b16 v[78:79], v23 offset:32768\n"
    "ds_read_b64_tr_b16 v[72:73], v22 offset:33792\n"
    "ds_read_b64_tr_b16 v[80:81], v23 offset:33792\n"
    "ds_read_b64_tr_b16 v[74:75], v22 offset:40960\n"
    "ds_read_b64_tr_b16 v[82:83], v23 offset:40960\n"
    "ds_read_b64_tr_b16 v[76:77], v22 offset:41984\n"
    "ds_read_b64_tr_b16 v[84:85], v23 offset:41984\n"
    "ds_write_b128 v107, v[86:89] offset:28672\n"
    "ds_write_b128 v107, v[90:93] offset:29696\n"
    "ds_write_b128 v107, v[94:97] offset:30720\n"
    "ds_write_b128 v107, v[98:101] offset:31744\n"
    "s_waitcnt vmcnt(17) lgkmcnt(0)\n"
    "s_barrier\n"
    "ds_read_b64_tr_b16 v[86:87], v22 offset:49152\n"
    "ds_read_b64_tr_b16 v[94:95], v23 offset:49152\n"
    "ds_read_b64_tr_b16 v[88:89], v22 offset:50176\n"
    "ds_read_b64_tr_b16 v[96:97], v23 offset:50176\n"
    "ds_read_b64_tr_b16 v[90:91], v22 offset:57344\n"
    "ds_read_b64_tr_b16 v[98:99], v23 offset:57344\n"
    "ds_read_b64_tr_b16 v[92:93], v22 offset:58368\n"
    "ds_read_b64_tr_b16 v[100:101], v23 offset:58368\n"
    "ds_write_b128 v107, v[70:73] offset:45056\n"
    "ds_write_b128 v107, v[74:77] offset:46080\n"
    "ds_write_b128 v107, v[78:81] offset:47104\n"
    "ds_write_b128 v107, v[82:85] offset:48128\n"
    "s_waitcnt vmcnt(13) lgkmcnt(0)\n"
    "s_barrier\n"
    "v_add_u32 v22, 65536, v22\n"
    "v_add_u32 v23, 65536, v23\n"
    "v_add_u32 v26, 65536, v26\n"
    "v_add_u32 v27, 65536, v27\n"
    "v_add_u32 v28, 65536, v28\n"
    "v_add_u32 v29, 65536, v29\n"
    "ds_read_b64_tr_b16 a[128:129], v22 offset:0\n"
    "ds_read_b64_tr_b16 a[144:145], v23 offset:0\n"
    "ds_read_b64_tr_b16 a[130:131], v22 offset:1024\n"
    "ds_read_b64_tr_b16 a[146:147], v23 offset:1024\n"
    "ds_read_b64_tr_b16 a[132:133], v22 offset:8192\n"
    "ds_read_b64_tr_b16 a[148:149], v23 offset:8192\n"
    "ds_read_b64_tr_b16 a[134:135], v22 offset:9216\n"
    "ds_read_b64_tr_b16 a[150:151], v23 offset:9216\n"
    "ds_write_b128 v107, v[86:89] offset:61440\n"
    "ds_write_b128 v107, v[90:93] offset:62464\n"
    "ds_write_b128 v107, v[94:97] offset:63488\n"
    "ds_write_b128 v107, v[98:101] offset:64512\n"
    "s_waitcnt vmcnt(9) lgkmcnt(0)\n"
    "s_barrier\n"
    "v_lshrrev_b32 v6, 2, v0\n"
    "v_and_b32 v7, 3, v0\n"
    "v_lshl_add_u32 v6, v6, 3, v7\n"
    "v_lshlrev_b32 v6, 7, v6\n"
    "s_mov_b32 s46, 81793536\n"
    "buffer_load_dword v7, v6, s[20:23], s46 offen sc1\n"
    "s_mov_b32 s46, 81809920\n"
    "buffer_load_dword v8, v6, s[20:23], s46 offen sc1\n"
    "buffer_load_dword v115, off, s[20:23], s41 sc1\n"
    "ds_read_b64_tr_b16 a[136:137], v22 offset:16384\n"
    "ds_read_b64_tr_b16 a[152:153], v23 offset:16384\n"
    "ds_read_b64_tr_b16 a[138:139], v22 offset:17408\n"
    "ds_read_b64_tr_b16 a[154:155], v23 offset:17408\n"
    "ds_read_b64_tr_b16 a[140:141], v22 offset:24576\n"
    "ds_read_b64_tr_b16 a[156:157], v23 offset:24576\n"
    "ds_read_b64_tr_b16 a[142:143], v22 offset:25600\n"
    "ds_read_b64_tr_b16 a[158:159], v23 offset:25600\n"
    "ds_write_b128 v108, v[32:35] offset:12288\n"
    "ds_write_b128 v108, v[36:39] offset:13312\n"
    "ds_write_b128 v108, v[40:43] offset:14336\n"
    "ds_write_b128 v108, v[44:47] offset:15360\n"
    "s_waitcnt vmcnt(8) lgkmcnt(0)\n"
    "s_barrier\n"
    "ds_read_b64_tr_b16 v[70:71], v22 offset:32768\n"
    "ds_read_b64_tr_b16 v[78:79], v23 offset:32768\n"
    "ds_read_b64_tr_b16 v[72:73], v22 offset:33792\n"
    "ds_read_b64_tr_b16 v[80:81], v23 offset:33792\n"
    "ds_read_b64_tr_b16 v[74:75], v22 offset:40960\n"
    "ds_read_b64_tr_b16 v[82:83], v23 offset:40960\n"
    "ds_read_b64_tr_b16 v[76:77], v22 offset:41984\n"
    "ds_read_b64_tr_b16 v[84:85], v23 offset:41984\n"
    "ds_write_b128 v108, v[48:51] offset:28672\n"
    "ds_write_b128 v108, v[52:55] offset:29696\n"
    "ds_write_b128 v108, v[56:59] offset:30720\n"
    "ds_write_b128 v108, v[60:63] offset:31744\n"
    "s_waitcnt vmcnt(4) lgkmcnt(0)\n"
    "s_barrier\n"
    "ds_read_b64_tr_b16 v[86:87], v22 offset:49152\n"
    "ds_read_b64_tr_b16 v[94:95], v23 offset:49152\n"
    "ds_read_b64_tr_b16 v[88:89], v22 offset:50176\n"
    "ds_read_b64_tr_b16 v[96:97], v23 offset:50176\n"
    "ds_read_b64_tr_b16 v[90:91], v22 offset:57344\n"
    "ds_read_b64_tr_b16 v[98:99], v23 offset:57344\n"
    "ds_read_b64_tr_b16 v[92:93], v22 offset:58368\n"
    "ds_read_b64_tr_b16 v[100:101], v23 offset:58368\n"
    "ds_write_b128 v108, v[70:73] offset:45056\n"
    "ds_write_b128 v108, v[74:77] offset:46080\n"
    "ds_write_b128 v108, v[78:81] offset:47104\n"
    "ds_write_b128 v108, v[82:85] offset:48128\n"
    "s_waitcnt lgkmcnt(0)\n"
    "s_barrier\n"
    "ds_write_b128 v108, v[86:89] offset:61440\n"
    "ds_write_b128 v108, v[90:93] offset:62464\n"
    "ds_write_b128 v108, v[94:97] offset:63488\n"
    "ds_write_b128 v108, v[98:101] offset:64512\n"
    "ds_write_b128 v109, v[16:19] offset:8192\n"
    "s_waitcnt vmcnt(1)\n"
    "v_min_u32 v7, v7, v8\n"
    "v_cmp_gt_u32 vcc, 2, v7\n"
    "s_cmp_eq_u64 vcc, 0\n"
    "s_cselect_b32 s68, s68, 0\n"
    "s_waitcnt vmcnt(0)\n"
    "s_cmp_lg_u32 s68, 0\n"
    "s_cbranch_scc1 RF03_%=\n"
    "v_readfirstlane_b32 s46, v67\n"
    "s_cmp_lg_u32 s24, 0\n"
    "s_cselect_b32 s46, %2, s46\n"
    "s_mov_b32 s45, 0\n"
    "RBW3_%=:\n"
    "buffer_load_dwordx2 v[66:67], off, s[20:23], s63 sc1\n"
    "s_add_u32 s45, s45, 1\n"
    "s_cmp_gt_u32 s45, 262144\n"
    "s_cbranch_scc1 REND_%=\n"
    "s_waitcnt vmcnt(0)\n"
    "v_readfirstlane_b32 s62, v66\n"
    "v_cmp_eq_u32 vcc, s46, v67\n"
    "s_cbranch_vccnz RBW3_%=\n"
    "s_bitcmp1_b32 s62, 1\n"
    "s_cbranch_scc0 RBW3_%=\n"
    "s_xor_b32 s62, s62, s24\n"
    "s_and_b32 s62, s62, 1\n"
    "s_lshl_b32 s62, s62, 13\n"
    "v_lshrrev_b32 v4, 2, v0\n"
    "v_and_b32 v5, 3, v0\n"
    "v_lshl_add_u32 v4, v4, 3, v5\n"
    "v_lshlrev_b32 v4, 7, v4\n"
    "s_mov_b32 s45, 0\n"
    "RIW3_%=:\n"
    "s_mov_b32 s46, 81793536\n"
    "buffer_load_dword v5, v4, s[20:23], s46 offen sc1\n"
    "s_mov_b32 s46, 81809920\n"
    "buffer_load_dword v6, v4, s[20:23], s46 offen sc1\n"
    "s_add_u32 s45, s45, 1\n"
    "s_cmp_gt_u32 s45, 262144\n"
    "s_cbranch_scc1 REND_%=\n"
    "s_waitcnt vmcnt(0)\n"
    "v_min_u32 v5, v5, v6\n"
    "v_cmp_gt_u32 vcc, 2, v5\n"
    "s_cbranch_vccnz RIW3_%=\n"
    "s_add_u32 s30, s62, 0\n"
    "s_add_u32 s31, s30, 4096\n"
    "s_lshl_b32 s47, s25, 8\n"
    "s_add_u32 s36, s62, s47\n"
    "s_mov_b32 s50, 0\n"
    "s_mov_b32 s28, s30\n"
    "s_add_u32 s32, s62, s47\n"
    "s_add_u32 s32, s32, 67502080\n"
    "s_add_u32 s33, s32, 65536\n"
    "s_lshl_b32 s47, s25, 14\n"
    "s_add_u32 s34, s62, s47\n"
    "s_add_u32 s34, s34, 67108864\n"
    "s_add_u32 s35, s34, 4096\n"
    "s_add_u32 s44, s34, 4200448\n"
    "s_lshl_b32 s39, s25, 15\n"
    "s_add_u32 s39, s39, s62\n"
    "s_add_u32 s39, s39, 16797696\n"
    "s_xor_b32 s69, s62, 0x2000\n"
    "s_lshr_b32 s41, s25, 3\n"
    "s_lshl_b32 s41, s41, 14\n"
    "s_and_b32 s47, s25, 7\n"
    "s_lshl_b32 s47, s47, 10\n"
    "s_add_u32 s41, s41, s47\n"
    "s_add_u32 s41, s41, s69\n"
    "s_add_u32 s41, s41, 3146112\n"
    "s_add_u32 s69, s69, 3309568\n"
    "s_mov_b32 s37, 0x7f000000\n"
    "s_mov_b32 s42, 0\n"
    "s_cmp_eq_u32 s25, 0\n"
    "s_cselect_b32 s37, s69, s37\n"
    "s_add_u32 s69, s69, 16384\n"
    "s_cmp_eq_u32 s25, 11\n"
    "s_cselect_b32 s37, s69, s37\n"
    "s_add_u32 s69, s69, 16384\n"
    "s_cmp_eq_u32 s25, 22\n"
    "s_cselect_b32 s37, s69, s37\n"
    "s_add_u32 s69, s69, 16384\n"
    "v_mov_b32 v115, 0\n"
    "RF03_%=:\n"
    "v_cmp_gt_u32 vcc, 1, v115\n"
    "s_cbranch_vccz RF33_%=\n"
    "s_mov_b32 s45, 0\n"
    "RPK3_%=:\n"
    "buffer_load_dword v115, off, s[20:23], s41 sc1\n"
    "s_add_u32 s45, s45, 1\n"
    "s_cmp_gt_u32 s45, 0x40000\n"
    "s_cbranch_scc1 REND_%=\n"
    "s_waitcnt vmcnt(0)\n"
    "v_cmp_gt_u32 vcc, 1, v115\n"
    "s_cbranch_vccnz RPK3_%=\n"
    "RF33_%=:\n"
    "buffer_load_dwordx4 v[90:93], v107, s[20:23], s39 offen offset:0 sc1\n"
    "buffer_load_dwordx4 v[94:97], v107, s[20:23], s39 offen offset:1024 sc1\n"
    "buffer_load_dwordx4 v[98:101], v107, s[20:23], s39 offen offset:2048 sc1\n"
    "buffer_load_dwordx4 v[102:105], v107, s[20:23], s39 offen offset:3072 sc1\n"
    "s_mov_b32 s45, 0\n"
    "s_memrealtime s[86:87]\n"
    "s_waitcnt lgkmcnt(0)\n"
    "s_mul_i32 s61, s26, 21\n"
    "s_lshl_b32 s64, s25, 7\n"
    "s_add_u32 s64, s64, s62\n"
    "s_add_u32 s64, s64, 62914560\n"
    "v_mov_b32 v0, s26\n"
    "ds_write_b32 v109, v0 offset:9228\n"
    "ds_read_b128 v[32:35], v108 offset:45056\n"
    "ds_read_b128 v[36:39], v108 offset:46080\n"
    "ds_read_b128 v[40:43], v108 offset:47104\n"
    "ds_read_b128 v[44:47], v108 offset:48128\n"
    "ds_read_b128 v[48:51], v108 offset:61440\n"
    "ds_read_b128 v[52:55], v108 offset:62464\n"
    "ds_read_b128 v[56:59], v108 offset:63488\n"
    "ds_read_b128 v[60:63], v108 offset:64512\n"
    "v_mov_b64 v[66:67], 0\n"
    "v_mov_b64 v[68:69], 0\n"
    "v_mov_b64 v[70:71], 0\n"
    "v_mov_b64 v[72:73], 0\n"
    "v_mov_b64 v[74:75], 0\n"
    "v_mov_b64 v[76:77], 0\n"
    "v_mov_b64 v[78:79], 0\n"
    "v_mov_b64 v[80:81], 0\n"
    "v_mov_b64 v[82:83], 0\n"
    "v_mov_b64 v[84:85], 0\n"
    "v_mov_b64 v[86:87], 0\n"
    "v_mov_b64 v[88:89], 0\n"
    "s_waitcnt lgkmcnt(0)\n"
    ".p2align 6\n"
    "RLP3_%=:\n"
    "buffer_load_dwordx4 v[0:3], v107, s[20:23], s31 offen offset:2048 sc1\n"
    "s_memrealtime s[90:91]\n"
    "s_add_u32 s42, s42, 1\n"
    "s_add_u32 s43, s42, 1\n"
    "s_and_b32 s46, s42, 1\n"
    "s_lshl_b32 s46, s46, 9\n"
    "s_add_u32 s40, s41, s46\n"
    "s_and_b32 s46, s42, 7\n"
    "s_lshl_b32 s46, s46, 20\n"
    "s_add_u32 s38, s39, s46\n"
    "s_nop 4\n"
    "buffer_load_dwordx4 v[4:7], v107, s[20:23], s31 offen offset:3072 sc1\n"
    "s_lshr_b32 s46, s38, 8\n"
    "s_and_b32 s46, s46, 0x2000\n"
    "s_xor_b32 s38, s38, s46\n"
    "s_cmp_eq_u32 s42, 63\n"
    "s_cselect_b32 s43, 0, s43\n"
    "s_nop 8\n"
    "buffer_load_dwordx4 v[8:11], v107, s[20:23], s30 offen offset:0 sc1\n"
    "s_nop 13\n"
    "buffer_load_dwordx4 v[12:15], v107, s[20:23], s30 offen offset:1024 sc1\n"
    "s_nop 13\n"
    "buffer_load_dwordx4 v[16:19], v107, s[20:23], s30 offen offset:2048 sc1\n"
    "s_waitcnt lgkmcnt(0)\n"
    "s_nop 12\n"
    "buffer_load_dwordx4 v[20:23], v107, s[20:23], s30 offen offset:3072 sc1\n"
    "s_nop 13\n"
    "buffer_load_dwordx4 v[24:27], v107, s[20:23], s31 offen offset:0 sc1\n"
    "s_nop 13\n"
    "buffer_load_dwordx4 v[28:31], v107, s[20:23], s31 offen offset:1024 sc1\n"
    "RRB3_%=:\n"
    "s_waitcnt vmcnt(7)\n"
    "v_smfmac_f32_16x16x64_bf16 v[66:69], v[0:3], a[0:7], v106\n"
    "v_smfmac_f32_16x16x64_bf16 v[70:73], v[0:3], v[32:39], v106\n"
    "ds_read_b128 v[32:35], v107 offset:12288\n"
    "v_smfmac_f32_16x16x64_bf16 v[74:77], v[0:3], a[64:71], v106\n"
    "ds_read_b128 v[36:39], v107 offset:13312\n"
    "v_smfmac_f32_16x16x64_bf16 v[78:81], v[0:3], v[40:47], v106\n"
    "ds_read_b128 v[40:43], v107 offset:14336\n"
    "s_waitcnt vmcnt(6)\n"
    "v_smfmac_f32_16x16x64_bf16 v[66:69], v[4:7], a[8:15], v106\n"
    "ds_read_b128 v[44:47], v107 offset:15360\n"
    "v_smfmac_f32_16x16x64_bf16 v[70:73], v[4:7], v[48:55], v106\n"
    "ds_read_b128 v[48:51], v107 offset:28672\n"
    "v_max_u32 v116, v0, v2\n"
    "v_smfmac_f32_16x16x64_bf16 v[74:77], v[4:7], a[72:79], v106\n"
    "ds_read_b128 v[52:55], v107 offset:29696\n"
    "v_smfmac_f32_16x16x64_bf16 v[78:81], v[4:7], v[56:63], v106\n"
    "ds_read_b128 v[56:59], v107 offset:30720\n"
    "s_waitcnt vmcnt(5)\n"
    "v_smfmac_f32_16x16x64_bf16 v[66:69], v[8:11], a[16:23], v106\n"
    "ds_read_b128 v[60:63], v107 offset:31744\n"
    "s_waitcnt lgkmcnt(6)\n"
    "v_smfmac_f32_16x16x64_bf16 v[70:73], v[8:11], v[32:39], v106\n"
    "ds_read_b128 v[32:35], v107 offset:45056\n"
    "v_max3_u32 v116, v116, v4, v6\n"
    "v_smfmac_f32_16x16x64_bf16 v[74:77], v[8:11], a[80:87], v106\n"
    "ds_read_b128 v[36:39], v107 offset:46080\n"
    "s_waitcnt lgkmcnt(6)\n"
    "v_smfmac_f32_16x16x64_bf16 v[78:81], v[8:11], v[40:47], v106\n"
    "ds_read_b128 v[40:43], v107 offset:47104\n"
    "s_waitcnt vmcnt(4)\n"
    "v_smfmac_f32_16x16x64_bf16 v[66:69], v[12:15], a[24:31], v106\n"
    "ds_read_b128 v[44:47], v107 offset:48128\n"
    "s_waitcnt lgkmcnt(6)\n"
    "v_smfmac_f32_16x16x64_bf16 v[70:73], v[12:15], v[48:55], v106\n"
    "ds_read_b128 v[48:51], v107 offset:61440\n"
    "v_max3_u32 v116, v116, v8, v10\n"
    "v_smfmac_f32_16x16x64_bf16 v[74:77], v[12:15], a[88:95], v106\n"
    "ds_read_b128 v[52:55], v107 offset:62464\n"
    "s_waitcnt lgkmcnt(6)\n"
    "v_smfmac_f32_16x16x64_bf16 v[78:81], v[12:15], v[56:63], v106\n"
    "ds_read_b128 v[56:59], v107 offset:63488\n"
    "s_waitcnt vmcnt(3)\n"
    "v_smfmac_f32_16x16x64_bf16 v[66:69], v[16:19], a[32:39], v106\n"
    "ds_read_b128 v[60:63], v107 offset:64512\n"
    "s_waitcnt lgkmcnt(6)\n"
    "v_smfmac_f32_16x16x64_bf16 v[70:73], v[16:19], v[32:39], v106\n"
    "ds_read_b128 v[32:35], v108 offset:12288\n"
    "v_max3_u32 v116, v116, v12, v14\n"
    "v_smfmac_f32_16x16x64_bf16 v[74:77], v[16:19], a[96:103], v106\n"
    "ds_read_b128 v[36:39], v108 offset:13312\n"
    "s_waitcnt lgkmcnt(6)\n"
    "v_smfmac_f32_16x16x64_bf16 v[78:81], v[16:19], v[40:47], v106\n"
    "ds_read_b128 v[40:43], v108 offset:14336\n"
    "s_waitcnt vmcnt(2)\n"
    "v_smfmac_f32_16x16x64_bf16 v[66:69], v[20:23], a[40:47], v106\n"
    "ds_read_b128 v[44:47], v108 offset:15360\n"
    "s_waitcnt lgkmcnt(6)\n"
    "v_smfmac_f32_16x16x64_bf16 v[70:73], v[20:23], v[48:55], v106\n"
    "ds_read_b128 v[48:51], v108 offset:28672\n"
    "v_max3_u32 v116, v116, v16, v18\n"
    "v_smfmac_f32_16x16x64_bf16 v[74:77], v[20:23], a[104:111], v106\n"
    "ds_read_b128 v[52:55], v108 offset:29696\n"
    "s_waitcnt lgkmcnt(6)\n"
    "v_smfmac_f32_16x16x64_bf16 v[78:81], v[20:23], v[56:63], v106\n"
    "ds_read_b128 v[56:59], v108 offset:30720\n"
    "s_waitcnt vmcnt(1)\n"
    "v_smfmac_f32_16x16x64_bf16 v[66:69], v[24:27], a[48:55], v106\n"
    "ds_read_b128 v[60:63], v108 offset:31744\n"
    "v_smfmac_f32_16x16x64_bf16 v[70:73], v[24:27], a[128:135], v106\n"
    "v_max3_u32 v116, v116, v20, v22\n"
    "v_smfmac_f32_16x16x64_bf16 v[74:77], v[24:27], a[112:119], v106\n"
    "v_max3_u32 v116, v116, v24, v26\n"
    "v_smfmac_f32_16x16x64_bf16 v[78:81], v[24:27], a[144:151], v106\n"
    "s_waitcnt vmcnt(0)\n"
    "v_smfmac_f32_16x16x64_bf16 v[66:69], v[28:31], a[56:63], v106\n"
    "v_max3_u32 v116, v116, v28, v30\n"
    "v_cmp_eq_u32 vcc, -1, v116\n"
    "v_smfmac_f32_16x16x64_bf16 v[70:73], v[28:31], a[136:143], v106\n"
    "s_cbranch_vccnz RRT3_%=\n"
    "v_smfmac_f32_16x16x64_bf16 v[74:77], v[28:31], a[120:127], v106\n"
    "s_nop 1\n"
    "s_nop 0\n"
    "v_cvt_pk_bf16_f32 v69, v68, v69\n"
    "v_smfmac_f32_16x16x64_bf16 v[78:81], v[28:31], a[152:159], v106\n"
    "s_nop 0\n"
    "v_cvt_pk_bf16_f32 v68, v66, v67\n"
    "v_cvt_pk_bf16_f32 v70, v70, v71\n"
    "v_cvt_pk_bf16_f32 v71, v72, v73\n"
    "v_cvt_pk_bf16_f32 v77, v76, v77\n"
    "v_cvt_pk_bf16_f32 v76, v74, v75\n"
    "v_mfma_f32_16x16x32_bf16 v[68:71], v[68:71], v[120:123], 0\n"
    "s_nop 0\n"
    "v_cvt_pk_bf16_f32 v78, v78, v79\n"
    "v_cvt_pk_bf16_f32 v79, v80, v81\n"
    "s_nop 1\n"
    "v_mfma_f32_16x16x32_bf16 v[76:79], v[76:79], v[120:123], 0\n"
    "s_waitcnt lgkmcnt(6)\n"
    "v_smfmac_f32_16x16x64_bf16 v[82:85], v[0:3], v[32:39], v106\n"
    "v_cvt_pk_bf16_f32 v72, v68, v69\n"
    "v_cvt_pk_bf16_f32 v73, v70, v71\n"
    "s_waitcnt lgkmcnt(4)\n"
    "v_smfmac_f32_16x16x64_bf16 v[86:89], v[0:3], v[40:47], v106\n"
    "s_nop 1\n"
    "v_cvt_pk_bf16_f32 v74, v76, v77\n"
    "v_cvt_pk_bf16_f32 v75, v78, v79\n"
    "s_nop 1\n"
    "v_mfma_f32_16x16x32_bf16 v[66:69], v[90:93], v[72:75], 0\n"
    "v_mfma_f32_16x16x32_bf16 v[78:81], v[94:97], v[72:75], 0\n"
    "s_nop 0\n"
    "v_mfma_f32_16x16x32_bf16 v[72:75], v[72:75], v[72:75], 0\n"
    "s_nop 1\n"
    "s_waitcnt lgkmcnt(2)\n"
    "v_smfmac_f32_16x16x64_bf16 v[82:85], v[4:7], v[48:55], v106\n"
    "s_nop 1\n"
    "v_mov_b32_dpp v66, v78 row_ror:8 row_mask:0xf bank_mask:0xc\n"
    "v_mov_b32_dpp v67, v79 row_ror:8 row_mask:0xf bank_mask:0xc\n"
    "v_mov_b32_dpp v68, v80 row_ror:8 row_mask:0xf bank_mask:0xc\n"
    "v_mov_b32_dpp v69, v81 row_ror:8 row_mask:0xf bank_mask:0xc\n"
    "ds_write_b128 v109, v[66:69] offset:3072\n"
    "v_mov_b32_dpp v72, v74 row_ror:8 row_mask:0xf bank_mask:0xc\n"
    "v_mov_b32_dpp v73, v75 row_ror:8 row_mask:0xf bank_mask:0xc\n"
    "ds_write_b64 v117, v[72:73] offset:1032\n"
    "s_waitcnt lgkmcnt(2)\n"
    "v_smfmac_f32_16x16x64_bf16 v[86:89], v[4:7], v[56:63], v106\n"
    "v_smfmac_f32_16x16x64_bf16 v[82:85], v[8:11], a[160:167], v106\n"
    "v_smfmac_f32_16x16x64_bf16 v[86:89], v[8:11], a[208:215], v106\n"
    "v_smfmac_f32_16x16x64_bf16 v[82:85], v[12:15], a[168:175], v106\n"
    "s_waitcnt lgkmcnt(0)\n"
    "s_barrier\n"
    "ds_read_b128 v[32:35], v109 offset:0\n"
    "v_smfmac_f32_16x16x64_bf16 v[86:89], v[12:15], a[216:223], v106\n"
    "ds_read_b128 v[36:39], v109 offset:1024\n"
    "v_smfmac_f32_16x16x64_bf16 v[82:85], v[16:19], a[176:183], v106\n"
    "ds_read_b128 v[40:43], v109 offset:2048\n"
    "v_smfmac_f32_16x16x64_bf16 v[86:89], v[16:19], a[224:231], v106\n"
    "ds_read_b128 v[44:47], v110 offset:0\n"
    "v_smfmac_f32_16x16x64_bf16 v[82:85], v[20:23], a[184:191], v106\n"
    "ds_read_b128 v[48:51], v110 offset:1024\n"
    "v_smfmac_f32_16x16x64_bf16 v[86:89], v[20:23], a[232:239], v106\n"
    "s_waitcnt lgkmcnt(2)\n"
    "v_pk_add_f32 v[66:67], v[66:67], v[32:33]\n"
    "v_pk_add_f32 v[68:69], v[68:69], v[34:35]\n"
    "v_pk_add_f32 v[66:67], v[66:67], v[36:37]\n"
    "v_pk_add_f32 v[68:69], v[68:69], v[38:39]\n"
    "v_pk_add_f32 v[66:67], v[66:67], v[40:41]\n"
    "v_pk_add_f32 v[68:69], v[68:69], v[42:43]\n"
    "s_waitcnt lgkmcnt(0)\n"
    "v_pk_add_f32 v[70:71], v[44:45], v[46:47]\n"
    "v_pk_add_f32 v[70:71], v[70:71], v[48:49]\n"
    "v_pk_add_f32 v[70:71], v[70:71], v[50:51]\n"
    "v_max3_f32 v72, v66, v67, v68\n"
    "v_max3_f32 v72, v72, v69, v70\n"
    "v_max_f32 v72, v72, v71\n"
    "v_smfmac_f32_16x16x64_bf16 v[82:85], v[24:27], a[192:199], v106\n"
    "s_nop 0\n"
    "v_max_f32_dpp v73, v72, v72 row_ror:8 row_mask:0xf bank_mask:0xf\n"
    "v_max_f32_dpp v72, v72, v72 row_ror:8 row_mask:0xf bank_mask:0xf\n"
    "v_smfmac_f32_16x16x64_bf16 v[86:89], v[24:27], a[240:247], v106\n"
    "s_nop 0\n"
    "v_permlane16_swap_b32 v72, v73\n"
    "v_max_f32 v74, v72, v73\n"
    "v_max_f32 v72, v72, v73\n"
    "v_smfmac_f32_16x16x64_bf16 v[82:85], v[28:31], a[200:207], v106\n"
    "s_nop 0\n"
    "v_permlane32_swap_b32 v72, v74\n"
    "v_max_f32 v72, v72, v74\n"
    "v_mul_f32 v72, s48, v72\n"
    "v_fma_f32 v66, v66, s48, -v72\n"
    "v_fma_f32 v67, v67, s48, -v72\n"
    "v_fma_f32 v68, v68, s48, -v72\n"
    "v_fma_f32 v69, v69, s48, -v72\n"
    "v_exp_f32 v66, v66\n"
    "v_exp_f32 v67, v67\n"
    "v_exp_f32 v68, v68\n"
    "v_cvt_pk_bf16_f32 v62, v66, v67\n"
    "v_exp_f32 v69, v69\n"
    "v_smfmac_f32_16x16x64_bf16 v[86:89], v[28:31], a[248:255], v106\n"
    "v_mov_b32_dpp v60, v62 row_ror:8 row_mask:0xf bank_mask:0xf\n"
    "v_cvt_pk_bf16_f32 v63, v68, v69\n"
    "v_fma_f32 v70, v70, s48, -v72\n"
    "v_fma_f32 v71, v71, s48, -v72\n"
    "v_mov_b32_dpp v61, v63 row_ror:8 row_mask:0xf bank_mask:0xf\n"
    "v_exp_f32 v70, v70\n"
    "v_exp_f32 v71, v71\n"
    "v_mfma_f32_16x16x32_bf16 v[36:39], v[98:101], v[60:63], 0\n"
    "v_cvt_pk_bf16_f32 v64, v70, v71\n"
    "v_cvt_pk_bf16_f32 v82, v82, v83\n"
    "v_mfma_f32_16x16x32_bf16 v[40:43], v[102:105], v[60:63], 0\n"
    "v_cvt_pk_bf16_f32 v83, v84, v85\n"
    "v_cvt_pk_bf16_f32 v84, v88, v89\n"
    "v_mfma_f32_16x16x32_bf16 v[32:35], v[124:127], v[62:65], 0\n"
    "v_mov_b32_dpp v65, v64 row_ror:8 row_mask:0xf bank_mask:0xf\n"
    "v_cvt_pk_bf16_f32 v85, v86, v87\n"
    "s_nop 0\n"
    "v_mfma_f32_16x16x16_bf16 v[36:39], v[82:83], v[64:65], v[36:39]\n"
    "s_nop 0\n"
    "s_nop 1\n"
    "v_mfma_f32_16x16x16_bf16 v[40:43], v[84:85], v[64:65], v[40:43]\n"
    "s_nop 0\n"
    "v_add_f32_dpp v32, v32, v32 row_ror:8 row_mask:0xf bank_mask:0xf\n"
    "v_rcp_f32 v32, v32\n"
    "s_nop 0\n"
    "v_pk_mul_f32 v[36:37], v[36:37], v[32:33] op_sel_hi:[1,0]\n"
    "v_pk_mul_f32 v[38:39], v[38:39], v[32:33] op_sel_hi:[1,0]\n"
    "s_mov_b64 exec, s[98:99]\n"
    "s_nop 0\n"
    "v_pk_mul_f32 v[36:37], v[40:41], v[32:33] op_sel_hi:[1,0]\n"
    "v_pk_mul_f32 v[38:39], v[42:43], v[32:33] op_sel_hi:[1,0]\n"
    "s_mov_b64 exec, -1\n"
    "v_cvt_pk_bf16_f32 v0, v36, v37\n"
    "v_cvt_pk_bf16_f32 v1, v38, v39\n"
    "ds_write_b64 v111, v[0:1] offset:1536\n"
    "v_mov_b32_dpp v2, v0 row_ror:8 row_mask:0xf bank_mask:0xf\n"
    "v_mov_b32_dpp v3, v1 row_ror:8 row_mask:0xf bank_mask:0xf\n"
    "v_mov_b64 v[16:17], 0\n"
    "v_mov_b64 v[18:19], 0\n"
    "v_mov_b64 v[20:21], 0\n"
    "v_mov_b64 v[22:23], 0\n"
    "v_mov_b64 v[24:25], 0\n"
    "v_mov_b64 v[26:27], 0\n"
    "v_mov_b64 v[28:29], 0\n"
    "v_mov_b64 v[30:31], 0\n"
    "v_smfmac_f32_16x16x64_bf16 v[16:19], v[0:3], v[128:135], v106\n"
    "s_waitcnt lgkmcnt(0)\n"
    "s_barrier\n"
    "ds_read_b128 v[4:7], v112 offset:0\n"
    "ds_read_b128 v[8:11], v112 offset:512\n"
    "ds_read_b128 v[12:15], v112 offset:1024\n"
    "v_smfmac_f32_16x16x64_bf16 v[20:23], v[0:3], v[160:167], v106\n"
    "v_smfmac_f32_16x16x64_bf16 v[24:27], v[0:3], v[192:199], v106\n"
    "v_smfmac_f32_16x16x64_bf16 v[28:31], v[0:3], v[224:231], v106\n"
    "s_waitcnt lgkmcnt(2)\n"
    "v_smfmac_f32_16x16x64_bf16 v[16:19], v[4:7], v[136:143], v106\n"
    "v_smfmac_f32_16x16x64_bf16 v[20:23], v[4:7], v[168:175], v106\n"
    "s_waitcnt lgkmcnt(1)\n"
    "v_smfmac_f32_16x16x64_bf16 v[16:19], v[8:11], v[144:151], v106\n"
    "v_smfmac_f32_16x16x64_bf16 v[20:23], v[8:11], v[176:183], v106\n"
    "s_waitcnt lgkmcnt(0)\n"
    "v_smfmac_f32_16x16x64_bf16 v[16:19], v[12:15], v[152:159], v106\n"
    "s_nop 1\n"
    "v_smfmac_f32_16x16x64_bf16 v[20:23], v[12:15], v[184:191], v106\n"
    "s_nop 0\n"
    "buffer_load_dword v115, off, s[20:23], s40 sc1\n"
    "v_smfmac_f32_16x16x64_bf16 v[24:27], v[4:7], v[200:207], v106\n"
    "s_nop 1\n"
    "v_cvt_pk_bf16_f32 v19, v18, v19\n"
    "v_smfmac_f32_16x16x64_bf16 v[24:27], v[8:11], v[208:215], v106\n"
    "v_cvt_pk_bf16_f32 v18, v16, v17\n"
    "v_smfmac_f32_16x16x64_bf16 v[24:27], v[12:15], v[216:223], v106\n"
    "v_or_b32 v18, 1, v18\n"
    "v_smfmac_f32_16x16x64_bf16 v[28:31], v[4:7], v[232:239], v106\n"
    "v_cvt_pk_bf16_f32 v20, v20, v21\n"
    "v_smfmac_f32_16x16x64_bf16 v[28:31], v[8:11], v[240:247], v106\n"
    "v_cvt_pk_bf16_f32 v21, v22, v23\n"
    "v_or_b32 v20, 1, v20\n"
    "buffer_store_dwordx4 v[18:21], v113, s[20:23], s32 offen sc1\n"
    "v_smfmac_f32_16x16x64_bf16 v[28:31], v[12:15], v[248:255], v106\n"
    "s_nop 1\n"
    "v_cvt_pk_bf16_f32 v27, v26, v27\n"
    "v_cvt_pk_bf16_f32 v26, v24, v25\n"
    "v_or_b32 v26, 1, v26\n"
    "s_nop 2\n"
    "v_cvt_pk_bf16_f32 v28, v28, v29\n"
    "v_cvt_pk_bf16_f32 v29, v30, v31\n"
    "v_or_b32 v28, 1, v28\n"
    "buffer_store_dwordx4 v[26:29], v113, s[20:23], s33 offen sc1\n"
    "s_mov_b32 s45, 0\n"
    "s_add_u32 s36, s36, 16384\n"
    "s_bitcmp1_b32 s32, 19\n"
    "s_cselect_b32 s46, 4194304, 0\n"
    "s_bitset1_b32 s46, 19\n"
    "s_xor_b32 s32, s32, s46\n"
    "s_add_u32 s33, s32, 65536\n"
    "ds_read_b128 v[32:35], v108 offset:45056\n"
    "ds_read_b128 v[36:39], v108 offset:46080\n"
    "ds_read_b128 v[40:43], v108 offset:47104\n"
    "ds_read_b128 v[44:47], v108 offset:48128\n"
    "ds_read_b128 v[48:51], v108 offset:61440\n"
    "ds_read_b128 v[52:55], v108 offset:62464\n"
    "ds_read_b128 v[56:59], v108 offset:63488\n"
    "ds_read_b128 v[60:63], v108 offset:64512\n"
    "v_mov_b64 v[66:67], 0\n"
    "v_mov_b64 v[68:69], 0\n"
    "v_mov_b64 v[70:71], 0\n"
    "v_mov_b64 v[72:73], 0\n"
    "v_mov_b64 v[74:75], 0\n"
    "v_mov_b64 v[76:77], 0\n"
    "v_mov_b64 v[78:79], 0\n"
    "v_mov_b64 v[80:81], 0\n"
    "v_mov_b64 v[82:83], 0\n"
    "v_mov_b64 v[84:85], 0\n"
    "v_mov_b64 v[86:87], 0\n"
    "v_mov_b64 v[88:89], 0\n"
    "s_waitcnt vmcnt(2)\n"
    "v_cmp_gt_u32 vcc, s43, v115\n"
    "s_cbranch_vccnz RKW3_%=\n"
    "RKB3_%=:\n"
    "buffer_load_dwordx4 v[90:93], v107, s[20:23], s38 offen offset:0 sc1\n"
    "buffer_load_dwordx4 v[94:97], v107, s[20:23], s38 offen offset:1024 sc1\n"
    "buffer_load_dwordx4 v[98:101], v107, s[20:23], s38 offen offset:2048 sc1\n"
    "buffer_load_dwordx4 v[102:105], v107, s[20:23], s38 offen offset:3072 sc1\n"
    "v_max_u32_dpp v118, v118, v118 row_ror:1 row_mask:0xf bank_mask:0xf\n"
    "s_nop 1\n"
    "v_max_u32_dpp v118, v118, v118 row_ror:2 row_mask:0xf bank_mask:0xf\n"
    "s_nop 1\n"
    "v_max_u32_dpp v118, v118, v118 row_ror:4 row_mask:0xf bank_mask:0xf\n"
    "s_nop 1\n"
    "v_max_u32_dpp v118, v118, v118 row_ror:8 row_mask:0xf bank_mask:0xf\n"
    "s_nop 0\n"
    "v_readlane_b32 s46, v118, 0\n"
    "v_readlane_b32 s65, v118, 16\n"
    "s_max_u32 s65, s65, s46\n"
    "s_cmp_gt_u32 s65, 0x10000\n"
    "s_cselect_b32 s46, 42, 0\n"
    "s_add_u32 s61, s61, s46\n"
    "s_min_u32 s61, s61, 4515\n"
    "s_sub_u32 s46, s65, 3360\n"
    "s_cmp_lt_u32 s46, 2100\n"
    "s_cselect_b32 s65, s65, s61\n"
    "s_sub_u32 s65, s65, s61\n"
    "s_min_i32 s65, s65, 128\n"
    "s_max_i32 s65, s65, -128\n"
    "s_cmp_lt_i32 s65, 0\n"
    "s_cselect_b32 s46, 2, 2\n"
    "s_cmp_lt_u32 s42, 11\n"
    "s_cselect_b32 s46, 1, s46\n"
    "s_ashr_i32 s65, s65, s46\n"
    "s_add_u32 s61, s61, s65\n"
    "s_mul_hi_u32 s60, s61, 204522253\n"
    "v_mov_b32 v118, s60\n"
    "ds_write_b32 v109, v118 offset:9228\n"
    "s_andn2_b32 s67, s64, 0xfff\n"
    "s_add_u32 s64, s64, 16384\n"
    "s_cmp_gt_u32 s42, 3\n"
    "s_cselect_b32 s67, s67, 0x7f000000\n"
    "v_lshlrev_b32 v116, 3, v107\n"
    "v_lshrrev_b32 v1, 2, v107\n"
    "s_waitcnt lgkmcnt(0)\n"
    "s_mov_b32 s45, 0\n"
    "s_add_u32 s37, s37, 128\n"
    "s_barrier\n"
    "ds_read_b64 v[118:119], v109 offset:9216\n"
    "ds_read_b32 v0, v109 offset:9224\n"
    "s_waitcnt lgkmcnt(1)\n"
    "v_readfirstlane_b32 s30, v119\n"
    "s_mov_b32 exec_hi, 0\n"
    "buffer_load_dword v118, v116, s[20:23], s67 offen sc1\n"
    "s_waitcnt lgkmcnt(0)\n"
    "buffer_store_dword v0, v1, s[20:23], s64 offen sc1\n"
    "s_mov_b32 exec_hi, -1\n"
    "s_add_u32 s31, s30, 4096\n"
    "s_cmp_lt_u32 s42, 63\n"
    "s_barrier\n"
    "s_cbranch_scc1 RLP3_%=\n"
    "s_branch REND_%=\n"
    "RRT3_%=:\n"
    "s_add_u32 s85, s90, 52\n"
    "s_add_u32 s45, s45, 1\n"
    "s_cmp_gt_u32 s45, 0x40000\n"
    "s_cbranch_scc1 REND_%=\n"
    "s_mov_b32 s50, 56\n"
    "s_or_b32 s86, s30, 1048576\n"
    "s_or_b32 s87, s31, 1048576\n"
    "RT3_%=:\n"
    "s_memrealtime s[90:91]\n"
    "s_waitcnt lgkmcnt(0)\n"
    "s_sub_u32 s46, s90, s85\n"
    "s_cmp_lt_i32 s46, 0\n"
    "s_cbranch_scc1 RT3_%=\n"
    "buffer_load_dwordx4 v[0:3], v107, s[20:23], s87 offen offset:2048 sc1\n"
    "ds_read_b128 v[32:35], v108 offset:45056\n"
    "ds_read_b128 v[36:39], v108 offset:46080\n"
    "ds_read_b128 v[40:43], v108 offset:47104\n"
    "ds_read_b128 v[44:47], v108 offset:48128\n"
    "s_nop 7\n"
    "buffer_load_dwordx4 v[4:7], v107, s[20:23], s87 offen offset:3072 sc1\n"
    "ds_read_b128 v[48:51], v108 offset:61440\n"
    "ds_read_b128 v[52:55], v108 offset:62464\n"
    "ds_read_b128 v[56:59], v108 offset:63488\n"
    "ds_read_b128 v[60:63], v108 offset:64512\n"
    "s_nop 7\n"
    "buffer_load_dwordx4 v[8:11], v107, s[20:23], s86 offen offset:0 sc1\n"
    "v_mov_b64 v[66:67], 0\n"
    "v_mov_b64 v[68:69], 0\n"
    "v_mov_b64 v[70:71], 0\n"
    "v_mov_b64 v[72:73], 0\n"
    "s_nop 7\n"
    "buffer_load_dwordx4 v[12:15], v107, s[20:23], s86 offen offset:1024 sc1\n"
    "v_mov_b64 v[74:75], 0\n"
    "v_mov_b64 v[76:77], 0\n"
    "v_mov_b64 v[78:79], 0\n"
    "v_mov_b64 v[80:81], 0\n"
    "s_nop 7\n"
    "buffer_load_dwordx4 v[16:19], v107, s[20:23], s86 offen offset:2048 sc1\n"
    "s_nop 13\n"
    "buffer_load_dwordx4 v[20:23], v107, s[20:23], s86 offen offset:3072 sc1\n"
    "s_nop 13\n"
    "buffer_load_dwordx4 v[24:27], v107, s[20:23], s87 offen offset:0 sc1\n"
    "s_nop 13\n"
    "buffer_load_dwordx4 v[28:31], v107, s[20:23], s87 offen offset:1024 sc1\n"
    "s_waitcnt lgkmcnt(0)\n"
    "s_branch RRB3_%=\n"
    "RKW3_%=:\n"
    "s_mov_b32 s45, 0\n"
    "RK23_%=:\n"
    "buffer_load_dword v115, off, s[20:23], s40 sc1\n"
    "s_add_u32 s45, s45, 1\n"
    "s_cmp_gt_u32 s45, 0x40000\n"
    "s_cbranch_scc1 REND_%=\n"
    "s_waitcnt vmcnt(0)\n"
    "v_cmp_gt_u32 vcc, s43, v115\n"
    "s_cbranch_vccnz RK23_%=\n"
    "s_branch RKB3_%=\n"
    "REND_%=:\n"
    ".Lcode_end:\n"
    :: "s"(__builtin_amdgcn_kernarg_segment_ptr()), "s"(RFL(ids)), "s"(RFL(h0)) : "v0", "v1", "v2", "v3", "v4", "v5", "v6", "v7", "v8", "v9", "v10", "v11", "v12", "v13", "v14", "v15", "v16", "v17", "v18", "v19", "v20", "v21", "v22", "v23", "v24", "v25", "v26", "v27", "v28", "v29", "v30", "v31", "v32", "v33", "v34", "v35", "v36", "v37", "v38", "v39", "v40", "v41", "v42", "v43", "v44", "v45", "v46", "v47", "v48", "v49", "v50", "v51", "v52", "v53", "v54", "v55", "v56", "v57", "v58", "v59", "v60", "v61", "v62", "v63", "v64", "v65", "v66", "v67", "v68", "v69", "v70", "v71", "v72", "v73", "v74", "v75", "v76", "v77", "v78", "v79", "v80", "v81", "v82", "v83", "v84", "v85", "v86", "v87", "v88", "v89", "v90", "v91", "v92", "v93", "v94", "v95", "v96", "v97", "v98", "v99", "v100", "v101", "v102", "v103", "v104", "v105", "v106", "v107", "v108", "v109", "v110", "v111", "v112", "v113", "v114", "v115", "v116", "v117", "v118", "v119", "v120", "v121", "v122", "v123", "v124", "v125", "v126", "v127", "v128", "v129", "v130", "v131", "v132", "v133", "v134", "v135", "v136", "v137", "v138", "v139", "v140", "v141", "v142", "v143", "v144", "v145", "v146", "v147", "v148", "v149", "v150", "v151", "v152", "v153", "v154", "v155", "v156", "v157", "v158", "v159", "v160", "v161", "v162", "v163", "v164", "v165", "v166", "v167", "v168", "v169", "v170", "v171", "v172", "v173", "v174", "v175", "v176", "v177", "v178", "v179", "v180", "v181", "v182", "v183", "v184", "v185", "v186", "v187", "v188", "v189", "v190", "v191", "v192", "v193", "v194", "v195", "v196", "v197", "v198", "v199", "v200", "v201", "v202", "v203", "v204", "v205", "v206", "v207", "v208", "v209", "v210", "v211", "v212", "v213", "v214", "v215", "v216", "v217", "v218", "v219", "v220", "v221", "v222", "v223", "v224", "v225", "v226", "v227", "v228", "v229", "v230", "v231", "v232", "v233", "v234", "v235", "v236", "v237", "v238", "v239", "v240", "v241", "v242", "v243", "v244", "v245", "v246", "v247", "v248", "v249", "v250", "v251", "v252", "v253", "v254", "v255", "a0", "a1", "a2", "a3", "a4", "a5", "a6", "a7", "a8", "a9", "a10", "a11", "a12", "a13", "a14", "a15", "a16", "a17", "a18", "a19", "a20", "a21", "a22", "a23", "a24", "a25", "a26", "a27", "a28", "a29", "a30", "a31", "a32", "a33", "a34", "a35", "a36", "a37", "a38", "a39", "a40", "a41", "a42", "a43", "a44", "a45", "a46", "a47", "a48", "a49", "a50", "a51", "a52", "a53", "a54", "a55", "a56", "a57", "a58", "a59", "a60", "a61", "a62", "a63", "a64", "a65", "a66", "a67", "a68", "a69", "a70", "a71", "a72", "a73", "a74", "a75", "a76", "a77", "a78", "a79", "a80", "a81", "a82", "a83", "a84", "a85", "a86", "a87", "a88", "a89", "a90", "a91", "a92", "a93", "a94", "a95", "a96", "a97", "a98", "a99", "a100", "a101", "a102", "a103", "a104", "a105", "a106", "a107", "a108", "a109", "a110", "a111", "a112", "a113", "a114", "a115", "a116", "a117", "a118", "a119", "a120", "a121", "a122", "a123", "a124", "a125", "a126", "a127", "a128", "a129", "a130", "a131", "a132", "a133", "a134", "a135", "a136", "a137", "a138", "a139", "a140", "a141", "a142", "a143", "a144", "a145", "a146", "a147", "a148", "a149", "a150", "a151", "a152", "a153", "a154", "a155", "a156", "a157", "a158", "a159", "a160", "a161", "a162", "a163", "a164", "a165", "a166", "a167", "a168", "a169", "a170", "a171", "a172", "a173", "a174", "a175", "a176", "a177", "a178", "a179", "a180", "a181", "a182", "a183", "a184", "a185", "a186", "a187", "a188", "a189", "a190", "a191", "a192", "a193", "a194", "a195", "a196", "a197", "a198", "a199", "a200", "a201", "a202", "a203", "a204", "a205", "a206", "a207", "a208", "a209", "a210", "a211", "a212", "a213", "a214", "a215", "a216", "a217", "a218", "a219", "a220", "a221", "a222", "a223", "a224", "a225", "a226", "a227", "a228", "a229", "a230", "a231", "a232", "a233", "a234", "a235", "a236", "a237", "a238", "a239", "a240", "a241", "a242", "a243", "a244", "a245", "a246", "a247", "a248", "a249", "a250", "a251", "a252", "a253", "a254", "a255", "s4", "s5", "s6", "s7", "s8", "s9", "s10", "s11", "s12", "s13", "s14", "s15", "s16", "s17", "s18", "s19", "s20", "s21", "s22", "s23", "s24", "s25", "s26", "s27", "s28", "s29", "s30", "s31", "s32", "s33", "s34", "s35", "s36", "s37", "s38", "s39", "s40", "s41", "s42", "s43", "s44", "s45", "s46", "s47", "s48", "s49", "s50", "s51", "s52", "s53", "s54", "s55", "s56", "s57", "s58", "s59", "s60", "s61", "s62", "s63", "s64", "s65", "s66", "s67", "s68", "s69", "s70", "s71", "s72", "s73", "s74", "s75", "s76", "s77", "s78", "s79", "s80", "s81", "s82", "s83", "s84", "s85", "s86", "s87", "s88", "s89", "s90", "s91", "s92", "s93", "s94", "s95", "s96", "s97", "s98", "s99", "vcc", "scc", "m0", "memory");
}
}

extern "C" void kernel_launch(void* const* d_in, const int* in_sizes, int n_in, void* d_out, int out_size, void* d_ws, size_t ws_size, hipStream_t stream) {
  Args A;
  A.q = (const u16*)d_in[0]; A.k = (const u16*)d_in[1]; A.v = (const u16*)d_in[2]; A.sk = (const u16*)d_in[3]; A.ssm = (const u16*)d_in[4]; A.sD = (const u16*)d_in[5];
  A.mem0 = (const u16*)d_in[7]; A.Wq = (const u16*)d_in[8]; A.Wk = (const u16*)d_in[9]; A.Wv = (const u16*)d_in[10]; A.Wo = (const u16*)d_in[11];
  A.ws = (unsigned char*)(((uintptr_t)d_ws + (2 * MB - 1)) & ~(uintptr_t)(2 * MB - 1)); A.out = (u16*)d_out;
  A.d1 = 201 | 240 << 16; A.d2 = 48 | 378 << 16; A.d4 = 54; A.d3 = 92;
  hipFuncSetAttribute((const void*)k_main, hipFuncAttributeMaxDynamicSharedMemorySize, LDS_BYTES);
  k_main<<<256, 256, LDS_BYTES, stream>>>(A);
}
```
